```python
import torch

def _softmax(x, axis=-1):
    return torch.softmax(x, dim=axis)
def _gelu(x):
    return torch.nn.functional.gelu(x, approximate="tanh")
def _silu(x):
    return torch.nn.functional.silu(x)
def _sigmoid(x):
    return torch.sigmoid(x)
def _layernorm(x, axis=-1, eps=1e-5):
    m = x.mean(dim=axis, keepdim=True)
    v = x.var(dim=axis, keepdim=True, unbiased=False)
    return (x - m) / torch.sqrt(v + eps)
def _scatter_add(tgt, idx, src, axis=0):
    return tgt.clone().index_add_(axis, idx, src)
def _segment_sum(src, seg, n):
    out = torch.zeros((n,) + src.shape[1:], dtype=src.dtype)
    return out.index_add_(0, seg, src)
def _pad(x, pw):
    flat = tuple(v for pair in reversed(pw) for v in pair)
    return torch.nn.functional.pad(x, flat)

kernel_name = 'standalone__deep_cross_wide_deep__recsys'

def setup_inputs():

    _w = (lambda a: (lambda t: t.to(torch.bfloat16) if t.is_floating_point() else t)(torch.as_tensor(a)))
    B, F, D = 16384, 26, 128
    V, Nd = min(1000000, 100000), 13
    Din = F * D
    H = 4 * D
    return {
        "idx": _w((lambda lo, hi, *s: torch.randint(lo, hi, s))(0, V, B, F).to(torch.int64)),
        "table": _w((lambda *s: torch.randn(*s, dtype=torch.bfloat16) * 1.6)(V, D) * 0.05),
        "x_wide": _w((lambda *s: torch.randn(*s, dtype=torch.bfloat16) * 1.6)(B, Nd)),
        "w_wide": _w((lambda *s: torch.randn(*s, dtype=torch.bfloat16) * 1.6)(Nd) * 0.05),
        "b_wide": _w((lambda *s: torch.randn(*s, dtype=torch.bfloat16) * 1.6)(1) * 0.01),
        "W1": _w((lambda *s: torch.randn(*s, dtype=torch.bfloat16) * 1.6)(Din, H) * (2.0 / Din) ** 0.5),
        "b1": _w((lambda *s: torch.randn(*s, dtype=torch.bfloat16) * 1.6)(H) * 0.01),
        "W2": _w((lambda *s: torch.randn(*s, dtype=torch.bfloat16) * 1.6)(H) * (1.0 / H) ** 0.5),
        "b2": _w((lambda *s: torch.randn(*s, dtype=torch.bfloat16) * 1.6)(1) * 0.01),
    }

def reference(idx, table, x_wide, w_wide, b_wide, W1, b1, W2, b2):
    emb = table[idx]
    deep_in = emb.reshape(emb.shape[0], -1)
    h = deep_in @ W1 + b1
    h = h * (lambda x, dt: x.to(dt))(h > 0, h.dtype)
    deep_logit = h @ W2 + b2
    wide_logit = x_wide @ w_wide + b_wide
    return _sigmoid(wide_logit + deep_logit)
```

```cpp
#include <hip/hip_runtime.h>
#include <hip/hip_bf16.h>
#include <stdint.h>

typedef unsigned short u16;
__device__ __forceinline__ float bf2f(u16 v){ return __uint_as_float(((unsigned)v)<<16); }

__global__ __launch_bounds__(256) void naive_kernel(const int* __restrict__ idx, const u16* __restrict__ table, const u16* __restrict__ xw,
   const u16* __restrict__ ww, const u16* __restrict__ bw, const u16* __restrict__ W1, const u16* __restrict__ b1,
   const u16* __restrict__ W2, const u16* __restrict__ b2, __hip_bfloat16* __restrict__ out) {
  __shared__ float xs[8][3328];
  __shared__ float red[8][256];
  int row0 = blockIdx.x * 8;
  int tid = threadIdx.x;
  for (int e = tid; e < 8 * 3328; e += 256) {
    int r = e / 3328, k = e % 3328;
    int f = k >> 7, d = k & 127;
    int id = idx[(row0 + r) * 26 + f];
    xs[r][k] = bf2f(table[(size_t)id * 128 + d]);
  }
  __syncthreads();
  float acc[8][2];
  for (int r = 0; r < 8; ++r) { acc[r][0] = 0.f; acc[r][1] = 0.f; }
  for (int k = 0; k < 3328; ++k) {
    unsigned w = *(const unsigned*)(W1 + (size_t)k * 512 + tid * 2);
    float w0 = __uint_as_float(w << 16), w1 = __uint_as_float(w & 0xffff0000u);
#pragma unroll
    for (int r = 0; r < 8; ++r) { float x = xs[r][k]; acc[r][0] += x * w0; acc[r][1] += x * w1; }
  }
  float bb0 = bf2f(b1[tid * 2]), bb1 = bf2f(b1[tid * 2 + 1]);
  float v0 = bf2f(W2[tid * 2]), v1 = bf2f(W2[tid * 2 + 1]);
  for (int r = 0; r < 8; ++r) {
    float h0 = fmaxf(acc[r][0] + bb0, 0.f), h1 = fmaxf(acc[r][1] + bb1, 0.f);
    red[r][tid] = h0 * v0 + h1 * v1;
  }
  __syncthreads();
  if (tid < 8) {
    float s = 0.f;
    for (int i = 0; i < 256; ++i) s += red[tid][i];
    float wd = 0.f;
    for (int i = 0; i < 13; ++i) wd += bf2f(xw[(row0 + tid) * 13 + i]) * bf2f(ww[i]);
    float z = s + bf2f(b2[0]) + wd + bf2f(bw[0]);
    out[row0 + tid] = __float2bfloat16(1.f / (1.f + __expf(-z)));
  }
}

extern "C" void kernel_launch(void* const* d_in, const int* in_sizes, int n_in,
                              void* d_out, int out_size, void* d_ws, size_t ws_size,
                              hipStream_t stream) {
  naive_kernel<<<16384 / 8, 256, 0, stream>>>((const int*)d_in[0], (const u16*)d_in[1], (const u16*)d_in[2], (const u16*)d_in[3],
     (const u16*)d_in[4], (const u16*)d_in[5], (const u16*)d_in[6], (const u16*)d_in[7], (const u16*)d_in[8], (__hip_bfloat16*)d_out);
}
```

```cpp
#include <hip/hip_runtime.h>
#include <hip/hip_bf16.h>
#include <stdint.h>

typedef unsigned short u16;
typedef unsigned int u32;
typedef u32 u32x4 __attribute__((ext_vector_type(4)));
typedef unsigned long long u64;
typedef __attribute__((address_space(1))) u64 gu64;
typedef __attribute__((address_space(1))) u32 gu32;
#define RLX_AGENT __ATOMIC_RELAXED, __HIP_MEMORY_SCOPE_AGENT

constexpr int NL = 2, NW = 4 + NL + 1, GPL = 16 / NL, NXU = 4, NWS = 6, LEADT = 2;
constexpr int BB = 16384, FF = 26, DD = 128, KK = FF * DD, HH = 512, ND = 13;
constexpr int WRING_OFF = NXU * 16384, LDS_BYTES = WRING_OFF + NWS * 16384;
constexpr size_t WS_SLOT_OFF = 0;
constexpr size_t WS_CTL_OFF = (size_t)BB * 8;
constexpr u32 SPIN_CAP = 1u << 20;

__device__ __forceinline__ float bf2f(u16 v) { return __uint_as_float(((u32)v) << 16); }
__device__ __forceinline__ u32x4 make_srd(const void* p, u32 bytes) {
  const u64 a = (u64)p;
  u32x4 r;
  r[0] = __builtin_amdgcn_readfirstlane((u32)a);
  r[1] = __builtin_amdgcn_readfirstlane((u32)(a >> 32) & 0xffffu);
  r[2] = __builtin_amdgcn_readfirstlane(bytes);
  r[3] = 0x00020000u;
  return r;
}
#define BAR() do { asm volatile("" ::: "memory"); __builtin_amdgcn_s_barrier(); asm volatile("" ::: "memory"); } while (0)

__global__ __launch_bounds__(NW * 64, 2) void dcwd_main(const int* __restrict__ idx, const u16* __restrict__ table,
    const u16* __restrict__ xw, const u16* __restrict__ ww, const u16* __restrict__ bw,
    const u16* __restrict__ W1, const u16* __restrict__ b1, const u16* __restrict__ W2, const u16* __restrict__ b2,
    char* __restrict__ ws, __hip_bfloat16* __restrict__ out, int V) {
  extern __shared__ __attribute__((aligned(16))) char smem[];
  const u32 tid = threadIdx.x, lane = tid & 63u;
  const u32 w = __builtin_amdgcn_readfirstlane(tid >> 6);
  const u32 b = blockIdx.x, xcd = b & 7u, jj = b >> 3, mt = xcd * 16u + (jj >> 1), h = jj & 1u, row0 = mt * 128u;
  const u32 l15 = lane & 15u, g = lane >> 4;
  float* red = (float*)smem;
  if (w < 4u) {
    const u32x4 srdW = make_srd(W1, KK * HH * 2);
    const u32 xa0 = l15 * 128u + ((g ^ (l15 >> 1)) << 4), xa1 = l15 * 128u + (((4u + g) ^ (l15 >> 1)) << 4);
    const u32 sWb = WRING_OFF + w * (NWS * 4096u);
    const u32 q4 = (lane >> 2) & 3u, pp = lane & 3u, fl = ((q4 >> 1) & 1u) | ((g & 1u) << 1);
    const u32 wab = sWb + (8u * g + q4) * 128u + 8u * pp;
    const u32 wa0 = wab + ((0u ^ fl) << 5), wa1 = wab + ((1u ^ fl) << 5), wa2 = wab + ((2u ^ fl) << 5), wa3 = wab + ((3u ^ fl) << 5);
    const u32 r8 = lane >> 3, c8 = lane & 7u, f0 = (r8 >> 1) & 1u;
    const u32 wvb = r8 * 1024u + (h * 256u + w * 64u) * 2u + (c8 & 1u) * 16u;
    const u32 wve = wvb + (((c8 >> 1) ^ f0) << 5), wvo = wvb + (((c8 >> 1) ^ (f0 | 2u)) << 5);
    const u32 wv0 = wve, wv1 = wvo + 7168u, wv2 = wve + 2u * 7168u, wv3 = wvo + 3u * 7168u;
    const u32 vb = (h * 256u + w * 64u + g * 4u) * 2u;
    u32 sW = h ? 2u * 32768u : 0u, sC = 0;
    float p0, p1, p2, p3, p4, p5, p6, p7;
    asm volatile(
      "s_nop 4\n"
      "s_add_u32 m0, %[sWb], 0\n"
      "s_nop 0\n"
      "buffer_load_dwordx4 %[wv0], %[srdW], %[sW] offen offset:0 lds\n"
      "buffer_load_dwordx4 %[wv1], %[srdW], %[sW] offen offset:1024 lds\n"
      "buffer_load_dwordx4 %[wv2], %[srdW], %[sW] offen offset:2048 lds\n"
      "buffer_load_dwordx4 %[wv3], %[srdW], %[sW] offen offset:3072 lds\n"
      "global_load_dwordx2 v[24:25], %[vb], %[pb1] offset:0\n"
      "global_load_dwordx2 v[26:27], %[vb], %[pb1] offset:32\n"
      "global_load_dwordx2 v[28:29], %[vb], %[pb1] offset:64\n"
      "global_load_dwordx2 v[30:31], %[vb], %[pb1] offset:96\n"
      "s_add_u32 m0, %[sWb], 4096\n"
      "s_add_u32 %[sW], %[sW], 0x8000\n"
      "buffer_load_dwordx4 %[wv0], %[srdW], %[sW] offen offset:0 lds\n"
      "buffer_load_dwordx4 %[wv1], %[srdW], %[sW] offen offset:1024 lds\n"
      "buffer_load_dwordx4 %[wv2], %[srdW], %[sW] offen offset:2048 lds\n"
      "buffer_load_dwordx4 %[wv3], %[srdW], %[sW] offen offset:3072 lds\n"
      "s_add_u32 m0, %[sWb], 8192\n"
      "s_add_u32 %[sW], %[sW], %[d0]\n"
      "buffer_load_dwordx4 %[wv0], %[srdW], %[sW] offen offset:0 lds\n"
      "buffer_load_dwordx4 %[wv1], %[srdW], %[sW] offen offset:1024 lds\n"
      "buffer_load_dwordx4 %[wv2], %[srdW], %[sW] offen offset:2048 lds\n"
      "buffer_load_dwordx4 %[wv3], %[srdW], %[sW] offen offset:3072 lds\n"
      "s_add_u32 m0, %[sWb], 12288\n"
      "s_add_u32 %[sW], %[sW], 0x8000\n"
      "buffer_load_dwordx4 %[wv0], %[srdW], %[sW] offen offset:0 lds\n"
      "buffer_load_dwordx4 %[wv1], %[srdW], %[sW] offen offset:1024 lds\n"
      "buffer_load_dwordx4 %[wv2], %[srdW], %[sW] offen offset:2048 lds\n"
      "buffer_load_dwordx4 %[wv3], %[srdW], %[sW] offen offset:3072 lds\n"
      "s_add_u32 m0, %[sWb], 16384\n"
      "s_add_u32 %[sW], %[sW], %[d1]\n"
      "buffer_load_dwordx4 %[wv0], %[srdW], %[sW] offen offset:0 lds\n"
      "buffer_load_dwordx4 %[wv1], %[srdW], %[sW] offen offset:1024 lds\n"
      "buffer_load_dwordx4 %[wv2], %[srdW], %[sW] offen offset:2048 lds\n"
      "buffer_load_dwordx4 %[wv3], %[srdW], %[sW] offen offset:3072 lds\n"
      "s_add_u32 m0, %[sWb], 20480\n"
      "s_add_u32 %[sW], %[sW], 0x8000\n"
      "buffer_load_dwordx4 %[wv0], %[srdW], %[sW] offen offset:0 lds\n"
      "buffer_load_dwordx4 %[wv1], %[srdW], %[sW] offen offset:1024 lds\n"
      "buffer_load_dwordx4 %[wv2], %[srdW], %[sW] offen offset:2048 lds\n"
      "buffer_load_dwordx4 %[wv3], %[srdW], %[sW] offen offset:3072 lds\n"
      "s_waitcnt vmcnt(20)\n"
      "v_lshlrev_b32 v128, 16, v24\n"
      "v_and_b32 v129, 0xffff0000, v24\n"
      "v_lshlrev_b32 v130, 16, v25\n"
      "v_and_b32 v131, 0xffff0000, v25\n"
      "v_mov_b32 v132, v128\n"
      "v_mov_b32 v133, v129\n"
      "v_mov_b32 v134, v130\n"
      "v_mov_b32 v135, v131\n"
      "v_mov_b32 v136, v128\n"
      "v_mov_b32 v137, v129\n"
      "v_mov_b32 v138, v130\n"
      "v_mov_b32 v139, v131\n"
      "v_mov_b32 v140, v128\n"
      "v_mov_b32 v141, v129\n"
      "v_mov_b32 v142, v130\n"
      "v_mov_b32 v143, v131\n"
      "v_mov_b32 v144, v128\n"
      "v_mov_b32 v145, v129\n"
      "v_mov_b32 v146, v130\n"
      "v_mov_b32 v147, v131\n"
      "v_mov_b32 v148, v128\n"
      "v_mov_b32 v149, v129\n"
      "v_mov_b32 v150, v130\n"
      "v_mov_b32 v151, v131\n"
      "v_mov_b32 v152, v128\n"
      "v_mov_b32 v153, v129\n"
      "v_mov_b32 v154, v130\n"
      "v_mov_b32 v155, v131\n"
      "v_mov_b32 v156, v128\n"
      "v_mov_b32 v157, v129\n"
      "v_mov_b32 v158, v130\n"
      "v_mov_b32 v159, v131\n"
      "v_lshlrev_b32 v160, 16, v26\n"
      "v_and_b32 v161, 0xffff0000, v26\n"
      "v_lshlrev_b32 v162, 16, v27\n"
      "v_and_b32 v163, 0xffff0000, v27\n"
      "v_mov_b32 v164, v160\n"
      "v_mov_b32 v165, v161\n"
      "v_mov_b32 v166, v162\n"
      "v_mov_b32 v167, v163\n"
      "v_mov_b32 v168, v160\n"
      "v_mov_b32 v169, v161\n"
      "v_mov_b32 v170, v162\n"
      "v_mov_b32 v171, v163\n"
      "v_mov_b32 v172, v160\n"
      "v_mov_b32 v173, v161\n"
      "v_mov_b32 v174, v162\n"
      "v_mov_b32 v175, v163\n"
      "v_mov_b32 v176, v160\n"
      "v_mov_b32 v177, v161\n"
      "v_mov_b32 v178, v162\n"
      "v_mov_b32 v179, v163\n"
      "v_mov_b32 v180, v160\n"
      "v_mov_b32 v181, v161\n"
      "v_mov_b32 v182, v162\n"
      "v_mov_b32 v183, v163\n"
      "v_mov_b32 v184, v160\n"
      "v_mov_b32 v185, v161\n"
      "v_mov_b32 v186, v162\n"
      "v_mov_b32 v187, v163\n"
      "v_mov_b32 v188, v160\n"
      "v_mov_b32 v189, v161\n"
      "v_mov_b32 v190, v162\n"
      "v_mov_b32 v191, v163\n"
      "v_lshlrev_b32 v192, 16, v28\n"
      "v_and_b32 v193, 0xffff0000, v28\n"
      "v_lshlrev_b32 v194, 16, v29\n"
      "v_and_b32 v195, 0xffff0000, v29\n"
      "v_mov_b32 v196, v192\n"
      "v_mov_b32 v197, v193\n"
      "v_mov_b32 v198, v194\n"
      "v_mov_b32 v199, v195\n"
      "v_mov_b32 v200, v192\n"
      "v_mov_b32 v201, v193\n"
      "v_mov_b32 v202, v194\n"
      "v_mov_b32 v203, v195\n"
      "v_mov_b32 v204, v192\n"
      "v_mov_b32 v205, v193\n"
      "v_mov_b32 v206, v194\n"
      "v_mov_b32 v207, v195\n"
      "v_mov_b32 v208, v192\n"
      "v_mov_b32 v209, v193\n"
      "v_mov_b32 v210, v194\n"
      "v_mov_b32 v211, v195\n"
      "v_mov_b32 v212, v192\n"
      "v_mov_b32 v213, v193\n"
      "v_mov_b32 v214, v194\n"
      "v_mov_b32 v215, v195\n"
      "v_mov_b32 v216, v192\n"
      "v_mov_b32 v217, v193\n"
      "v_mov_b32 v218, v194\n"
      "v_mov_b32 v219, v195\n"
      "v_mov_b32 v220, v192\n"
      "v_mov_b32 v221, v193\n"
      "v_mov_b32 v222, v194\n"
      "v_mov_b32 v223, v195\n"
      "v_lshlrev_b32 v224, 16, v30\n"
      "v_and_b32 v225, 0xffff0000, v30\n"
      "v_lshlrev_b32 v226, 16, v31\n"
      "v_and_b32 v227, 0xffff0000, v31\n"
      "v_mov_b32 v228, v224\n"
      "v_mov_b32 v229, v225\n"
      "v_mov_b32 v230, v226\n"
      "v_mov_b32 v231, v227\n"
      "v_mov_b32 v232, v224\n"
      "v_mov_b32 v233, v225\n"
      "v_mov_b32 v234, v226\n"
      "v_mov_b32 v235, v227\n"
      "v_mov_b32 v236, v224\n"
      "v_mov_b32 v237, v225\n"
      "v_mov_b32 v238, v226\n"
      "v_mov_b32 v239, v227\n"
      "v_mov_b32 v240, v224\n"
      "v_mov_b32 v241, v225\n"
      "v_mov_b32 v242, v226\n"
      "v_mov_b32 v243, v227\n"
      "v_mov_b32 v244, v224\n"
      "v_mov_b32 v245, v225\n"
      "v_mov_b32 v246, v226\n"
      "v_mov_b32 v247, v227\n"
      "v_mov_b32 v248, v224\n"
      "v_mov_b32 v249, v225\n"
      "v_mov_b32 v250, v226\n"
      "v_mov_b32 v251, v227\n"
      "v_mov_b32 v252, v224\n"
      "v_mov_b32 v253, v225\n"
      "v_mov_b32 v254, v226\n"
      "v_mov_b32 v255, v227\n"
      "s_waitcnt vmcnt(16)\n"
      "ds_read_b64_tr_b16 v[32:33], %[wa0] offset:0\n"
      "ds_read_b64_tr_b16 v[34:35], %[wa0] offset:512\n"
      "ds_read_b64_tr_b16 v[48:49], %[wa0] offset:4096\n"
      "ds_read_b64_tr_b16 v[50:51], %[wa0] offset:4608\n"
      "ds_read_b64_tr_b16 v[36:37], %[wa1] offset:0\n"
      "ds_read_b64_tr_b16 v[38:39], %[wa1] offset:512\n"
      "ds_read_b64_tr_b16 v[52:53], %[wa1] offset:4096\n"
      "ds_read_b64_tr_b16 v[54:55], %[wa1] offset:4608\n"
      "s_barrier\n"
      "ds_read_b128 v[64:67], %[xa0] offset:0\n"
      "ds_read_b128 v[96:99], %[xa1] offset:0\n"
      "ds_read_b128 v[68:71], %[xa0] offset:2048\n"
      "ds_read_b128 v[100:103], %[xa1] offset:2048\n"
      "ds_read_b128 v[72:75], %[xa0] offset:4096\n"
      "ds_read_b128 v[104:107], %[xa1] offset:4096\n"
      "ds_read_b128 v[76:79], %[xa0] offset:6144\n"
      "ds_read_b128 v[108:111], %[xa1] offset:6144\n"
      "ds_read_b128 v[80:83], %[xa0] offset:8192\n"
      "ds_read_b128 v[112:115], %[xa1] offset:8192\n"
      "ds_read_b128 v[84:87], %[xa0] offset:10240\n"
      "ds_read_b128 v[116:119], %[xa1] offset:10240\n"
      "ds_read_b128 v[88:91], %[xa0] offset:12288\n"
      "ds_read_b128 v[120:123], %[xa1] offset:12288\n"
      "ds_read_b128 v[92:95], %[xa0] offset:14336\n"
      "ds_read_b128 v[124:127], %[xa1] offset:14336\n"
      "s_waitcnt lgkmcnt(8)\n"
      "v_mfma_f32_16x16x32_bf16 v[128:131], v[32:35], v[64:67], v[128:131]\n"
      "v_mfma_f32_16x16x32_bf16 v[128:131], v[48:51], v[96:99], v[128:131]\n"
      "v_mfma_f32_16x16x32_bf16 v[160:163], v[52:55], v[96:99], v[160:163]\n"
      "ds_read_b64_tr_b16 v[40:41], %[wa2] offset:0\n"
      "v_mfma_f32_16x16x32_bf16 v[160:163], v[36:39], v[64:67], v[160:163]\n"
      "v_mfma_f32_16x16x32_bf16 v[164:167], v[36:39], v[68:71], v[164:167]\n"
      "ds_read_b64_tr_b16 v[42:43], %[wa2] offset:512\n"
      "v_mfma_f32_16x16x32_bf16 v[164:167], v[52:55], v[100:103], v[164:167]\n"
      "ds_read_b64_tr_b16 v[56:57], %[wa2] offset:4096\n"
      "v_mfma_f32_16x16x32_bf16 v[132:135], v[48:51], v[100:103], v[132:135]\n"
      "v_mfma_f32_16x16x32_bf16 v[132:135], v[32:35], v[68:71], v[132:135]\n"
      "ds_read_b64_tr_b16 v[58:59], %[wa2] offset:4608\n"
      "v_mfma_f32_16x16x32_bf16 v[136:139], v[32:35], v[72:75], v[136:139]\n"
      "ds_read_b64_tr_b16 v[44:45], %[wa3] offset:0\n"
      "v_mfma_f32_16x16x32_bf16 v[136:139], v[48:51], v[104:107], v[136:139]\n"
      "v_mfma_f32_16x16x32_bf16 v[168:171], v[52:55], v[104:107], v[168:171]\n"
      "ds_read_b64_tr_b16 v[46:47], %[wa3] offset:512\n"
      "v_mfma_f32_16x16x32_bf16 v[168:171], v[36:39], v[72:75], v[168:171]\n"
      "ds_read_b64_tr_b16 v[60:61], %[wa3] offset:4096\n"
      "v_mfma_f32_16x16x32_bf16 v[172:175], v[36:39], v[76:79], v[172:175]\n"
      "v_mfma_f32_16x16x32_bf16 v[172:175], v[52:55], v[108:111], v[172:175]\n"
      "ds_read_b64_tr_b16 v[62:63], %[wa3] offset:4608\n"
      "v_mfma_f32_16x16x32_bf16 v[140:143], v[48:51], v[108:111], v[140:143]\n"
      "v_mfma_f32_16x16x32_bf16 v[140:143], v[32:35], v[76:79], v[140:143]\n"
      "s_waitcnt lgkmcnt(8)\n"
      "v_mfma_f32_16x16x32_bf16 v[144:147], v[32:35], v[80:83], v[144:147]\n"
      "v_mfma_f32_16x16x32_bf16 v[144:147], v[48:51], v[112:115], v[144:147]\n"
      "v_mfma_f32_16x16x32_bf16 v[176:179], v[52:55], v[112:115], v[176:179]\n"
      "v_mfma_f32_16x16x32_bf16 v[176:179], v[36:39], v[80:83], v[176:179]\n"
      "v_mfma_f32_16x16x32_bf16 v[180:183], v[36:39], v[84:87], v[180:183]\n"
      "v_mfma_f32_16x16x32_bf16 v[180:183], v[52:55], v[116:119], v[180:183]\n"
      "v_mfma_f32_16x16x32_bf16 v[148:151], v[48:51], v[116:119], v[148:151]\n"
      "v_mfma_f32_16x16x32_bf16 v[148:151], v[32:35], v[84:87], v[148:151]\n"
      "v_mfma_f32_16x16x32_bf16 v[152:155], v[32:35], v[88:91], v[152:155]\n"
      "v_mfma_f32_16x16x32_bf16 v[152:155], v[48:51], v[120:123], v[152:155]\n"
      "v_mfma_f32_16x16x32_bf16 v[184:187], v[52:55], v[120:123], v[184:187]\n"
      "v_mfma_f32_16x16x32_bf16 v[184:187], v[36:39], v[88:91], v[184:187]\n"
      "v_mfma_f32_16x16x32_bf16 v[188:191], v[36:39], v[92:95], v[188:191]\n"
      "v_mfma_f32_16x16x32_bf16 v[188:191], v[52:55], v[124:127], v[188:191]\n"
      "v_mfma_f32_16x16x32_bf16 v[156:159], v[48:51], v[124:127], v[156:159]\n"
      "v_mfma_f32_16x16x32_bf16 v[156:159], v[32:35], v[92:95], v[156:159]\n"
      "s_waitcnt lgkmcnt(0)\n"
      "v_mfma_f32_16x16x32_bf16 v[192:195], v[40:43], v[64:67], v[192:195]\n"
      "s_waitcnt vmcnt(12)\n"
      "v_mfma_f32_16x16x32_bf16 v[192:195], v[56:59], v[96:99], v[192:195]\n"
      "s_barrier\n"
      "v_mfma_f32_16x16x32_bf16 v[224:227], v[60:63], v[96:99], v[224:227]\n"
      "ds_read_b64_tr_b16 v[32:33], %[wa0] offset:8192\n"
      "v_mfma_f32_16x16x32_bf16 v[224:227], v[44:47], v[64:67], v[224:227]\n"
      "ds_read_b128 v[64:67], %[xa0] offset:16384\n"
      "v_mfma_f32_16x16x32_bf16 v[228:231], v[44:47], v[68:71], v[228:231]\n"
      "ds_read_b128 v[96:99], %[xa1] offset:16384\n"
      "v_mfma_f32_16x16x32_bf16 v[228:231], v[60:63], v[100:103], v[228:231]\n"
      "ds_read_b64_tr_b16 v[34:35], %[wa0] offset:8704\n"
      "v_mfma_f32_16x16x32_bf16 v[196:199], v[56:59], v[100:103], v[196:199]\n"
      "ds_read_b64_tr_b16 v[36:37], %[wa1] offset:8192\n"
      "v_mfma_f32_16x16x32_bf16 v[196:199], v[40:43], v[68:71], v[196:199]\n"
      "ds_read_b128 v[68:71], %[xa0] offset:18432\n"
      "v_mfma_f32_16x16x32_bf16 v[200:203], v[40:43], v[72:75], v[200:203]\n"
      "ds_read_b128 v[100:103], %[xa1] offset:18432\n"
      "v_mfma_f32_16x16x32_bf16 v[200:203], v[56:59], v[104:107], v[200:203]\n"
      "ds_read_b64_tr_b16 v[38:39], %[wa1] offset:8704\n"
      "v_mfma_f32_16x16x32_bf16 v[232:235], v[60:63], v[104:107], v[232:235]\n"
      "s_waitcnt vmcnt(8)\n"
      "v_mfma_f32_16x16x32_bf16 v[232:235], v[44:47], v[72:75], v[232:235]\n"
      "ds_read_b128 v[72:75], %[xa0] offset:20480\n"
      "v_mfma_f32_16x16x32_bf16 v[236:239], v[44:47], v[76:79], v[236:239]\n"
      "ds_read_b128 v[104:107], %[xa1] offset:20480\n"
      "v_mfma_f32_16x16x32_bf16 v[236:239], v[60:63], v[108:111], v[236:239]\n"
      "ds_read_b64_tr_b16 v[48:49], %[wa0] offset:12288\n"
      "v_mfma_f32_16x16x32_bf16 v[204:207], v[56:59], v[108:111], v[204:207]\n"
      "ds_read_b64_tr_b16 v[50:51], %[wa0] offset:12800\n"
      "v_mfma_f32_16x16x32_bf16 v[204:207], v[40:43], v[76:79], v[204:207]\n"
      "ds_read_b128 v[76:79], %[xa0] offset:22528\n"
      "v_mfma_f32_16x16x32_bf16 v[208:211], v[40:43], v[80:83], v[208:211]\n"
      "ds_read_b128 v[108:111], %[xa1] offset:22528\n"
      "v_mfma_f32_16x16x32_bf16 v[208:211], v[56:59], v[112:115], v[208:211]\n"
      "ds_read_b64_tr_b16 v[52:53], %[wa1] offset:12288\n"
      "v_mfma_f32_16x16x32_bf16 v[240:243], v[60:63], v[112:115], v[240:243]\n"
      "ds_read_b64_tr_b16 v[54:55], %[wa1] offset:12800\n"
      "v_mfma_f32_16x16x32_bf16 v[240:243], v[44:47], v[80:83], v[240:243]\n"
      "ds_read_b128 v[80:83], %[xa0] offset:24576\n"
      "v_mfma_f32_16x16x32_bf16 v[244:247], v[44:47], v[84:87], v[244:247]\n"
      "ds_read_b128 v[112:115], %[xa1] offset:24576\n"
      "v_mfma_f32_16x16x32_bf16 v[244:247], v[60:63], v[116:119], v[244:247]\n"
      "v_mfma_f32_16x16x32_bf16 v[212:215], v[56:59], v[116:119], v[212:215]\n"
      "v_mfma_f32_16x16x32_bf16 v[212:215], v[40:43], v[84:87], v[212:215]\n"
      "ds_read_b128 v[84:87], %[xa0] offset:26624\n"
      "v_mfma_f32_16x16x32_bf16 v[216:219], v[40:43], v[88:91], v[216:219]\n"
      "ds_read_b128 v[116:119], %[xa1] offset:26624\n"
      "v_mfma_f32_16x16x32_bf16 v[216:219], v[56:59], v[120:123], v[216:219]\n"
      "v_mfma_f32_16x16x32_bf16 v[248:251], v[60:63], v[120:123], v[248:251]\n"
      "v_mfma_f32_16x16x32_bf16 v[248:251], v[44:47], v[88:91], v[248:251]\n"
      "ds_read_b128 v[88:91], %[xa0] offset:28672\n"
      "v_mfma_f32_16x16x32_bf16 v[252:255], v[44:47], v[92:95], v[252:255]\n"
      "ds_read_b128 v[120:123], %[xa1] offset:28672\n"
      "v_mfma_f32_16x16x32_bf16 v[252:255], v[60:63], v[124:127], v[252:255]\n"
      "v_mfma_f32_16x16x32_bf16 v[220:223], v[56:59], v[124:127], v[220:223]\n"
      "v_mfma_f32_16x16x32_bf16 v[220:223], v[40:43], v[92:95], v[220:223]\n"
      "ds_read_b128 v[92:95], %[xa0] offset:30720\n"
      "s_waitcnt lgkmcnt(7)\n"
      "v_mfma_f32_16x16x32_bf16 v[128:131], v[32:35], v[64:67], v[128:131]\n"
      "ds_read_b128 v[124:127], %[xa1] offset:30720\n"
      "v_mfma_f32_16x16x32_bf16 v[128:131], v[48:51], v[96:99], v[128:131]\n"
      "s_add_u32 m0, %[sWb], 0\n"
      "v_mfma_f32_16x16x32_bf16 v[160:163], v[52:55], v[96:99], v[160:163]\n"
      "s_add_u32 %[sW], %[sW], %[d0]\n"
      "v_mfma_f32_16x16x32_bf16 v[160:163], v[36:39], v[64:67], v[160:163]\n"
      "ds_read_b64_tr_b16 v[40:41], %[wa2] offset:8192\n"
      "v_mfma_f32_16x16x32_bf16 v[164:167], v[36:39], v[68:71], v[164:167]\n"
      "buffer_load_dwordx4 %[wv0], %[srdW], %[sW] offen offset:0 lds\n"
      "v_mfma_f32_16x16x32_bf16 v[164:167], v[52:55], v[100:103], v[164:167]\n"
      "ds_read_b64_tr_b16 v[42:43], %[wa2] offset:8704\n"
      "v_mfma_f32_16x16x32_bf16 v[132:135], v[48:51], v[100:103], v[132:135]\n"
      "ds_read_b64_tr_b16 v[56:57], %[wa2] offset:12288\n"
      "v_mfma_f32_16x16x32_bf16 v[132:135], v[32:35], v[68:71], v[132:135]\n"
      "buffer_load_dwordx4 %[wv1], %[srdW], %[sW] offen offset:1024 lds\n"
      "v_mfma_f32_16x16x32_bf16 v[136:139], v[32:35], v[72:75], v[136:139]\n"
      "ds_read_b64_tr_b16 v[58:59], %[wa2] offset:12800\n"
      "v_mfma_f32_16x16x32_bf16 v[136:139], v[48:51], v[104:107], v[136:139]\n"
      "ds_read_b64_tr_b16 v[44:45], %[wa3] offset:8192\n"
      "v_mfma_f32_16x16x32_bf16 v[168:171], v[52:55], v[104:107], v[168:171]\n"
      "buffer_load_dwordx4 %[wv2], %[srdW], %[sW] offen offset:2048 lds\n"
      "v_mfma_f32_16x16x32_bf16 v[168:171], v[36:39], v[72:75], v[168:171]\n"
      "ds_read_b64_tr_b16 v[46:47], %[wa3] offset:8704\n"
      "v_mfma_f32_16x16x32_bf16 v[172:175], v[36:39], v[76:79], v[172:175]\n"
      "ds_read_b64_tr_b16 v[60:61], %[wa3] offset:12288\n"
      "v_mfma_f32_16x16x32_bf16 v[172:175], v[52:55], v[108:111], v[172:175]\n"
      "buffer_load_dwordx4 %[wv3], %[srdW], %[sW] offen offset:3072 lds\n"
      "v_mfma_f32_16x16x32_bf16 v[140:143], v[48:51], v[108:111], v[140:143]\n"
      "ds_read_b64_tr_b16 v[62:63], %[wa3] offset:12800\n"
      "v_mfma_f32_16x16x32_bf16 v[140:143], v[32:35], v[76:79], v[140:143]\n"
      "s_add_u32 m0, %[sWb], 4096\n"
      "s_waitcnt lgkmcnt(8)\n"
      "v_mfma_f32_16x16x32_bf16 v[144:147], v[32:35], v[80:83], v[144:147]\n"
      "s_add_u32 %[sW], %[sW], 0x8000\n"
      "v_mfma_f32_16x16x32_bf16 v[144:147], v[48:51], v[112:115], v[144:147]\n"
      "v_mfma_f32_16x16x32_bf16 v[176:179], v[52:55], v[112:115], v[176:179]\n"
      "buffer_load_dwordx4 %[wv0], %[srdW], %[sW] offen offset:0 lds\n"
      "v_mfma_f32_16x16x32_bf16 v[176:179], v[36:39], v[80:83], v[176:179]\n"
      "v_mfma_f32_16x16x32_bf16 v[180:183], v[36:39], v[84:87], v[180:183]\n"
      "v_mfma_f32_16x16x32_bf16 v[180:183], v[52:55], v[116:119], v[180:183]\n"
      "buffer_load_dwordx4 %[wv1], %[srdW], %[sW] offen offset:1024 lds\n"
      "v_mfma_f32_16x16x32_bf16 v[148:151], v[48:51], v[116:119], v[148:151]\n"
      "v_mfma_f32_16x16x32_bf16 v[148:151], v[32:35], v[84:87], v[148:151]\n"
      "v_mfma_f32_16x16x32_bf16 v[152:155], v[32:35], v[88:91], v[152:155]\n"
      "buffer_load_dwordx4 %[wv2], %[srdW], %[sW] offen offset:2048 lds\n"
      "v_mfma_f32_16x16x32_bf16 v[152:155], v[48:51], v[120:123], v[152:155]\n"
      "v_mfma_f32_16x16x32_bf16 v[184:187], v[52:55], v[120:123], v[184:187]\n"
      "v_mfma_f32_16x16x32_bf16 v[184:187], v[36:39], v[88:91], v[184:187]\n"
      "buffer_load_dwordx4 %[wv3], %[srdW], %[sW] offen offset:3072 lds\n"
      "v_mfma_f32_16x16x32_bf16 v[188:191], v[36:39], v[92:95], v[188:191]\n"
      "v_mfma_f32_16x16x32_bf16 v[188:191], v[52:55], v[124:127], v[188:191]\n"
      "v_mfma_f32_16x16x32_bf16 v[156:159], v[48:51], v[124:127], v[156:159]\n"
      "v_mfma_f32_16x16x32_bf16 v[156:159], v[32:35], v[92:95], v[156:159]\n"
      "s_waitcnt lgkmcnt(0)\n"
      "v_mfma_f32_16x16x32_bf16 v[192:195], v[40:43], v[64:67], v[192:195]\n"
      "s_waitcnt vmcnt(12)\n"
      "v_mfma_f32_16x16x32_bf16 v[192:195], v[56:59], v[96:99], v[192:195]\n"
      "s_barrier\n"
      "v_mfma_f32_16x16x32_bf16 v[224:227], v[60:63], v[96:99], v[224:227]\n"
      "ds_read_b64_tr_b16 v[32:33], %[wa0] offset:16384\n"
      "v_mfma_f32_16x16x32_bf16 v[224:227], v[44:47], v[64:67], v[224:227]\n"
      "ds_read_b128 v[64:67], %[xa0] offset:32768\n"
      "v_mfma_f32_16x16x32_bf16 v[228:231], v[44:47], v[68:71], v[228:231]\n"
      "ds_read_b128 v[96:99], %[xa1] offset:32768\n"
      "v_mfma_f32_16x16x32_bf16 v[228:231], v[60:63], v[100:103], v[228:231]\n"
      "ds_read_b64_tr_b16 v[34:35], %[wa0] offset:16896\n"
      "v_mfma_f32_16x16x32_bf16 v[196:199], v[56:59], v[100:103], v[196:199]\n"
      "ds_read_b64_tr_b16 v[36:37], %[wa1] offset:16384\n"
      "v_mfma_f32_16x16x32_bf16 v[196:199], v[40:43], v[68:71], v[196:199]\n"
      "ds_read_b128 v[68:71], %[xa0] offset:34816\n"
      "v_mfma_f32_16x16x32_bf16 v[200:203], v[40:43], v[72:75], v[200:203]\n"
      "ds_read_b128 v[100:103], %[xa1] offset:34816\n"
      "v_mfma_f32_16x16x32_bf16 v[200:203], v[56:59], v[104:107], v[200:203]\n"
      "ds_read_b64_tr_b16 v[38:39], %[wa1] offset:16896\n"
      "v_mfma_f32_16x16x32_bf16 v[232:235], v[60:63], v[104:107], v[232:235]\n"
      "s_waitcnt vmcnt(8)\n"
      "v_mfma_f32_16x16x32_bf16 v[232:235], v[44:47], v[72:75], v[232:235]\n"
      "ds_read_b128 v[72:75], %[xa0] offset:36864\n"
      "v_mfma_f32_16x16x32_bf16 v[236:239], v[44:47], v[76:79], v[236:239]\n"
      "ds_read_b128 v[104:107], %[xa1] offset:36864\n"
      "v_mfma_f32_16x16x32_bf16 v[236:239], v[60:63], v[108:111], v[236:239]\n"
      "ds_read_b64_tr_b16 v[48:49], %[wa0] offset:20480\n"
      "v_mfma_f32_16x16x32_bf16 v[204:207], v[56:59], v[108:111], v[204:207]\n"
      "ds_read_b64_tr_b16 v[50:51], %[wa0] offset:20992\n"
      "v_mfma_f32_16x16x32_bf16 v[204:207], v[40:43], v[76:79], v[204:207]\n"
      "ds_read_b128 v[76:79], %[xa0] offset:38912\n"
      "v_mfma_f32_16x16x32_bf16 v[208:211], v[40:43], v[80:83], v[208:211]\n"
      "ds_read_b128 v[108:111], %[xa1] offset:38912\n"
      "v_mfma_f32_16x16x32_bf16 v[208:211], v[56:59], v[112:115], v[208:211]\n"
      "ds_read_b64_tr_b16 v[52:53], %[wa1] offset:20480\n"
      "v_mfma_f32_16x16x32_bf16 v[240:243], v[60:63], v[112:115], v[240:243]\n"
      "ds_read_b64_tr_b16 v[54:55], %[wa1] offset:20992\n"
      "v_mfma_f32_16x16x32_bf16 v[240:243], v[44:47], v[80:83], v[240:243]\n"
      "ds_read_b128 v[80:83], %[xa0] offset:40960\n"
      "v_mfma_f32_16x16x32_bf16 v[244:247], v[44:47], v[84:87], v[244:247]\n"
      "ds_read_b128 v[112:115], %[xa1] offset:40960\n"
      "v_mfma_f32_16x16x32_bf16 v[244:247], v[60:63], v[116:119], v[244:247]\n"
      "v_mfma_f32_16x16x32_bf16 v[212:215], v[56:59], v[116:119], v[212:215]\n"
      "v_mfma_f32_16x16x32_bf16 v[212:215], v[40:43], v[84:87], v[212:215]\n"
      "ds_read_b128 v[84:87], %[xa0] offset:43008\n"
      "v_mfma_f32_16x16x32_bf16 v[216:219], v[40:43], v[88:91], v[216:219]\n"
      "ds_read_b128 v[116:119], %[xa1] offset:43008\n"
      "v_mfma_f32_16x16x32_bf16 v[216:219], v[56:59], v[120:123], v[216:219]\n"
      "v_mfma_f32_16x16x32_bf16 v[248:251], v[60:63], v[120:123], v[248:251]\n"
      "v_mfma_f32_16x16x32_bf16 v[248:251], v[44:47], v[88:91], v[248:251]\n"
      "ds_read_b128 v[88:91], %[xa0] offset:45056\n"
      "v_mfma_f32_16x16x32_bf16 v[252:255], v[44:47], v[92:95], v[252:255]\n"
      "ds_read_b128 v[120:123], %[xa1] offset:45056\n"
      "v_mfma_f32_16x16x32_bf16 v[252:255], v[60:63], v[124:127], v[252:255]\n"
      "v_mfma_f32_16x16x32_bf16 v[220:223], v[56:59], v[124:127], v[220:223]\n"
      "v_mfma_f32_16x16x32_bf16 v[220:223], v[40:43], v[92:95], v[220:223]\n"
      "ds_read_b128 v[92:95], %[xa0] offset:47104\n"
      "s_waitcnt lgkmcnt(7)\n"
      "v_mfma_f32_16x16x32_bf16 v[128:131], v[32:35], v[64:67], v[128:131]\n"
      "ds_read_b128 v[124:127], %[xa1] offset:47104\n"
      "v_mfma_f32_16x16x32_bf16 v[128:131], v[48:51], v[96:99], v[128:131]\n"
      "s_add_u32 m0, %[sWb], 8192\n"
      "v_mfma_f32_16x16x32_bf16 v[160:163], v[52:55], v[96:99], v[160:163]\n"
      "s_add_u32 %[sW], %[sW], %[d1]\n"
      "v_mfma_f32_16x16x32_bf16 v[160:163], v[36:39], v[64:67], v[160:163]\n"
      "ds_read_b64_tr_b16 v[40:41], %[wa2] offset:16384\n"
      "v_mfma_f32_16x16x32_bf16 v[164:167], v[36:39], v[68:71], v[164:167]\n"
      "buffer_load_dwordx4 %[wv0], %[srdW], %[sW] offen offset:0 lds\n"
      "v_mfma_f32_16x16x32_bf16 v[164:167], v[52:55], v[100:103], v[164:167]\n"
      "ds_read_b64_tr_b16 v[42:43], %[wa2] offset:16896\n"
      "v_mfma_f32_16x16x32_bf16 v[132:135], v[48:51], v[100:103], v[132:135]\n"
      "ds_read_b64_tr_b16 v[56:57], %[wa2] offset:20480\n"
      "v_mfma_f32_16x16x32_bf16 v[132:135], v[32:35], v[68:71], v[132:135]\n"
      "buffer_load_dwordx4 %[wv1], %[srdW], %[sW] offen offset:1024 lds\n"
      "v_mfma_f32_16x16x32_bf16 v[136:139], v[32:35], v[72:75], v[136:139]\n"
      "ds_read_b64_tr_b16 v[58:59], %[wa2] offset:20992\n"
      "v_mfma_f32_16x16x32_bf16 v[136:139], v[48:51], v[104:107], v[136:139]\n"
      "ds_read_b64_tr_b16 v[44:45], %[wa3] offset:16384\n"
      "v_mfma_f32_16x16x32_bf16 v[168:171], v[52:55], v[104:107], v[168:171]\n"
      "buffer_load_dwordx4 %[wv2], %[srdW], %[sW] offen offset:2048 lds\n"
      "v_mfma_f32_16x16x32_bf16 v[168:171], v[36:39], v[72:75], v[168:171]\n"
      "ds_read_b64_tr_b16 v[46:47], %[wa3] offset:16896\n"
      "v_mfma_f32_16x16x32_bf16 v[172:175], v[36:39], v[76:79], v[172:175]\n"
      "ds_read_b64_tr_b16 v[60:61], %[wa3] offset:20480\n"
      "v_mfma_f32_16x16x32_bf16 v[172:175], v[52:55], v[108:111], v[172:175]\n"
      "buffer_load_dwordx4 %[wv3], %[srdW], %[sW] offen offset:3072 lds\n"
      "v_mfma_f32_16x16x32_bf16 v[140:143], v[48:51], v[108:111], v[140:143]\n"
      "ds_read_b64_tr_b16 v[62:63], %[wa3] offset:20992\n"
      "v_mfma_f32_16x16x32_bf16 v[140:143], v[32:35], v[76:79], v[140:143]\n"
      "s_add_u32 m0, %[sWb], 12288\n"
      "s_waitcnt lgkmcnt(8)\n"
      "v_mfma_f32_16x16x32_bf16 v[144:147], v[32:35], v[80:83], v[144:147]\n"
      "s_add_u32 %[sW], %[sW], 0x8000\n"
      "v_mfma_f32_16x16x32_bf16 v[144:147], v[48:51], v[112:115], v[144:147]\n"
      "v_mfma_f32_16x16x32_bf16 v[176:179], v[52:55], v[112:115], v[176:179]\n"
      "buffer_load_dwordx4 %[wv0], %[srdW], %[sW] offen offset:0 lds\n"
      "v_mfma_f32_16x16x32_bf16 v[176:179], v[36:39], v[80:83], v[176:179]\n"
      "v_mfma_f32_16x16x32_bf16 v[180:183], v[36:39], v[84:87], v[180:183]\n"
      "v_mfma_f32_16x16x32_bf16 v[180:183], v[52:55], v[116:119], v[180:183]\n"
      "buffer_load_dwordx4 %[wv1], %[srdW], %[sW] offen offset:1024 lds\n"
      "v_mfma_f32_16x16x32_bf16 v[148:151], v[48:51], v[116:119], v[148:151]\n"
      "v_mfma_f32_16x16x32_bf16 v[148:151], v[32:35], v[84:87], v[148:151]\n"
      "v_mfma_f32_16x16x32_bf16 v[152:155], v[32:35], v[88:91], v[152:155]\n"
      "buffer_load_dwordx4 %[wv2], %[srdW], %[sW] offen offset:2048 lds\n"
      "v_mfma_f32_16x16x32_bf16 v[152:155], v[48:51], v[120:123], v[152:155]\n"
      "v_mfma_f32_16x16x32_bf16 v[184:187], v[52:55], v[120:123], v[184:187]\n"
      "v_mfma_f32_16x16x32_bf16 v[184:187], v[36:39], v[88:91], v[184:187]\n"
      "buffer_load_dwordx4 %[wv3], %[srdW], %[sW] offen offset:3072 lds\n"
      "v_mfma_f32_16x16x32_bf16 v[188:191], v[36:39], v[92:95], v[188:191]\n"
      "v_mfma_f32_16x16x32_bf16 v[188:191], v[52:55], v[124:127], v[188:191]\n"
      "v_mfma_f32_16x16x32_bf16 v[156:159], v[48:51], v[124:127], v[156:159]\n"
      "v_mfma_f32_16x16x32_bf16 v[156:159], v[32:35], v[92:95], v[156:159]\n"
      "s_waitcnt lgkmcnt(0)\n"
      "v_mfma_f32_16x16x32_bf16 v[192:195], v[40:43], v[64:67], v[192:195]\n"
      "s_waitcnt vmcnt(12)\n"
      "v_mfma_f32_16x16x32_bf16 v[192:195], v[56:59], v[96:99], v[192:195]\n"
      "s_barrier\n"
      "v_mfma_f32_16x16x32_bf16 v[224:227], v[60:63], v[96:99], v[224:227]\n"
      "ds_read_b64_tr_b16 v[32:33], %[wa0] offset:0\n"
      "v_mfma_f32_16x16x32_bf16 v[224:227], v[44:47], v[64:67], v[224:227]\n"
      "ds_read_b128 v[64:67], %[xa0] offset:49152\n"
      "v_mfma_f32_16x16x32_bf16 v[228:231], v[44:47], v[68:71], v[228:231]\n"
      "ds_read_b128 v[96:99], %[xa1] offset:49152\n"
      "v_mfma_f32_16x16x32_bf16 v[228:231], v[60:63], v[100:103], v[228:231]\n"
      "ds_read_b64_tr_b16 v[34:35], %[wa0] offset:512\n"
      "v_mfma_f32_16x16x32_bf16 v[196:199], v[56:59], v[100:103], v[196:199]\n"
      "ds_read_b64_tr_b16 v[36:37], %[wa1] offset:0\n"
      "v_mfma_f32_16x16x32_bf16 v[196:199], v[40:43], v[68:71], v[196:199]\n"
      "ds_read_b128 v[68:71], %[xa0] offset:51200\n"
      "v_mfma_f32_16x16x32_bf16 v[200:203], v[40:43], v[72:75], v[200:203]\n"
      "ds_read_b128 v[100:103], %[xa1] offset:51200\n"
      "v_mfma_f32_16x16x32_bf16 v[200:203], v[56:59], v[104:107], v[200:203]\n"
      "ds_read_b64_tr_b16 v[38:39], %[wa1] offset:512\n"
      "v_mfma_f32_16x16x32_bf16 v[232:235], v[60:63], v[104:107], v[232:235]\n"
      "s_waitcnt vmcnt(8)\n"
      "v_mfma_f32_16x16x32_bf16 v[232:235], v[44:47], v[72:75], v[232:235]\n"
      "ds_read_b128 v[72:75], %[xa0] offset:53248\n"
      "v_mfma_f32_16x16x32_bf16 v[236:239], v[44:47], v[76:79], v[236:239]\n"
      "ds_read_b128 v[104:107], %[xa1] offset:53248\n"
      "v_mfma_f32_16x16x32_bf16 v[236:239], v[60:63], v[108:111], v[236:239]\n"
      "ds_read_b64_tr_b16 v[48:49], %[wa0] offset:4096\n"
      "v_mfma_f32_16x16x32_bf16 v[204:207], v[56:59], v[108:111], v[204:207]\n"
      "ds_read_b64_tr_b16 v[50:51], %[wa0] offset:4608\n"
      "v_mfma_f32_16x16x32_bf16 v[204:207], v[40:43], v[76:79], v[204:207]\n"
      "ds_read_b128 v[76:79], %[xa0] offset:55296\n"
      "v_mfma_f32_16x16x32_bf16 v[208:211], v[40:43], v[80:83], v[208:211]\n"
      "ds_read_b128 v[108:111], %[xa1] offset:55296\n"
      "v_mfma_f32_16x16x32_bf16 v[208:211], v[56:59], v[112:115], v[208:211]\n"
      "ds_read_b64_tr_b16 v[52:53], %[wa1] offset:4096\n"
      "v_mfma_f32_16x16x32_bf16 v[240:243], v[60:63], v[112:115], v[240:243]\n"
      "ds_read_b64_tr_b16 v[54:55], %[wa1] offset:4608\n"
      "v_mfma_f32_16x16x32_bf16 v[240:243], v[44:47], v[80:83], v[240:243]\n"
      "ds_read_b128 v[80:83], %[xa0] offset:57344\n"
      "v_mfma_f32_16x16x32_bf16 v[244:247], v[44:47], v[84:87], v[244:247]\n"
      "ds_read_b128 v[112:115], %[xa1] offset:57344\n"
      "v_mfma_f32_16x16x32_bf16 v[244:247], v[60:63], v[116:119], v[244:247]\n"
      "v_mfma_f32_16x16x32_bf16 v[212:215], v[56:59], v[116:119], v[212:215]\n"
      "v_mfma_f32_16x16x32_bf16 v[212:215], v[40:43], v[84:87], v[212:215]\n"
      "ds_read_b128 v[84:87], %[xa0] offset:59392\n"
      "v_mfma_f32_16x16x32_bf16 v[216:219], v[40:43], v[88:91], v[216:219]\n"
      "ds_read_b128 v[116:119], %[xa1] offset:59392\n"
      "v_mfma_f32_16x16x32_bf16 v[216:219], v[56:59], v[120:123], v[216:219]\n"
      "v_mfma_f32_16x16x32_bf16 v[248:251], v[60:63], v[120:123], v[248:251]\n"
      "v_mfma_f32_16x16x32_bf16 v[248:251], v[44:47], v[88:91], v[248:251]\n"
      "ds_read_b128 v[88:91], %[xa0] offset:61440\n"
      "v_mfma_f32_16x16x32_bf16 v[252:255], v[44:47], v[92:95], v[252:255]\n"
      "ds_read_b128 v[120:123], %[xa1] offset:61440\n"
      "v_mfma_f32_16x16x32_bf16 v[252:255], v[60:63], v[124:127], v[252:255]\n"
      "v_mfma_f32_16x16x32_bf16 v[220:223], v[56:59], v[124:127], v[220:223]\n"
      "v_mfma_f32_16x16x32_bf16 v[220:223], v[40:43], v[92:95], v[220:223]\n"
      "ds_read_b128 v[92:95], %[xa0] offset:63488\n"
      "s_waitcnt lgkmcnt(7)\n"
      "v_mfma_f32_16x16x32_bf16 v[128:131], v[32:35], v[64:67], v[128:131]\n"
      "ds_read_b128 v[124:127], %[xa1] offset:63488\n"
      "v_mfma_f32_16x16x32_bf16 v[128:131], v[48:51], v[96:99], v[128:131]\n"
      "s_add_u32 m0, %[sWb], 16384\n"
      "v_mfma_f32_16x16x32_bf16 v[160:163], v[52:55], v[96:99], v[160:163]\n"
      "s_add_u32 %[sW], %[sW], %[d0]\n"
      "v_mfma_f32_16x16x32_bf16 v[160:163], v[36:39], v[64:67], v[160:163]\n"
      "ds_read_b64_tr_b16 v[40:41], %[wa2] offset:0\n"
      "v_mfma_f32_16x16x32_bf16 v[164:167], v[36:39], v[68:71], v[164:167]\n"
      "buffer_load_dwordx4 %[wv0], %[srdW], %[sW] offen offset:0 lds\n"
      "v_mfma_f32_16x16x32_bf16 v[164:167], v[52:55], v[100:103], v[164:167]\n"
      "ds_read_b64_tr_b16 v[42:43], %[wa2] offset:512\n"
      "v_mfma_f32_16x16x32_bf16 v[132:135], v[48:51], v[100:103], v[132:135]\n"
      "ds_read_b64_tr_b16 v[56:57], %[wa2] offset:4096\n"
      "v_mfma_f32_16x16x32_bf16 v[132:135], v[32:35], v[68:71], v[132:135]\n"
      "buffer_load_dwordx4 %[wv1], %[srdW], %[sW] offen offset:1024 lds\n"
      "v_mfma_f32_16x16x32_bf16 v[136:139], v[32:35], v[72:75], v[136:139]\n"
      "ds_read_b64_tr_b16 v[58:59], %[wa2] offset:4608\n"
      "v_mfma_f32_16x16x32_bf16 v[136:139], v[48:51], v[104:107], v[136:139]\n"
      "ds_read_b64_tr_b16 v[44:45], %[wa3] offset:0\n"
      "v_mfma_f32_16x16x32_bf16 v[168:171], v[52:55], v[104:107], v[168:171]\n"
      "buffer_load_dwordx4 %[wv2], %[srdW], %[sW] offen offset:2048 lds\n"
      "v_mfma_f32_16x16x32_bf16 v[168:171], v[36:39], v[72:75], v[168:171]\n"
      "ds_read_b64_tr_b16 v[46:47], %[wa3] offset:512\n"
      "v_mfma_f32_16x16x32_bf16 v[172:175], v[36:39], v[76:79], v[172:175]\n"
      "ds_read_b64_tr_b16 v[60:61], %[wa3] offset:4096\n"
      "v_mfma_f32_16x16x32_bf16 v[172:175], v[52:55], v[108:111], v[172:175]\n"
      "buffer_load_dwordx4 %[wv3], %[srdW], %[sW] offen offset:3072 lds\n"
      "v_mfma_f32_16x16x32_bf16 v[140:143], v[48:51], v[108:111], v[140:143]\n"
      "ds_read_b64_tr_b16 v[62:63], %[wa3] offset:4608\n"
      "v_mfma_f32_16x16x32_bf16 v[140:143], v[32:35], v[76:79], v[140:143]\n"
      "s_add_u32 m0, %[sWb], 20480\n"
      "s_waitcnt lgkmcnt(8)\n"
      "v_mfma_f32_16x16x32_bf16 v[144:147], v[32:35], v[80:83], v[144:147]\n"
      "s_add_u32 %[sW], %[sW], 0x8000\n"
      "v_mfma_f32_16x16x32_bf16 v[144:147], v[48:51], v[112:115], v[144:147]\n"
      "v_mfma_f32_16x16x32_bf16 v[176:179], v[52:55], v[112:115], v[176:179]\n"
      "buffer_load_dwordx4 %[wv0], %[srdW], %[sW] offen offset:0 lds\n"
      "v_mfma_f32_16x16x32_bf16 v[176:179], v[36:39], v[80:83], v[176:179]\n"
      "v_mfma_f32_16x16x32_bf16 v[180:183], v[36:39], v[84:87], v[180:183]\n"
      "v_mfma_f32_16x16x32_bf16 v[180:183], v[52:55], v[116:119], v[180:183]\n"
      "buffer_load_dwordx4 %[wv1], %[srdW], %[sW] offen offset:1024 lds\n"
      "v_mfma_f32_16x16x32_bf16 v[148:151], v[48:51], v[116:119], v[148:151]\n"
      "v_mfma_f32_16x16x32_bf16 v[148:151], v[32:35], v[84:87], v[148:151]\n"
      "v_mfma_f32_16x16x32_bf16 v[152:155], v[32:35], v[88:91], v[152:155]\n"
      "buffer_load_dwordx4 %[wv2], %[srdW], %[sW] offen offset:2048 lds\n"
      "v_mfma_f32_16x16x32_bf16 v[152:155], v[48:51], v[120:123], v[152:155]\n"
      "v_mfma_f32_16x16x32_bf16 v[184:187], v[52:55], v[120:123], v[184:187]\n"
      "v_mfma_f32_16x16x32_bf16 v[184:187], v[36:39], v[88:91], v[184:187]\n"
      "buffer_load_dwordx4 %[wv3], %[srdW], %[sW] offen offset:3072 lds\n"
      "v_mfma_f32_16x16x32_bf16 v[188:191], v[36:39], v[92:95], v[188:191]\n"
      "v_mfma_f32_16x16x32_bf16 v[188:191], v[52:55], v[124:127], v[188:191]\n"
      "v_mfma_f32_16x16x32_bf16 v[156:159], v[48:51], v[124:127], v[156:159]\n"
      "v_mfma_f32_16x16x32_bf16 v[156:159], v[32:35], v[92:95], v[156:159]\n"
      "s_waitcnt lgkmcnt(0)\n"
      "v_mfma_f32_16x16x32_bf16 v[192:195], v[40:43], v[64:67], v[192:195]\n"
      "s_waitcnt vmcnt(12)\n"
      "v_mfma_f32_16x16x32_bf16 v[192:195], v[56:59], v[96:99], v[192:195]\n"
      "s_barrier\n"
      "v_mfma_f32_16x16x32_bf16 v[224:227], v[60:63], v[96:99], v[224:227]\n"
      "ds_read_b64_tr_b16 v[32:33], %[wa0] offset:8192\n"
      "v_mfma_f32_16x16x32_bf16 v[224:227], v[44:47], v[64:67], v[224:227]\n"
      "ds_read_b128 v[64:67], %[xa0] offset:0\n"
      "v_mfma_f32_16x16x32_bf16 v[228:231], v[44:47], v[68:71], v[228:231]\n"
      "ds_read_b128 v[96:99], %[xa1] offset:0\n"
      "v_mfma_f32_16x16x32_bf16 v[228:231], v[60:63], v[100:103], v[228:231]\n"
      "ds_read_b64_tr_b16 v[34:35], %[wa0] offset:8704\n"
      "v_mfma_f32_16x16x32_bf16 v[196:199], v[56:59], v[100:103], v[196:199]\n"
      "ds_read_b64_tr_b16 v[36:37], %[wa1] offset:8192\n"
      "v_mfma_f32_16x16x32_bf16 v[196:199], v[40:43], v[68:71], v[196:199]\n"
      "ds_read_b128 v[68:71], %[xa0] offset:2048\n"
      "v_mfma_f32_16x16x32_bf16 v[200:203], v[40:43], v[72:75], v[200:203]\n"
      "ds_read_b128 v[100:103], %[xa1] offset:2048\n"
      "v_mfma_f32_16x16x32_bf16 v[200:203], v[56:59], v[104:107], v[200:203]\n"
      "ds_read_b64_tr_b16 v[38:39], %[wa1] offset:8704\n"
      "v_mfma_f32_16x16x32_bf16 v[232:235], v[60:63], v[104:107], v[232:235]\n"
      "s_waitcnt vmcnt(8)\n"
      "v_mfma_f32_16x16x32_bf16 v[232:235], v[44:47], v[72:75], v[232:235]\n"
      "ds_read_b128 v[72:75], %[xa0] offset:4096\n"
      "v_mfma_f32_16x16x32_bf16 v[236:239], v[44:47], v[76:79], v[236:239]\n"
      "ds_read_b128 v[104:107], %[xa1] offset:4096\n"
      "v_mfma_f32_16x16x32_bf16 v[236:239], v[60:63], v[108:111], v[236:239]\n"
      "ds_read_b64_tr_b16 v[48:49], %[wa0] offset:12288\n"
      "v_mfma_f32_16x16x32_bf16 v[204:207], v[56:59], v[108:111], v[204:207]\n"
      "ds_read_b64_tr_b16 v[50:51], %[wa0] offset:12800\n"
      "v_mfma_f32_16x16x32_bf16 v[204:207], v[40:43], v[76:79], v[204:207]\n"
      "ds_read_b128 v[76:79], %[xa0] offset:6144\n"
      "v_mfma_f32_16x16x32_bf16 v[208:211], v[40:43], v[80:83], v[208:211]\n"
      "ds_read_b128 v[108:111], %[xa1] offset:6144\n"
      "v_mfma_f32_16x16x32_bf16 v[208:211], v[56:59], v[112:115], v[208:211]\n"
      "ds_read_b64_tr_b16 v[52:53], %[wa1] offset:12288\n"
      "v_mfma_f32_16x16x32_bf16 v[240:243], v[60:63], v[112:115], v[240:243]\n"
      "ds_read_b64_tr_b16 v[54:55], %[wa1] offset:12800\n"
      "v_mfma_f32_16x16x32_bf16 v[240:243], v[44:47], v[80:83], v[240:243]\n"
      "ds_read_b128 v[80:83], %[xa0] offset:8192\n"
      "v_mfma_f32_16x16x32_bf16 v[244:247], v[44:47], v[84:87], v[244:247]\n"
      "ds_read_b128 v[112:115], %[xa1] offset:8192\n"
      "v_mfma_f32_16x16x32_bf16 v[244:247], v[60:63], v[116:119], v[244:247]\n"
      "v_mfma_f32_16x16x32_bf16 v[212:215], v[56:59], v[116:119], v[212:215]\n"
      "v_mfma_f32_16x16x32_bf16 v[212:215], v[40:43], v[84:87], v[212:215]\n"
      "ds_read_b128 v[84:87], %[xa0] offset:10240\n"
      "v_mfma_f32_16x16x32_bf16 v[216:219], v[40:43], v[88:91], v[216:219]\n"
      "ds_read_b128 v[116:119], %[xa1] offset:10240\n"
      "v_mfma_f32_16x16x32_bf16 v[216:219], v[56:59], v[120:123], v[216:219]\n"
      "v_mfma_f32_16x16x32_bf16 v[248:251], v[60:63], v[120:123], v[248:251]\n"
      "v_mfma_f32_16x16x32_bf16 v[248:251], v[44:47], v[88:91], v[248:251]\n"
      "ds_read_b128 v[88:91], %[xa0] offset:12288\n"
      "v_mfma_f32_16x16x32_bf16 v[252:255], v[44:47], v[92:95], v[252:255]\n"
      "ds_read_b128 v[120:123], %[xa1] offset:12288\n"
      "v_mfma_f32_16x16x32_bf16 v[252:255], v[60:63], v[124:127], v[252:255]\n"
      "v_mfma_f32_16x16x32_bf16 v[220:223], v[56:59], v[124:127], v[220:223]\n"
      "v_mfma_f32_16x16x32_bf16 v[220:223], v[40:43], v[92:95], v[220:223]\n"
      "ds_read_b128 v[92:95], %[xa0] offset:14336\n"
      "s_waitcnt lgkmcnt(7)\n"
      "v_mfma_f32_16x16x32_bf16 v[128:131], v[32:35], v[64:67], v[128:131]\n"
      "ds_read_b128 v[124:127], %[xa1] offset:14336\n"
      "v_mfma_f32_16x16x32_bf16 v[128:131], v[48:51], v[96:99], v[128:131]\n"
      "s_add_u32 m0, %[sWb], 0\n"
      "v_mfma_f32_16x16x32_bf16 v[160:163], v[52:55], v[96:99], v[160:163]\n"
      "s_add_u32 %[sW], %[sW], %[d1]\n"
      "v_mfma_f32_16x16x32_bf16 v[160:163], v[36:39], v[64:67], v[160:163]\n"
      "ds_read_b64_tr_b16 v[40:41], %[wa2] offset:8192\n"
      "v_mfma_f32_16x16x32_bf16 v[164:167], v[36:39], v[68:71], v[164:167]\n"
      "buffer_load_dwordx4 %[wv0], %[srdW], %[sW] offen offset:0 lds\n"
      "v_mfma_f32_16x16x32_bf16 v[164:167], v[52:55], v[100:103], v[164:167]\n"
      "ds_read_b64_tr_b16 v[42:43], %[wa2] offset:8704\n"
      "v_mfma_f32_16x16x32_bf16 v[132:135], v[48:51], v[100:103], v[132:135]\n"
      "ds_read_b64_tr_b16 v[56:57], %[wa2] offset:12288\n"
      "v_mfma_f32_16x16x32_bf16 v[132:135], v[32:35], v[68:71], v[132:135]\n"
      "buffer_load_dwordx4 %[wv1], %[srdW], %[sW] offen offset:1024 lds\n"
      "v_mfma_f32_16x16x32_bf16 v[136:139], v[32:35], v[72:75], v[136:139]\n"
      "ds_read_b64_tr_b16 v[58:59], %[wa2] offset:12800\n"
      "v_mfma_f32_16x16x32_bf16 v[136:139], v[48:51], v[104:107], v[136:139]\n"
      "ds_read_b64_tr_b16 v[44:45], %[wa3] offset:8192\n"
      "v_mfma_f32_16x16x32_bf16 v[168:171], v[52:55], v[104:107], v[168:171]\n"
      "buffer_load_dwordx4 %[wv2], %[srdW], %[sW] offen offset:2048 lds\n"
      "v_mfma_f32_16x16x32_bf16 v[168:171], v[36:39], v[72:75], v[168:171]\n"
      "ds_read_b64_tr_b16 v[46:47], %[wa3] offset:8704\n"
      "v_mfma_f32_16x16x32_bf16 v[172:175], v[36:39], v[76:79], v[172:175]\n"
      "ds_read_b64_tr_b16 v[60:61], %[wa3] offset:12288\n"
      "v_mfma_f32_16x16x32_bf16 v[172:175], v[52:55], v[108:111], v[172:175]\n"
      "buffer_load_dwordx4 %[wv3], %[srdW], %[sW] offen offset:3072 lds\n"
      "v_mfma_f32_16x16x32_bf16 v[140:143], v[48:51], v[108:111], v[140:143]\n"
      "ds_read_b64_tr_b16 v[62:63], %[wa3] offset:12800\n"
      "v_mfma_f32_16x16x32_bf16 v[140:143], v[32:35], v[76:79], v[140:143]\n"
      "s_add_u32 m0, %[sWb], 4096\n"
      "s_waitcnt lgkmcnt(8)\n"
      "v_mfma_f32_16x16x32_bf16 v[144:147], v[32:35], v[80:83], v[144:147]\n"
      "s_add_u32 %[sW], %[sW], 0x8000\n"
      "v_mfma_f32_16x16x32_bf16 v[144:147], v[48:51], v[112:115], v[144:147]\n"
      "v_mfma_f32_16x16x32_bf16 v[176:179], v[52:55], v[112:115], v[176:179]\n"
      "buffer_load_dwordx4 %[wv0], %[srdW], %[sW] offen offset:0 lds\n"
      "v_mfma_f32_16x16x32_bf16 v[176:179], v[36:39], v[80:83], v[176:179]\n"
      "v_mfma_f32_16x16x32_bf16 v[180:183], v[36:39], v[84:87], v[180:183]\n"
      "v_mfma_f32_16x16x32_bf16 v[180:183], v[52:55], v[116:119], v[180:183]\n"
      "buffer_load_dwordx4 %[wv1], %[srdW], %[sW] offen offset:1024 lds\n"
      "v_mfma_f32_16x16x32_bf16 v[148:151], v[48:51], v[116:119], v[148:151]\n"
      "v_mfma_f32_16x16x32_bf16 v[148:151], v[32:35], v[84:87], v[148:151]\n"
      "v_mfma_f32_16x16x32_bf16 v[152:155], v[32:35], v[88:91], v[152:155]\n"
      "buffer_load_dwordx4 %[wv2], %[srdW], %[sW] offen offset:2048 lds\n"
      "v_mfma_f32_16x16x32_bf16 v[152:155], v[48:51], v[120:123], v[152:155]\n"
      "v_mfma_f32_16x16x32_bf16 v[184:187], v[52:55], v[120:123], v[184:187]\n"
      "v_mfma_f32_16x16x32_bf16 v[184:187], v[36:39], v[88:91], v[184:187]\n"
      "buffer_load_dwordx4 %[wv3], %[srdW], %[sW] offen offset:3072 lds\n"
      "v_mfma_f32_16x16x32_bf16 v[188:191], v[36:39], v[92:95], v[188:191]\n"
      "v_mfma_f32_16x16x32_bf16 v[188:191], v[52:55], v[124:127], v[188:191]\n"
      "v_mfma_f32_16x16x32_bf16 v[156:159], v[48:51], v[124:127], v[156:159]\n"
      "v_mfma_f32_16x16x32_bf16 v[156:159], v[32:35], v[92:95], v[156:159]\n"
      "s_waitcnt lgkmcnt(0)\n"
      "v_mfma_f32_16x16x32_bf16 v[192:195], v[40:43], v[64:67], v[192:195]\n"
      "s_waitcnt vmcnt(12)\n"
      "v_mfma_f32_16x16x32_bf16 v[192:195], v[56:59], v[96:99], v[192:195]\n"
      "s_barrier\n"
      "v_mfma_f32_16x16x32_bf16 v[224:227], v[60:63], v[96:99], v[224:227]\n"
      "ds_read_b64_tr_b16 v[32:33], %[wa0] offset:16384\n"
      "v_mfma_f32_16x16x32_bf16 v[224:227], v[44:47], v[64:67], v[224:227]\n"
      "ds_read_b128 v[64:67], %[xa0] offset:16384\n"
      "v_mfma_f32_16x16x32_bf16 v[228:231], v[44:47], v[68:71], v[228:231]\n"
      "ds_read_b128 v[96:99], %[xa1] offset:16384\n"
      "v_mfma_f32_16x16x32_bf16 v[228:231], v[60:63], v[100:103], v[228:231]\n"
      "ds_read_b64_tr_b16 v[34:35], %[wa0] offset:16896\n"
      "v_mfma_f32_16x16x32_bf16 v[196:199], v[56:59], v[100:103], v[196:199]\n"
      "ds_read_b64_tr_b16 v[36:37], %[wa1] offset:16384\n"
      "v_mfma_f32_16x16x32_bf16 v[196:199], v[40:43], v[68:71], v[196:199]\n"
      "ds_read_b128 v[68:71], %[xa0] offset:18432\n"
      "v_mfma_f32_16x16x32_bf16 v[200:203], v[40:43], v[72:75], v[200:203]\n"
      "ds_read_b128 v[100:103], %[xa1] offset:18432\n"
      "v_mfma_f32_16x16x32_bf16 v[200:203], v[56:59], v[104:107], v[200:203]\n"
      "ds_read_b64_tr_b16 v[38:39], %[wa1] offset:16896\n"
      "v_mfma_f32_16x16x32_bf16 v[232:235], v[60:63], v[104:107], v[232:235]\n"
      "s_waitcnt vmcnt(8)\n"
      "v_mfma_f32_16x16x32_bf16 v[232:235], v[44:47], v[72:75], v[232:235]\n"
      "ds_read_b128 v[72:75], %[xa0] offset:20480\n"
      "v_mfma_f32_16x16x32_bf16 v[236:239], v[44:47], v[76:79], v[236:239]\n"
      "ds_read_b128 v[104:107], %[xa1] offset:20480\n"
      "v_mfma_f32_16x16x32_bf16 v[236:239], v[60:63], v[108:111], v[236:239]\n"
      "ds_read_b64_tr_b16 v[48:49], %[wa0] offset:20480\n"
      "v_mfma_f32_16x16x32_bf16 v[204:207], v[56:59], v[108:111], v[204:207]\n"
      "ds_read_b64_tr_b16 v[50:51], %[wa0] offset:20992\n"
      "v_mfma_f32_16x16x32_bf16 v[204:207], v[40:43], v[76:79], v[204:207]\n"
      "ds_read_b128 v[76:79], %[xa0] offset:22528\n"
      "v_mfma_f32_16x16x32_bf16 v[208:211], v[40:43], v[80:83], v[208:211]\n"
      "ds_read_b128 v[108:111], %[xa1] offset:22528\n"
      "v_mfma_f32_16x16x32_bf16 v[208:211], v[56:59], v[112:115], v[208:211]\n"
      "ds_read_b64_tr_b16 v[52:53], %[wa1] offset:20480\n"
      "v_mfma_f32_16x16x32_bf16 v[240:243], v[60:63], v[112:115], v[240:243]\n"
      "ds_read_b64_tr_b16 v[54:55], %[wa1] offset:20992\n"
      "v_mfma_f32_16x16x32_bf16 v[240:243], v[44:47], v[80:83], v[240:243]\n"
      "ds_read_b128 v[80:83], %[xa0] offset:24576\n"
      "v_mfma_f32_16x16x32_bf16 v[244:247], v[44:47], v[84:87], v[244:247]\n"
      "ds_read_b128 v[112:115], %[xa1] offset:24576\n"
      "v_mfma_f32_16x16x32_bf16 v[244:247], v[60:63], v[116:119], v[244:247]\n"
      "v_mfma_f32_16x16x32_bf16 v[212:215], v[56:59], v[116:119], v[212:215]\n"
      "v_mfma_f32_16x16x32_bf16 v[212:215], v[40:43], v[84:87], v[212:215]\n"
      "ds_read_b128 v[84:87], %[xa0] offset:26624\n"
      "v_mfma_f32_16x16x32_bf16 v[216:219], v[40:43], v[88:91], v[216:219]\n"
      "ds_read_b128 v[116:119], %[xa1] offset:26624\n"
      "v_mfma_f32_16x16x32_bf16 v[216:219], v[56:59], v[120:123], v[216:219]\n"
      "v_mfma_f32_16x16x32_bf16 v[248:251], v[60:63], v[120:123], v[248:251]\n"
      "v_mfma_f32_16x16x32_bf16 v[248:251], v[44:47], v[88:91], v[248:251]\n"
      "ds_read_b128 v[88:91], %[xa0] offset:28672\n"
      "v_mfma_f32_16x16x32_bf16 v[252:255], v[44:47], v[92:95], v[252:255]\n"
      "ds_read_b128 v[120:123], %[xa1] offset:28672\n"
      "v_mfma_f32_16x16x32_bf16 v[252:255], v[60:63], v[124:127], v[252:255]\n"
      "v_mfma_f32_16x16x32_bf16 v[220:223], v[56:59], v[124:127], v[220:223]\n"
      "v_mfma_f32_16x16x32_bf16 v[220:223], v[40:43], v[92:95], v[220:223]\n"
      "ds_read_b128 v[92:95], %[xa0] offset:30720\n"
      "s_waitcnt lgkmcnt(7)\n"
      "v_mfma_f32_16x16x32_bf16 v[128:131], v[32:35], v[64:67], v[128:131]\n"
      "ds_read_b128 v[124:127], %[xa1] offset:30720\n"
      "v_mfma_f32_16x16x32_bf16 v[128:131], v[48:51], v[96:99], v[128:131]\n"
      "s_add_u32 m0, %[sWb], 8192\n"
      "v_mfma_f32_16x16x32_bf16 v[160:163], v[52:55], v[96:99], v[160:163]\n"
      "s_add_u32 %[sW], %[sW], %[d0]\n"
      "v_mfma_f32_16x16x32_bf16 v[160:163], v[36:39], v[64:67], v[160:163]\n"
      "ds_read_b64_tr_b16 v[40:41], %[wa2] offset:16384\n"
      "v_mfma_f32_16x16x32_bf16 v[164:167], v[36:39], v[68:71], v[164:167]\n"
      "buffer_load_dwordx4 %[wv0], %[srdW], %[sW] offen offset:0 lds\n"
      "v_mfma_f32_16x16x32_bf16 v[164:167], v[52:55], v[100:103], v[164:167]\n"
      "ds_read_b64_tr_b16 v[42:43], %[wa2] offset:16896\n"
      "v_mfma_f32_16x16x32_bf16 v[132:135], v[48:51], v[100:103], v[132:135]\n"
      "ds_read_b64_tr_b16 v[56:57], %[wa2] offset:20480\n"
      "v_mfma_f32_16x16x32_bf16 v[132:135], v[32:35], v[68:71], v[132:135]\n"
      "buffer_load_dwordx4 %[wv1], %[srdW], %[sW] offen offset:1024 lds\n"
      "v_mfma_f32_16x16x32_bf16 v[136:139], v[32:35], v[72:75], v[136:139]\n"
      "ds_read_b64_tr_b16 v[58:59], %[wa2] offset:20992\n"
      "v_mfma_f32_16x16x32_bf16 v[136:139], v[48:51], v[104:107], v[136:139]\n"
      "ds_read_b64_tr_b16 v[44:45], %[wa3] offset:16384\n"
      "v_mfma_f32_16x16x32_bf16 v[168:171], v[52:55], v[104:107], v[168:171]\n"
      "buffer_load_dwordx4 %[wv2], %[srdW], %[sW] offen offset:2048 lds\n"
      "v_mfma_f32_16x16x32_bf16 v[168:171], v[36:39], v[72:75], v[168:171]\n"
      "ds_read_b64_tr_b16 v[46:47], %[wa3] offset:16896\n"
      "v_mfma_f32_16x16x32_bf16 v[172:175], v[36:39], v[76:79], v[172:175]\n"
      "ds_read_b64_tr_b16 v[60:61], %[wa3] offset:20480\n"
      "v_mfma_f32_16x16x32_bf16 v[172:175], v[52:55], v[108:111], v[172:175]\n"
      "buffer_load_dwordx4 %[wv3], %[srdW], %[sW] offen offset:3072 lds\n"
      "v_mfma_f32_16x16x32_bf16 v[140:143], v[48:51], v[108:111], v[140:143]\n"
      "ds_read_b64_tr_b16 v[62:63], %[wa3] offset:20992\n"
      "v_mfma_f32_16x16x32_bf16 v[140:143], v[32:35], v[76:79], v[140:143]\n"
      "s_add_u32 m0, %[sWb], 12288\n"
      "s_waitcnt lgkmcnt(8)\n"
      "v_mfma_f32_16x16x32_bf16 v[144:147], v[32:35], v[80:83], v[144:147]\n"
      "s_add_u32 %[sW], %[sW], 0x8000\n"
      "v_mfma_f32_16x16x32_bf16 v[144:147], v[48:51], v[112:115], v[144:147]\n"
      "v_mfma_f32_16x16x32_bf16 v[176:179], v[52:55], v[112:115], v[176:179]\n"
      "buffer_load_dwordx4 %[wv0], %[srdW], %[sW] offen offset:0 lds\n"
      "v_mfma_f32_16x16x32_bf16 v[176:179], v[36:39], v[80:83], v[176:179]\n"
      "v_mfma_f32_16x16x32_bf16 v[180:183], v[36:39], v[84:87], v[180:183]\n"
      "v_mfma_f32_16x16x32_bf16 v[180:183], v[52:55], v[116:119], v[180:183]\n"
      "buffer_load_dwordx4 %[wv1], %[srdW], %[sW] offen offset:1024 lds\n"
      "v_mfma_f32_16x16x32_bf16 v[148:151], v[48:51], v[116:119], v[148:151]\n"
      "v_mfma_f32_16x16x32_bf16 v[148:151], v[32:35], v[84:87], v[148:151]\n"
      "v_mfma_f32_16x16x32_bf16 v[152:155], v[32:35], v[88:91], v[152:155]\n"
      "buffer_load_dwordx4 %[wv2], %[srdW], %[sW] offen offset:2048 lds\n"
      "v_mfma_f32_16x16x32_bf16 v[152:155], v[48:51], v[120:123], v[152:155]\n"
      "v_mfma_f32_16x16x32_bf16 v[184:187], v[52:55], v[120:123], v[184:187]\n"
      "v_mfma_f32_16x16x32_bf16 v[184:187], v[36:39], v[88:91], v[184:187]\n"
      "buffer_load_dwordx4 %[wv3], %[srdW], %[sW] offen offset:3072 lds\n"
      "v_mfma_f32_16x16x32_bf16 v[188:191], v[36:39], v[92:95], v[188:191]\n"
      "v_mfma_f32_16x16x32_bf16 v[188:191], v[52:55], v[124:127], v[188:191]\n"
      "v_mfma_f32_16x16x32_bf16 v[156:159], v[48:51], v[124:127], v[156:159]\n"
      "v_mfma_f32_16x16x32_bf16 v[156:159], v[32:35], v[92:95], v[156:159]\n"
      "s_waitcnt lgkmcnt(0)\n"
      "v_mfma_f32_16x16x32_bf16 v[192:195], v[40:43], v[64:67], v[192:195]\n"
      "s_waitcnt vmcnt(12)\n"
      "v_mfma_f32_16x16x32_bf16 v[192:195], v[56:59], v[96:99], v[192:195]\n"
      "s_barrier\n"
      "v_mfma_f32_16x16x32_bf16 v[224:227], v[60:63], v[96:99], v[224:227]\n"
      "ds_read_b64_tr_b16 v[32:33], %[wa0] offset:0\n"
      "v_mfma_f32_16x16x32_bf16 v[224:227], v[44:47], v[64:67], v[224:227]\n"
      "ds_read_b128 v[64:67], %[xa0] offset:32768\n"
      "v_mfma_f32_16x16x32_bf16 v[228:231], v[44:47], v[68:71], v[228:231]\n"
      "ds_read_b128 v[96:99], %[xa1] offset:32768\n"
      "v_mfma_f32_16x16x32_bf16 v[228:231], v[60:63], v[100:103], v[228:231]\n"
      "ds_read_b64_tr_b16 v[34:35], %[wa0] offset:512\n"
      "v_mfma_f32_16x16x32_bf16 v[196:199], v[56:59], v[100:103], v[196:199]\n"
      "ds_read_b64_tr_b16 v[36:37], %[wa1] offset:0\n"
      "v_mfma_f32_16x16x32_bf16 v[196:199], v[40:43], v[68:71], v[196:199]\n"
      "ds_read_b128 v[68:71], %[xa0] offset:34816\n"
      "v_mfma_f32_16x16x32_bf16 v[200:203], v[40:43], v[72:75], v[200:203]\n"
      "ds_read_b128 v[100:103], %[xa1] offset:34816\n"
      "v_mfma_f32_16x16x32_bf16 v[200:203], v[56:59], v[104:107], v[200:203]\n"
      "ds_read_b64_tr_b16 v[38:39], %[wa1] offset:512\n"
      "v_mfma_f32_16x16x32_bf16 v[232:235], v[60:63], v[104:107], v[232:235]\n"
      "s_waitcnt vmcnt(8)\n"
      "v_mfma_f32_16x16x32_bf16 v[232:235], v[44:47], v[72:75], v[232:235]\n"
      "ds_read_b128 v[72:75], %[xa0] offset:36864\n"
      "v_mfma_f32_16x16x32_bf16 v[236:239], v[44:47], v[76:79], v[236:239]\n"
      "ds_read_b128 v[104:107], %[xa1] offset:36864\n"
      "v_mfma_f32_16x16x32_bf16 v[236:239], v[60:63], v[108:111], v[236:239]\n"
      "ds_read_b64_tr_b16 v[48:49], %[wa0] offset:4096\n"
      "v_mfma_f32_16x16x32_bf16 v[204:207], v[56:59], v[108:111], v[204:207]\n"
      "ds_read_b64_tr_b16 v[50:51], %[wa0] offset:4608\n"
      "v_mfma_f32_16x16x32_bf16 v[204:207], v[40:43], v[76:79], v[204:207]\n"
      "ds_read_b128 v[76:79], %[xa0] offset:38912\n"
      "v_mfma_f32_16x16x32_bf16 v[208:211], v[40:43], v[80:83], v[208:211]\n"
      "ds_read_b128 v[108:111], %[xa1] offset:38912\n"
      "v_mfma_f32_16x16x32_bf16 v[208:211], v[56:59], v[112:115], v[208:211]\n"
      "ds_read_b64_tr_b16 v[52:53], %[wa1] offset:4096\n"
      "v_mfma_f32_16x16x32_bf16 v[240:243], v[60:63], v[112:115], v[240:243]\n"
      "ds_read_b64_tr_b16 v[54:55], %[wa1] offset:4608\n"
      "v_mfma_f32_16x16x32_bf16 v[240:243], v[44:47], v[80:83], v[240:243]\n"
      "ds_read_b128 v[80:83], %[xa0] offset:40960\n"
      "v_mfma_f32_16x16x32_bf16 v[244:247], v[44:47], v[84:87], v[244:247]\n"
      "ds_read_b128 v[112:115], %[xa1] offset:40960\n"
      "v_mfma_f32_16x16x32_bf16 v[244:247], v[60:63], v[116:119], v[244:247]\n"
      "v_mfma_f32_16x16x32_bf16 v[212:215], v[56:59], v[116:119], v[212:215]\n"
      "v_mfma_f32_16x16x32_bf16 v[212:215], v[40:43], v[84:87], v[212:215]\n"
      "ds_read_b128 v[84:87], %[xa0] offset:43008\n"
      "v_mfma_f32_16x16x32_bf16 v[216:219], v[40:43], v[88:91], v[216:219]\n"
      "ds_read_b128 v[116:119], %[xa1] offset:43008\n"
      "v_mfma_f32_16x16x32_bf16 v[216:219], v[56:59], v[120:123], v[216:219]\n"
      "v_mfma_f32_16x16x32_bf16 v[248:251], v[60:63], v[120:123], v[248:251]\n"
      "v_mfma_f32_16x16x32_bf16 v[248:251], v[44:47], v[88:91], v[248:251]\n"
      "ds_read_b128 v[88:91], %[xa0] offset:45056\n"
      "v_mfma_f32_16x16x32_bf16 v[252:255], v[44:47], v[92:95], v[252:255]\n"
      "ds_read_b128 v[120:123], %[xa1] offset:45056\n"
      "v_mfma_f32_16x16x32_bf16 v[252:255], v[60:63], v[124:127], v[252:255]\n"
      "v_mfma_f32_16x16x32_bf16 v[220:223], v[56:59], v[124:127], v[220:223]\n"
      "v_mfma_f32_16x16x32_bf16 v[220:223], v[40:43], v[92:95], v[220:223]\n"
      "ds_read_b128 v[92:95], %[xa0] offset:47104\n"
      "s_waitcnt lgkmcnt(7)\n"
      "v_mfma_f32_16x16x32_bf16 v[128:131], v[32:35], v[64:67], v[128:131]\n"
      "ds_read_b128 v[124:127], %[xa1] offset:47104\n"
      "v_mfma_f32_16x16x32_bf16 v[128:131], v[48:51], v[96:99], v[128:131]\n"
      "s_add_u32 m0, %[sWb], 16384\n"
      "v_mfma_f32_16x16x32_bf16 v[160:163], v[52:55], v[96:99], v[160:163]\n"
      "s_add_u32 %[sW], %[sW], %[d1]\n"
      "v_mfma_f32_16x16x32_bf16 v[160:163], v[36:39], v[64:67], v[160:163]\n"
      "ds_read_b64_tr_b16 v[40:41], %[wa2] offset:0\n"
      "v_mfma_f32_16x16x32_bf16 v[164:167], v[36:39], v[68:71], v[164:167]\n"
      "buffer_load_dwordx4 %[wv0], %[srdW], %[sW] offen offset:0 lds\n"
      "v_mfma_f32_16x16x32_bf16 v[164:167], v[52:55], v[100:103], v[164:167]\n"
      "ds_read_b64_tr_b16 v[42:43], %[wa2] offset:512\n"
      "v_mfma_f32_16x16x32_bf16 v[132:135], v[48:51], v[100:103], v[132:135]\n"
      "ds_read_b64_tr_b16 v[56:57], %[wa2] offset:4096\n"
      "v_mfma_f32_16x16x32_bf16 v[132:135], v[32:35], v[68:71], v[132:135]\n"
      "buffer_load_dwordx4 %[wv1], %[srdW], %[sW] offen offset:1024 lds\n"
      "v_mfma_f32_16x16x32_bf16 v[136:139], v[32:35], v[72:75], v[136:139]\n"
      "ds_read_b64_tr_b16 v[58:59], %[wa2] offset:4608\n"
      "v_mfma_f32_16x16x32_bf16 v[136:139], v[48:51], v[104:107], v[136:139]\n"
      "ds_read_b64_tr_b16 v[44:45], %[wa3] offset:0\n"
      "v_mfma_f32_16x16x32_bf16 v[168:171], v[52:55], v[104:107], v[168:171]\n"
      "buffer_load_dwordx4 %[wv2], %[srdW], %[sW] offen offset:2048 lds\n"
      "v_mfma_f32_16x16x32_bf16 v[168:171], v[36:39], v[72:75], v[168:171]\n"
      "ds_read_b64_tr_b16 v[46:47], %[wa3] offset:512\n"
      "v_mfma_f32_16x16x32_bf16 v[172:175], v[36:39], v[76:79], v[172:175]\n"
      "ds_read_b64_tr_b16 v[60:61], %[wa3] offset:4096\n"
      "v_mfma_f32_16x16x32_bf16 v[172:175], v[52:55], v[108:111], v[172:175]\n"
      "buffer_load_dwordx4 %[wv3], %[srdW], %[sW] offen offset:3072 lds\n"
      "v_mfma_f32_16x16x32_bf16 v[140:143], v[48:51], v[108:111], v[140:143]\n"
      "ds_read_b64_tr_b16 v[62:63], %[wa3] offset:4608\n"
      "v_mfma_f32_16x16x32_bf16 v[140:143], v[32:35], v[76:79], v[140:143]\n"
      "s_add_u32 m0, %[sWb], 20480\n"
      "s_waitcnt lgkmcnt(8)\n"
      "v_mfma_f32_16x16x32_bf16 v[144:147], v[32:35], v[80:83], v[144:147]\n"
      "s_add_u32 %[sW], %[sW], 0x8000\n"
      "v_mfma_f32_16x16x32_bf16 v[144:147], v[48:51], v[112:115], v[144:147]\n"
      "v_mfma_f32_16x16x32_bf16 v[176:179], v[52:55], v[112:115], v[176:179]\n"
      "buffer_load_dwordx4 %[wv0], %[srdW], %[sW] offen offset:0 lds\n"
      "v_mfma_f32_16x16x32_bf16 v[176:179], v[36:39], v[80:83], v[176:179]\n"
      "v_mfma_f32_16x16x32_bf16 v[180:183], v[36:39], v[84:87], v[180:183]\n"
      "v_mfma_f32_16x16x32_bf16 v[180:183], v[52:55], v[116:119], v[180:183]\n"
      "buffer_load_dwordx4 %[wv1], %[srdW], %[sW] offen offset:1024 lds\n"
      "v_mfma_f32_16x16x32_bf16 v[148:151], v[48:51], v[116:119], v[148:151]\n"
      "v_mfma_f32_16x16x32_bf16 v[148:151], v[32:35], v[84:87], v[148:151]\n"
      "v_mfma_f32_16x16x32_bf16 v[152:155], v[32:35], v[88:91], v[152:155]\n"
      "buffer_load_dwordx4 %[wv2], %[srdW], %[sW] offen offset:2048 lds\n"
      "v_mfma_f32_16x16x32_bf16 v[152:155], v[48:51], v[120:123], v[152:155]\n"
      "v_mfma_f32_16x16x32_bf16 v[184:187], v[52:55], v[120:123], v[184:187]\n"
      "v_mfma_f32_16x16x32_bf16 v[184:187], v[36:39], v[88:91], v[184:187]\n"
      "buffer_load_dwordx4 %[wv3], %[srdW], %[sW] offen offset:3072 lds\n"
      "v_mfma_f32_16x16x32_bf16 v[188:191], v[36:39], v[92:95], v[188:191]\n"
      "v_mfma_f32_16x16x32_bf16 v[188:191], v[52:55], v[124:127], v[188:191]\n"
      "v_mfma_f32_16x16x32_bf16 v[156:159], v[48:51], v[124:127], v[156:159]\n"
      "v_mfma_f32_16x16x32_bf16 v[156:159], v[32:35], v[92:95], v[156:159]\n"
      "s_waitcnt lgkmcnt(0)\n"
      "v_mfma_f32_16x16x32_bf16 v[192:195], v[40:43], v[64:67], v[192:195]\n"
      "s_waitcnt vmcnt(12)\n"
      "v_mfma_f32_16x16x32_bf16 v[192:195], v[56:59], v[96:99], v[192:195]\n"
      "s_barrier\n"
      "v_mfma_f32_16x16x32_bf16 v[224:227], v[60:63], v[96:99], v[224:227]\n"
      "ds_read_b64_tr_b16 v[32:33], %[wa0] offset:8192\n"
      "v_mfma_f32_16x16x32_bf16 v[224:227], v[44:47], v[64:67], v[224:227]\n"
      "ds_read_b128 v[64:67], %[xa0] offset:49152\n"
      "v_mfma_f32_16x16x32_bf16 v[228:231], v[44:47], v[68:71], v[228:231]\n"
      "ds_read_b128 v[96:99], %[xa1] offset:49152\n"
      "v_mfma_f32_16x16x32_bf16 v[228:231], v[60:63], v[100:103], v[228:231]\n"
      "ds_read_b64_tr_b16 v[34:35], %[wa0] offset:8704\n"
      "v_mfma_f32_16x16x32_bf16 v[196:199], v[56:59], v[100:103], v[196:199]\n"
      "ds_read_b64_tr_b16 v[36:37], %[wa1] offset:8192\n"
      "v_mfma_f32_16x16x32_bf16 v[196:199], v[40:43], v[68:71], v[196:199]\n"
      "ds_read_b128 v[68:71], %[xa0] offset:51200\n"
      "v_mfma_f32_16x16x32_bf16 v[200:203], v[40:43], v[72:75], v[200:203]\n"
      "ds_read_b128 v[100:103], %[xa1] offset:51200\n"
      "v_mfma_f32_16x16x32_bf16 v[200:203], v[56:59], v[104:107], v[200:203]\n"
      "ds_read_b64_tr_b16 v[38:39], %[wa1] offset:8704\n"
      "v_mfma_f32_16x16x32_bf16 v[232:235], v[60:63], v[104:107], v[232:235]\n"
      "s_waitcnt vmcnt(8)\n"
      "v_mfma_f32_16x16x32_bf16 v[232:235], v[44:47], v[72:75], v[232:235]\n"
      "ds_read_b128 v[72:75], %[xa0] offset:53248\n"
      "v_mfma_f32_16x16x32_bf16 v[236:239], v[44:47], v[76:79], v[236:239]\n"
      "ds_read_b128 v[104:107], %[xa1] offset:53248\n"
      "v_mfma_f32_16x16x32_bf16 v[236:239], v[60:63], v[108:111], v[236:239]\n"
      "ds_read_b64_tr_b16 v[48:49], %[wa0] offset:12288\n"
      "v_mfma_f32_16x16x32_bf16 v[204:207], v[56:59], v[108:111], v[204:207]\n"
      "ds_read_b64_tr_b16 v[50:51], %[wa0] offset:12800\n"
      "v_mfma_f32_16x16x32_bf16 v[204:207], v[40:43], v[76:79], v[204:207]\n"
      "ds_read_b128 v[76:79], %[xa0] offset:55296\n"
      "v_mfma_f32_16x16x32_bf16 v[208:211], v[40:43], v[80:83], v[208:211]\n"
      "ds_read_b128 v[108:111], %[xa1] offset:55296\n"
      "v_mfma_f32_16x16x32_bf16 v[208:211], v[56:59], v[112:115], v[208:211]\n"
      "ds_read_b64_tr_b16 v[52:53], %[wa1] offset:12288\n"
      "v_mfma_f32_16x16x32_bf16 v[240:243], v[60:63], v[112:115], v[240:243]\n"
      "ds_read_b64_tr_b16 v[54:55], %[wa1] offset:12800\n"
      "v_mfma_f32_16x16x32_bf16 v[240:243], v[44:47], v[80:83], v[240:243]\n"
      "ds_read_b128 v[80:83], %[xa0] offset:57344\n"
      "v_mfma_f32_16x16x32_bf16 v[244:247], v[44:47], v[84:87], v[244:247]\n"
      "ds_read_b128 v[112:115], %[xa1] offset:57344\n"
      "v_mfma_f32_16x16x32_bf16 v[244:247], v[60:63], v[116:119], v[244:247]\n"
      "v_mfma_f32_16x16x32_bf16 v[212:215], v[56:59], v[116:119], v[212:215]\n"
      "v_mfma_f32_16x16x32_bf16 v[212:215], v[40:43], v[84:87], v[212:215]\n"
      "ds_read_b128 v[84:87], %[xa0] offset:59392\n"
      "v_mfma_f32_16x16x32_bf16 v[216:219], v[40:43], v[88:91], v[216:219]\n"
      "ds_read_b128 v[116:119], %[xa1] offset:59392\n"
      "v_mfma_f32_16x16x32_bf16 v[216:219], v[56:59], v[120:123], v[216:219]\n"
      "v_mfma_f32_16x16x32_bf16 v[248:251], v[60:63], v[120:123], v[248:251]\n"
      "v_mfma_f32_16x16x32_bf16 v[248:251], v[44:47], v[88:91], v[248:251]\n"
      "ds_read_b128 v[88:91], %[xa0] offset:61440\n"
      "v_mfma_f32_16x16x32_bf16 v[252:255], v[44:47], v[92:95], v[252:255]\n"
      "ds_read_b128 v[120:123], %[xa1] offset:61440\n"
      "v_mfma_f32_16x16x32_bf16 v[252:255], v[60:63], v[124:127], v[252:255]\n"
      "v_mfma_f32_16x16x32_bf16 v[220:223], v[56:59], v[124:127], v[220:223]\n"
      "v_mfma_f32_16x16x32_bf16 v[220:223], v[40:43], v[92:95], v[220:223]\n"
      "ds_read_b128 v[92:95], %[xa0] offset:63488\n"
      "s_waitcnt lgkmcnt(7)\n"
      "v_mfma_f32_16x16x32_bf16 v[128:131], v[32:35], v[64:67], v[128:131]\n"
      "ds_read_b128 v[124:127], %[xa1] offset:63488\n"
      "v_mfma_f32_16x16x32_bf16 v[128:131], v[48:51], v[96:99], v[128:131]\n"
      "s_add_u32 m0, %[sWb], 0\n"
      "v_mfma_f32_16x16x32_bf16 v[160:163], v[52:55], v[96:99], v[160:163]\n"
      "s_add_u32 %[sW], %[sW], %[d0]\n"
      "v_mfma_f32_16x16x32_bf16 v[160:163], v[36:39], v[64:67], v[160:163]\n"
      "ds_read_b64_tr_b16 v[40:41], %[wa2] offset:8192\n"
      "v_mfma_f32_16x16x32_bf16 v[164:167], v[36:39], v[68:71], v[164:167]\n"
      "buffer_load_dwordx4 %[wv0], %[srdW], %[sW] offen offset:0 lds\n"
      "v_mfma_f32_16x16x32_bf16 v[164:167], v[52:55], v[100:103], v[164:167]\n"
      "ds_read_b64_tr_b16 v[42:43], %[wa2] offset:8704\n"
      "v_mfma_f32_16x16x32_bf16 v[132:135], v[48:51], v[100:103], v[132:135]\n"
      "ds_read_b64_tr_b16 v[56:57], %[wa2] offset:12288\n"
      "v_mfma_f32_16x16x32_bf16 v[132:135], v[32:35], v[68:71], v[132:135]\n"
      "buffer_load_dwordx4 %[wv1], %[srdW], %[sW] offen offset:1024 lds\n"
      "v_mfma_f32_16x16x32_bf16 v[136:139], v[32:35], v[72:75], v[136:139]\n"
      "ds_read_b64_tr_b16 v[58:59], %[wa2] offset:12800\n"
      "v_mfma_f32_16x16x32_bf16 v[136:139], v[48:51], v[104:107], v[136:139]\n"
      "ds_read_b64_tr_b16 v[44:45], %[wa3] offset:8192\n"
      "v_mfma_f32_16x16x32_bf16 v[168:171], v[52:55], v[104:107], v[168:171]\n"
      "buffer_load_dwordx4 %[wv2], %[srdW], %[sW] offen offset:2048 lds\n"
      "v_mfma_f32_16x16x32_bf16 v[168:171], v[36:39], v[72:75], v[168:171]\n"
      "ds_read_b64_tr_b16 v[46:47], %[wa3] offset:8704\n"
      "v_mfma_f32_16x16x32_bf16 v[172:175], v[36:39], v[76:79], v[172:175]\n"
      "ds_read_b64_tr_b16 v[60:61], %[wa3] offset:12288\n"
      "v_mfma_f32_16x16x32_bf16 v[172:175], v[52:55], v[108:111], v[172:175]\n"
      "buffer_load_dwordx4 %[wv3], %[srdW], %[sW] offen offset:3072 lds\n"
      "v_mfma_f32_16x16x32_bf16 v[140:143], v[48:51], v[108:111], v[140:143]\n"
      "ds_read_b64_tr_b16 v[62:63], %[wa3] offset:12800\n"
      "v_mfma_f32_16x16x32_bf16 v[140:143], v[32:35], v[76:79], v[140:143]\n"
      "s_add_u32 m0, %[sWb], 4096\n"
      "s_waitcnt lgkmcnt(8)\n"
      "v_mfma_f32_16x16x32_bf16 v[144:147], v[32:35], v[80:83], v[144:147]\n"
      "s_add_u32 %[sW], %[sW], 0x8000\n"
      "v_mfma_f32_16x16x32_bf16 v[144:147], v[48:51], v[112:115], v[144:147]\n"
      "v_mfma_f32_16x16x32_bf16 v[176:179], v[52:55], v[112:115], v[176:179]\n"
      "buffer_load_dwordx4 %[wv0], %[srdW], %[sW] offen offset:0 lds\n"
      "v_mfma_f32_16x16x32_bf16 v[176:179], v[36:39], v[80:83], v[176:179]\n"
      "v_mfma_f32_16x16x32_bf16 v[180:183], v[36:39], v[84:87], v[180:183]\n"
      "v_mfma_f32_16x16x32_bf16 v[180:183], v[52:55], v[116:119], v[180:183]\n"
      "buffer_load_dwordx4 %[wv1], %[srdW], %[sW] offen offset:1024 lds\n"
      "v_mfma_f32_16x16x32_bf16 v[148:151], v[48:51], v[116:119], v[148:151]\n"
      "v_mfma_f32_16x16x32_bf16 v[148:151], v[32:35], v[84:87], v[148:151]\n"
      "v_mfma_f32_16x16x32_bf16 v[152:155], v[32:35], v[88:91], v[152:155]\n"
      "buffer_load_dwordx4 %[wv2], %[srdW], %[sW] offen offset:2048 lds\n"
      "v_mfma_f32_16x16x32_bf16 v[152:155], v[48:51], v[120:123], v[152:155]\n"
      "v_mfma_f32_16x16x32_bf16 v[184:187], v[52:55], v[120:123], v[184:187]\n"
      "v_mfma_f32_16x16x32_bf16 v[184:187], v[36:39], v[88:91], v[184:187]\n"
      "buffer_load_dwordx4 %[wv3], %[srdW], %[sW] offen offset:3072 lds\n"
      "v_mfma_f32_16x16x32_bf16 v[188:191], v[36:39], v[92:95], v[188:191]\n"
      "v_mfma_f32_16x16x32_bf16 v[188:191], v[52:55], v[124:127], v[188:191]\n"
      "v_mfma_f32_16x16x32_bf16 v[156:159], v[48:51], v[124:127], v[156:159]\n"
      "v_mfma_f32_16x16x32_bf16 v[156:159], v[32:35], v[92:95], v[156:159]\n"
      "s_waitcnt lgkmcnt(0)\n"
      "v_mfma_f32_16x16x32_bf16 v[192:195], v[40:43], v[64:67], v[192:195]\n"
      "s_waitcnt vmcnt(12)\n"
      "v_mfma_f32_16x16x32_bf16 v[192:195], v[56:59], v[96:99], v[192:195]\n"
      "s_barrier\n"
      "v_mfma_f32_16x16x32_bf16 v[224:227], v[60:63], v[96:99], v[224:227]\n"
      "ds_read_b64_tr_b16 v[32:33], %[wa0] offset:16384\n"
      "v_mfma_f32_16x16x32_bf16 v[224:227], v[44:47], v[64:67], v[224:227]\n"
      "ds_read_b128 v[64:67], %[xa0] offset:0\n"
      "v_mfma_f32_16x16x32_bf16 v[228:231], v[44:47], v[68:71], v[228:231]\n"
      "ds_read_b128 v[96:99], %[xa1] offset:0\n"
      "v_mfma_f32_16x16x32_bf16 v[228:231], v[60:63], v[100:103], v[228:231]\n"
      "ds_read_b64_tr_b16 v[34:35], %[wa0] offset:16896\n"
      "v_mfma_f32_16x16x32_bf16 v[196:199], v[56:59], v[100:103], v[196:199]\n"
      "ds_read_b64_tr_b16 v[36:37], %[wa1] offset:16384\n"
      "v_mfma_f32_16x16x32_bf16 v[196:199], v[40:43], v[68:71], v[196:199]\n"
      "ds_read_b128 v[68:71], %[xa0] offset:2048\n"
      "v_mfma_f32_16x16x32_bf16 v[200:203], v[40:43], v[72:75], v[200:203]\n"
      "ds_read_b128 v[100:103], %[xa1] offset:2048\n"
      "v_mfma_f32_16x16x32_bf16 v[200:203], v[56:59], v[104:107], v[200:203]\n"
      "ds_read_b64_tr_b16 v[38:39], %[wa1] offset:16896\n"
      "v_mfma_f32_16x16x32_bf16 v[232:235], v[60:63], v[104:107], v[232:235]\n"
      "s_waitcnt vmcnt(8)\n"
      "v_mfma_f32_16x16x32_bf16 v[232:235], v[44:47], v[72:75], v[232:235]\n"
      "ds_read_b128 v[72:75], %[xa0] offset:4096\n"
      "v_mfma_f32_16x16x32_bf16 v[236:239], v[44:47], v[76:79], v[236:239]\n"
      "ds_read_b128 v[104:107], %[xa1] offset:4096\n"
      "v_mfma_f32_16x16x32_bf16 v[236:239], v[60:63], v[108:111], v[236:239]\n"
      "ds_read_b64_tr_b16 v[48:49], %[wa0] offset:20480\n"
      "v_mfma_f32_16x16x32_bf16 v[204:207], v[56:59], v[108:111], v[204:207]\n"
      "ds_read_b64_tr_b16 v[50:51], %[wa0] offset:20992\n"
      "v_mfma_f32_16x16x32_bf16 v[204:207], v[40:43], v[76:79], v[204:207]\n"
      "ds_read_b128 v[76:79], %[xa0] offset:6144\n"
      "v_mfma_f32_16x16x32_bf16 v[208:211], v[40:43], v[80:83], v[208:211]\n"
      "ds_read_b128 v[108:111], %[xa1] offset:6144\n"
      "v_mfma_f32_16x16x32_bf16 v[208:211], v[56:59], v[112:115], v[208:211]\n"
      "ds_read_b64_tr_b16 v[52:53], %[wa1] offset:20480\n"
      "v_mfma_f32_16x16x32_bf16 v[240:243], v[60:63], v[112:115], v[240:243]\n"
      "ds_read_b64_tr_b16 v[54:55], %[wa1] offset:20992\n"
      "v_mfma_f32_16x16x32_bf16 v[240:243], v[44:47], v[80:83], v[240:243]\n"
      "ds_read_b128 v[80:83], %[xa0] offset:8192\n"
      "v_mfma_f32_16x16x32_bf16 v[244:247], v[44:47], v[84:87], v[244:247]\n"
      "ds_read_b128 v[112:115], %[xa1] offset:8192\n"
      "v_mfma_f32_16x16x32_bf16 v[244:247], v[60:63], v[116:119], v[244:247]\n"
      "v_mfma_f32_16x16x32_bf16 v[212:215], v[56:59], v[116:119], v[212:215]\n"
      "v_mfma_f32_16x16x32_bf16 v[212:215], v[40:43], v[84:87], v[212:215]\n"
      "ds_read_b128 v[84:87], %[xa0] offset:10240\n"
      "v_mfma_f32_16x16x32_bf16 v[216:219], v[40:43], v[88:91], v[216:219]\n"
      "ds_read_b128 v[116:119], %[xa1] offset:10240\n"
      "v_mfma_f32_16x16x32_bf16 v[216:219], v[56:59], v[120:123], v[216:219]\n"
      "v_mfma_f32_16x16x32_bf16 v[248:251], v[60:63], v[120:123], v[248:251]\n"
      "v_mfma_f32_16x16x32_bf16 v[248:251], v[44:47], v[88:91], v[248:251]\n"
      "ds_read_b128 v[88:91], %[xa0] offset:12288\n"
      "v_mfma_f32_16x16x32_bf16 v[252:255], v[44:47], v[92:95], v[252:255]\n"
      "ds_read_b128 v[120:123], %[xa1] offset:12288\n"
      "v_mfma_f32_16x16x32_bf16 v[252:255], v[60:63], v[124:127], v[252:255]\n"
      "v_mfma_f32_16x16x32_bf16 v[220:223], v[56:59], v[124:127], v[220:223]\n"
      "v_mfma_f32_16x16x32_bf16 v[220:223], v[40:43], v[92:95], v[220:223]\n"
      "ds_read_b128 v[92:95], %[xa0] offset:14336\n"
      "s_waitcnt lgkmcnt(7)\n"
      "v_mfma_f32_16x16x32_bf16 v[128:131], v[32:35], v[64:67], v[128:131]\n"
      "ds_read_b128 v[124:127], %[xa1] offset:14336\n"
      "v_mfma_f32_16x16x32_bf16 v[128:131], v[48:51], v[96:99], v[128:131]\n"
      "s_add_u32 m0, %[sWb], 8192\n"
      "v_mfma_f32_16x16x32_bf16 v[160:163], v[52:55], v[96:99], v[160:163]\n"
      "s_add_u32 %[sW], %[sW], %[d1]\n"
      "v_mfma_f32_16x16x32_bf16 v[160:163], v[36:39], v[64:67], v[160:163]\n"
      "ds_read_b64_tr_b16 v[40:41], %[wa2] offset:16384\n"
      "v_mfma_f32_16x16x32_bf16 v[164:167], v[36:39], v[68:71], v[164:167]\n"
      "buffer_load_dwordx4 %[wv0], %[srdW], %[sW] offen offset:0 lds\n"
      "v_mfma_f32_16x16x32_bf16 v[164:167], v[52:55], v[100:103], v[164:167]\n"
      "ds_read_b64_tr_b16 v[42:43], %[wa2] offset:16896\n"
      "v_mfma_f32_16x16x32_bf16 v[132:135], v[48:51], v[100:103], v[132:135]\n"
      "ds_read_b64_tr_b16 v[56:57], %[wa2] offset:20480\n"
      "v_mfma_f32_16x16x32_bf16 v[132:135], v[32:35], v[68:71], v[132:135]\n"
      "buffer_load_dwordx4 %[wv1], %[srdW], %[sW] offen offset:1024 lds\n"
      "v_mfma_f32_16x16x32_bf16 v[136:139], v[32:35], v[72:75], v[136:139]\n"
      "ds_read_b64_tr_b16 v[58:59], %[wa2] offset:20992\n"
      "v_mfma_f32_16x16x32_bf16 v[136:139], v[48:51], v[104:107], v[136:139]\n"
      "ds_read_b64_tr_b16 v[44:45], %[wa3] offset:16384\n"
      "v_mfma_f32_16x16x32_bf16 v[168:171], v[52:55], v[104:107], v[168:171]\n"
      "buffer_load_dwordx4 %[wv2], %[srdW], %[sW] offen offset:2048 lds\n"
      "v_mfma_f32_16x16x32_bf16 v[168:171], v[36:39], v[72:75], v[168:171]\n"
      "ds_read_b64_tr_b16 v[46:47], %[wa3] offset:16896\n"
      "v_mfma_f32_16x16x32_bf16 v[172:175], v[36:39], v[76:79], v[172:175]\n"
      "ds_read_b64_tr_b16 v[60:61], %[wa3] offset:20480\n"
      "v_mfma_f32_16x16x32_bf16 v[172:175], v[52:55], v[108:111], v[172:175]\n"
      "buffer_load_dwordx4 %[wv3], %[srdW], %[sW] offen offset:3072 lds\n"
      "v_mfma_f32_16x16x32_bf16 v[140:143], v[48:51], v[108:111], v[140:143]\n"
      "ds_read_b64_tr_b16 v[62:63], %[wa3] offset:20992\n"
      "v_mfma_f32_16x16x32_bf16 v[140:143], v[32:35], v[76:79], v[140:143]\n"
      "s_add_u32 m0, %[sWb], 12288\n"
      "s_waitcnt lgkmcnt(8)\n"
      "v_mfma_f32_16x16x32_bf16 v[144:147], v[32:35], v[80:83], v[144:147]\n"
      "s_add_u32 %[sW], %[sW], 0x8000\n"
      "v_mfma_f32_16x16x32_bf16 v[144:147], v[48:51], v[112:115], v[144:147]\n"
      "v_mfma_f32_16x16x32_bf16 v[176:179], v[52:55], v[112:115], v[176:179]\n"
      "buffer_load_dwordx4 %[wv0], %[srdW], %[sW] offen offset:0 lds\n"
      "v_mfma_f32_16x16x32_bf16 v[176:179], v[36:39], v[80:83], v[176:179]\n"
      "v_mfma_f32_16x16x32_bf16 v[180:183], v[36:39], v[84:87], v[180:183]\n"
      "v_mfma_f32_16x16x32_bf16 v[180:183], v[52:55], v[116:119], v[180:183]\n"
      "buffer_load_dwordx4 %[wv1], %[srdW], %[sW] offen offset:1024 lds\n"
      "v_mfma_f32_16x16x32_bf16 v[148:151], v[48:51], v[116:119], v[148:151]\n"
      "v_mfma_f32_16x16x32_bf16 v[148:151], v[32:35], v[84:87], v[148:151]\n"
      "v_mfma_f32_16x16x32_bf16 v[152:155], v[32:35], v[88:91], v[152:155]\n"
      "buffer_load_dwordx4 %[wv2], %[srdW], %[sW] offen offset:2048 lds\n"
      "v_mfma_f32_16x16x32_bf16 v[152:155], v[48:51], v[120:123], v[152:155]\n"
      "v_mfma_f32_16x16x32_bf16 v[184:187], v[52:55], v[120:123], v[184:187]\n"
      "v_mfma_f32_16x16x32_bf16 v[184:187], v[36:39], v[88:91], v[184:187]\n"
      "buffer_load_dwordx4 %[wv3], %[srdW], %[sW] offen offset:3072 lds\n"
      "v_mfma_f32_16x16x32_bf16 v[188:191], v[36:39], v[92:95], v[188:191]\n"
      "v_mfma_f32_16x16x32_bf16 v[188:191], v[52:55], v[124:127], v[188:191]\n"
      "v_mfma_f32_16x16x32_bf16 v[156:159], v[48:51], v[124:127], v[156:159]\n"
      "v_mfma_f32_16x16x32_bf16 v[156:159], v[32:35], v[92:95], v[156:159]\n"
      "s_waitcnt lgkmcnt(0)\n"
      "v_mfma_f32_16x16x32_bf16 v[192:195], v[40:43], v[64:67], v[192:195]\n"
      "s_waitcnt vmcnt(12)\n"
      "v_mfma_f32_16x16x32_bf16 v[192:195], v[56:59], v[96:99], v[192:195]\n"
      "s_barrier\n"
      "v_mfma_f32_16x16x32_bf16 v[224:227], v[60:63], v[96:99], v[224:227]\n"
      "ds_read_b64_tr_b16 v[32:33], %[wa0] offset:0\n"
      "v_mfma_f32_16x16x32_bf16 v[224:227], v[44:47], v[64:67], v[224:227]\n"
      "ds_read_b128 v[64:67], %[xa0] offset:16384\n"
      "v_mfma_f32_16x16x32_bf16 v[228:231], v[44:47], v[68:71], v[228:231]\n"
      "ds_read_b128 v[96:99], %[xa1] offset:16384\n"
      "v_mfma_f32_16x16x32_bf16 v[228:231], v[60:63], v[100:103], v[228:231]\n"
      "ds_read_b64_tr_b16 v[34:35], %[wa0] offset:512\n"
      "v_mfma_f32_16x16x32_bf16 v[196:199], v[56:59], v[100:103], v[196:199]\n"
      "ds_read_b64_tr_b16 v[36:37], %[wa1] offset:0\n"
      "v_mfma_f32_16x16x32_bf16 v[196:199], v[40:43], v[68:71], v[196:199]\n"
      "ds_read_b128 v[68:71], %[xa0] offset:18432\n"
      "v_mfma_f32_16x16x32_bf16 v[200:203], v[40:43], v[72:75], v[200:203]\n"
      "ds_read_b128 v[100:103], %[xa1] offset:18432\n"
      "v_mfma_f32_16x16x32_bf16 v[200:203], v[56:59], v[104:107], v[200:203]\n"
      "ds_read_b64_tr_b16 v[38:39], %[wa1] offset:512\n"
      "v_mfma_f32_16x16x32_bf16 v[232:235], v[60:63], v[104:107], v[232:235]\n"
      "s_waitcnt vmcnt(8)\n"
      "v_mfma_f32_16x16x32_bf16 v[232:235], v[44:47], v[72:75], v[232:235]\n"
      "ds_read_b128 v[72:75], %[xa0] offset:20480\n"
      "v_mfma_f32_16x16x32_bf16 v[236:239], v[44:47], v[76:79], v[236:239]\n"
      "ds_read_b128 v[104:107], %[xa1] offset:20480\n"
      "v_mfma_f32_16x16x32_bf16 v[236:239], v[60:63], v[108:111], v[236:239]\n"
      "ds_read_b64_tr_b16 v[48:49], %[wa0] offset:4096\n"
      "v_mfma_f32_16x16x32_bf16 v[204:207], v[56:59], v[108:111], v[204:207]\n"
      "ds_read_b64_tr_b16 v[50:51], %[wa0] offset:4608\n"
      "v_mfma_f32_16x16x32_bf16 v[204:207], v[40:43], v[76:79], v[204:207]\n"
      "ds_read_b128 v[76:79], %[xa0] offset:22528\n"
      "v_mfma_f32_16x16x32_bf16 v[208:211], v[40:43], v[80:83], v[208:211]\n"
      "ds_read_b128 v[108:111], %[xa1] offset:22528\n"
      "v_mfma_f32_16x16x32_bf16 v[208:211], v[56:59], v[112:115], v[208:211]\n"
      "ds_read_b64_tr_b16 v[52:53], %[wa1] offset:4096\n"
      "v_mfma_f32_16x16x32_bf16 v[240:243], v[60:63], v[112:115], v[240:243]\n"
      "ds_read_b64_tr_b16 v[54:55], %[wa1] offset:4608\n"
      "v_mfma_f32_16x16x32_bf16 v[240:243], v[44:47], v[80:83], v[240:243]\n"
      "ds_read_b128 v[80:83], %[xa0] offset:24576\n"
      "v_mfma_f32_16x16x32_bf16 v[244:247], v[44:47], v[84:87], v[244:247]\n"
      "ds_read_b128 v[112:115], %[xa1] offset:24576\n"
      "v_mfma_f32_16x16x32_bf16 v[244:247], v[60:63], v[116:119], v[244:247]\n"
      "v_mfma_f32_16x16x32_bf16 v[212:215], v[56:59], v[116:119], v[212:215]\n"
      "v_mfma_f32_16x16x32_bf16 v[212:215], v[40:43], v[84:87], v[212:215]\n"
      "ds_read_b128 v[84:87], %[xa0] offset:26624\n"
      "v_mfma_f32_16x16x32_bf16 v[216:219], v[40:43], v[88:91], v[216:219]\n"
      "ds_read_b128 v[116:119], %[xa1] offset:26624\n"
      "v_mfma_f32_16x16x32_bf16 v[216:219], v[56:59], v[120:123], v[216:219]\n"
      "v_mfma_f32_16x16x32_bf16 v[248:251], v[60:63], v[120:123], v[248:251]\n"
      "v_mfma_f32_16x16x32_bf16 v[248:251], v[44:47], v[88:91], v[248:251]\n"
      "ds_read_b128 v[88:91], %[xa0] offset:28672\n"
      "v_mfma_f32_16x16x32_bf16 v[252:255], v[44:47], v[92:95], v[252:255]\n"
      "ds_read_b128 v[120:123], %[xa1] offset:28672\n"
      "v_mfma_f32_16x16x32_bf16 v[252:255], v[60:63], v[124:127], v[252:255]\n"
      "v_mfma_f32_16x16x32_bf16 v[220:223], v[56:59], v[124:127], v[220:223]\n"
      "v_mfma_f32_16x16x32_bf16 v[220:223], v[40:43], v[92:95], v[220:223]\n"
      "ds_read_b128 v[92:95], %[xa0] offset:30720\n"
      "s_waitcnt lgkmcnt(7)\n"
      "v_mfma_f32_16x16x32_bf16 v[128:131], v[32:35], v[64:67], v[128:131]\n"
      "ds_read_b128 v[124:127], %[xa1] offset:30720\n"
      "v_mfma_f32_16x16x32_bf16 v[128:131], v[48:51], v[96:99], v[128:131]\n"
      "s_add_u32 m0, %[sWb], 16384\n"
      "v_mfma_f32_16x16x32_bf16 v[160:163], v[52:55], v[96:99], v[160:163]\n"
      "s_add_u32 %[sW], %[sW], %[d0]\n"
      "v_mfma_f32_16x16x32_bf16 v[160:163], v[36:39], v[64:67], v[160:163]\n"
      "ds_read_b64_tr_b16 v[40:41], %[wa2] offset:0\n"
      "v_mfma_f32_16x16x32_bf16 v[164:167], v[36:39], v[68:71], v[164:167]\n"
      "buffer_load_dwordx4 %[wv0], %[srdW], %[sW] offen offset:0 lds\n"
      "v_mfma_f32_16x16x32_bf16 v[164:167], v[52:55], v[100:103], v[164:167]\n"
      "ds_read_b64_tr_b16 v[42:43], %[wa2] offset:512\n"
      "v_mfma_f32_16x16x32_bf16 v[132:135], v[48:51], v[100:103], v[132:135]\n"
      "ds_read_b64_tr_b16 v[56:57], %[wa2] offset:4096\n"
      "v_mfma_f32_16x16x32_bf16 v[132:135], v[32:35], v[68:71], v[132:135]\n"
      "buffer_load_dwordx4 %[wv1], %[srdW], %[sW] offen offset:1024 lds\n"
      "v_mfma_f32_16x16x32_bf16 v[136:139], v[32:35], v[72:75], v[136:139]\n"
      "ds_read_b64_tr_b16 v[58:59], %[wa2] offset:4608\n"
      "v_mfma_f32_16x16x32_bf16 v[136:139], v[48:51], v[104:107], v[136:139]\n"
      "ds_read_b64_tr_b16 v[44:45], %[wa3] offset:0\n"
      "v_mfma_f32_16x16x32_bf16 v[168:171], v[52:55], v[104:107], v[168:171]\n"
      "buffer_load_dwordx4 %[wv2], %[srdW], %[sW] offen offset:2048 lds\n"
      "v_mfma_f32_16x16x32_bf16 v[168:171], v[36:39], v[72:75], v[168:171]\n"
      "ds_read_b64_tr_b16 v[46:47], %[wa3] offset:512\n"
      "v_mfma_f32_16x16x32_bf16 v[172:175], v[36:39], v[76:79], v[172:175]\n"
      "ds_read_b64_tr_b16 v[60:61], %[wa3] offset:4096\n"
      "v_mfma_f32_16x16x32_bf16 v[172:175], v[52:55], v[108:111], v[172:175]\n"
      "buffer_load_dwordx4 %[wv3], %[srdW], %[sW] offen offset:3072 lds\n"
      "v_mfma_f32_16x16x32_bf16 v[140:143], v[48:51], v[108:111], v[140:143]\n"
      "ds_read_b64_tr_b16 v[62:63], %[wa3] offset:4608\n"
      "v_mfma_f32_16x16x32_bf16 v[140:143], v[32:35], v[76:79], v[140:143]\n"
      "s_add_u32 m0, %[sWb], 20480\n"
      "s_waitcnt lgkmcnt(8)\n"
      "v_mfma_f32_16x16x32_bf16 v[144:147], v[32:35], v[80:83], v[144:147]\n"
      "s_add_u32 %[sW], %[sW], 0x8000\n"
      "v_mfma_f32_16x16x32_bf16 v[144:147], v[48:51], v[112:115], v[144:147]\n"
      "v_mfma_f32_16x16x32_bf16 v[176:179], v[52:55], v[112:115], v[176:179]\n"
      "buffer_load_dwordx4 %[wv0], %[srdW], %[sW] offen offset:0 lds\n"
      "v_mfma_f32_16x16x32_bf16 v[176:179], v[36:39], v[80:83], v[176:179]\n"
      "v_mfma_f32_16x16x32_bf16 v[180:183], v[36:39], v[84:87], v[180:183]\n"
      "v_mfma_f32_16x16x32_bf16 v[180:183], v[52:55], v[116:119], v[180:183]\n"
      "buffer_load_dwordx4 %[wv1], %[srdW], %[sW] offen offset:1024 lds\n"
      "v_mfma_f32_16x16x32_bf16 v[148:151], v[48:51], v[116:119], v[148:151]\n"
      "v_mfma_f32_16x16x32_bf16 v[148:151], v[32:35], v[84:87], v[148:151]\n"
      "v_mfma_f32_16x16x32_bf16 v[152:155], v[32:35], v[88:91], v[152:155]\n"
      "buffer_load_dwordx4 %[wv2], %[srdW], %[sW] offen offset:2048 lds\n"
      "v_mfma_f32_16x16x32_bf16 v[152:155], v[48:51], v[120:123], v[152:155]\n"
      "v_mfma_f32_16x16x32_bf16 v[184:187], v[52:55], v[120:123], v[184:187]\n"
      "v_mfma_f32_16x16x32_bf16 v[184:187], v[36:39], v[88:91], v[184:187]\n"
      "buffer_load_dwordx4 %[wv3], %[srdW], %[sW] offen offset:3072 lds\n"
      "v_mfma_f32_16x16x32_bf16 v[188:191], v[36:39], v[92:95], v[188:191]\n"
      "v_mfma_f32_16x16x32_bf16 v[188:191], v[52:55], v[124:127], v[188:191]\n"
      "v_mfma_f32_16x16x32_bf16 v[156:159], v[48:51], v[124:127], v[156:159]\n"
      "v_mfma_f32_16x16x32_bf16 v[156:159], v[32:35], v[92:95], v[156:159]\n"
      "s_waitcnt lgkmcnt(0)\n"
      "v_mfma_f32_16x16x32_bf16 v[192:195], v[40:43], v[64:67], v[192:195]\n"
      "s_waitcnt vmcnt(12)\n"
      "v_mfma_f32_16x16x32_bf16 v[192:195], v[56:59], v[96:99], v[192:195]\n"
      "s_barrier\n"
      "v_mfma_f32_16x16x32_bf16 v[224:227], v[60:63], v[96:99], v[224:227]\n"
      "ds_read_b64_tr_b16 v[32:33], %[wa0] offset:8192\n"
      "v_mfma_f32_16x16x32_bf16 v[224:227], v[44:47], v[64:67], v[224:227]\n"
      "ds_read_b128 v[64:67], %[xa0] offset:32768\n"
      "v_mfma_f32_16x16x32_bf16 v[228:231], v[44:47], v[68:71], v[228:231]\n"
      "ds_read_b128 v[96:99], %[xa1] offset:32768\n"
      "v_mfma_f32_16x16x32_bf16 v[228:231], v[60:63], v[100:103], v[228:231]\n"
      "ds_read_b64_tr_b16 v[34:35], %[wa0] offset:8704\n"
      "v_mfma_f32_16x16x32_bf16 v[196:199], v[56:59], v[100:103], v[196:199]\n"
      "ds_read_b64_tr_b16 v[36:37], %[wa1] offset:8192\n"
      "v_mfma_f32_16x16x32_bf16 v[196:199], v[40:43], v[68:71], v[196:199]\n"
      "ds_read_b128 v[68:71], %[xa0] offset:34816\n"
      "v_mfma_f32_16x16x32_bf16 v[200:203], v[40:43], v[72:75], v[200:203]\n"
      "ds_read_b128 v[100:103], %[xa1] offset:34816\n"
      "v_mfma_f32_16x16x32_bf16 v[200:203], v[56:59], v[104:107], v[200:203]\n"
      "ds_read_b64_tr_b16 v[38:39], %[wa1] offset:8704\n"
      "v_mfma_f32_16x16x32_bf16 v[232:235], v[60:63], v[104:107], v[232:235]\n"
      "s_waitcnt vmcnt(8)\n"
      "v_mfma_f32_16x16x32_bf16 v[232:235], v[44:47], v[72:75], v[232:235]\n"
      "ds_read_b128 v[72:75], %[xa0] offset:36864\n"
      "v_mfma_f32_16x16x32_bf16 v[236:239], v[44:47], v[76:79], v[236:239]\n"
      "ds_read_b128 v[104:107], %[xa1] offset:36864\n"
      "v_mfma_f32_16x16x32_bf16 v[236:239], v[60:63], v[108:111], v[236:239]\n"
      "ds_read_b64_tr_b16 v[48:49], %[wa0] offset:12288\n"
      "v_mfma_f32_16x16x32_bf16 v[204:207], v[56:59], v[108:111], v[204:207]\n"
      "ds_read_b64_tr_b16 v[50:51], %[wa0] offset:12800\n"
      "v_mfma_f32_16x16x32_bf16 v[204:207], v[40:43], v[76:79], v[204:207]\n"
      "ds_read_b128 v[76:79], %[xa0] offset:38912\n"
      "v_mfma_f32_16x16x32_bf16 v[208:211], v[40:43], v[80:83], v[208:211]\n"
      "ds_read_b128 v[108:111], %[xa1] offset:38912\n"
      "v_mfma_f32_16x16x32_bf16 v[208:211], v[56:59], v[112:115], v[208:211]\n"
      "ds_read_b64_tr_b16 v[52:53], %[wa1] offset:12288\n"
      "v_mfma_f32_16x16x32_bf16 v[240:243], v[60:63], v[112:115], v[240:243]\n"
      "ds_read_b64_tr_b16 v[54:55], %[wa1] offset:12800\n"
      "v_mfma_f32_16x16x32_bf16 v[240:243], v[44:47], v[80:83], v[240:243]\n"
      "ds_read_b128 v[80:83], %[xa0] offset:40960\n"
      "v_mfma_f32_16x16x32_bf16 v[244:247], v[44:47], v[84:87], v[244:247]\n"
      "ds_read_b128 v[112:115], %[xa1] offset:40960\n"
      "v_mfma_f32_16x16x32_bf16 v[244:247], v[60:63], v[116:119], v[244:247]\n"
      "v_mfma_f32_16x16x32_bf16 v[212:215], v[56:59], v[116:119], v[212:215]\n"
      "v_mfma_f32_16x16x32_bf16 v[212:215], v[40:43], v[84:87], v[212:215]\n"
      "ds_read_b128 v[84:87], %[xa0] offset:43008\n"
      "v_mfma_f32_16x16x32_bf16 v[216:219], v[40:43], v[88:91], v[216:219]\n"
      "ds_read_b128 v[116:119], %[xa1] offset:43008\n"
      "v_mfma_f32_16x16x32_bf16 v[216:219], v[56:59], v[120:123], v[216:219]\n"
      "v_mfma_f32_16x16x32_bf16 v[248:251], v[60:63], v[120:123], v[248:251]\n"
      "v_mfma_f32_16x16x32_bf16 v[248:251], v[44:47], v[88:91], v[248:251]\n"
      "ds_read_b128 v[88:91], %[xa0] offset:45056\n"
      "v_mfma_f32_16x16x32_bf16 v[252:255], v[44:47], v[92:95], v[252:255]\n"
      "ds_read_b128 v[120:123], %[xa1] offset:45056\n"
      "v_mfma_f32_16x16x32_bf16 v[252:255], v[60:63], v[124:127], v[252:255]\n"
      "v_mfma_f32_16x16x32_bf16 v[220:223], v[56:59], v[124:127], v[220:223]\n"
      "v_mfma_f32_16x16x32_bf16 v[220:223], v[40:43], v[92:95], v[220:223]\n"
      "ds_read_b128 v[92:95], %[xa0] offset:47104\n"
      "s_waitcnt lgkmcnt(7)\n"
      "v_mfma_f32_16x16x32_bf16 v[128:131], v[32:35], v[64:67], v[128:131]\n"
      "ds_read_b128 v[124:127], %[xa1] offset:47104\n"
      "v_mfma_f32_16x16x32_bf16 v[128:131], v[48:51], v[96:99], v[128:131]\n"
      "s_add_u32 m0, %[sWb], 0\n"
      "v_mfma_f32_16x16x32_bf16 v[160:163], v[52:55], v[96:99], v[160:163]\n"
      "s_add_u32 %[sW], %[sW], %[d1]\n"
      "v_mfma_f32_16x16x32_bf16 v[160:163], v[36:39], v[64:67], v[160:163]\n"
      "ds_read_b64_tr_b16 v[40:41], %[wa2] offset:8192\n"
      "v_mfma_f32_16x16x32_bf16 v[164:167], v[36:39], v[68:71], v[164:167]\n"
      "buffer_load_dwordx4 %[wv0], %[srdW], %[sW] offen offset:0 lds\n"
      "v_mfma_f32_16x16x32_bf16 v[164:167], v[52:55], v[100:103], v[164:167]\n"
      "ds_read_b64_tr_b16 v[42:43], %[wa2] offset:8704\n"
      "v_mfma_f32_16x16x32_bf16 v[132:135], v[48:51], v[100:103], v[132:135]\n"
      "ds_read_b64_tr_b16 v[56:57], %[wa2] offset:12288\n"
      "v_mfma_f32_16x16x32_bf16 v[132:135], v[32:35], v[68:71], v[132:135]\n"
      "buffer_load_dwordx4 %[wv1], %[srdW], %[sW] offen offset:1024 lds\n"
      "v_mfma_f32_16x16x32_bf16 v[136:139], v[32:35], v[72:75], v[136:139]\n"
      "ds_read_b64_tr_b16 v[58:59], %[wa2] offset:12800\n"
      "v_mfma_f32_16x16x32_bf16 v[136:139], v[48:51], v[104:107], v[136:139]\n"
      "ds_read_b64_tr_b16 v[44:45], %[wa3] offset:8192\n"
      "v_mfma_f32_16x16x32_bf16 v[168:171], v[52:55], v[104:107], v[168:171]\n"
      "buffer_load_dwordx4 %[wv2], %[srdW], %[sW] offen offset:2048 lds\n"
      "v_mfma_f32_16x16x32_bf16 v[168:171], v[36:39], v[72:75], v[168:171]\n"
      "ds_read_b64_tr_b16 v[46:47], %[wa3] offset:8704\n"
      "v_mfma_f32_16x16x32_bf16 v[172:175], v[36:39], v[76:79], v[172:175]\n"
      "ds_read_b64_tr_b16 v[60:61], %[wa3] offset:12288\n"
      "v_mfma_f32_16x16x32_bf16 v[172:175], v[52:55], v[108:111], v[172:175]\n"
      "buffer_load_dwordx4 %[wv3], %[srdW], %[sW] offen offset:3072 lds\n"
      "v_mfma_f32_16x16x32_bf16 v[140:143], v[48:51], v[108:111], v[140:143]\n"
      "ds_read_b64_tr_b16 v[62:63], %[wa3] offset:12800\n"
      "v_mfma_f32_16x16x32_bf16 v[140:143], v[32:35], v[76:79], v[140:143]\n"
      "s_add_u32 m0, %[sWb], 4096\n"
      "s_waitcnt lgkmcnt(8)\n"
      "v_mfma_f32_16x16x32_bf16 v[144:147], v[32:35], v[80:83], v[144:147]\n"
      "s_add_u32 %[sW], %[sW], 0x8000\n"
      "v_mfma_f32_16x16x32_bf16 v[144:147], v[48:51], v[112:115], v[144:147]\n"
      "v_mfma_f32_16x16x32_bf16 v[176:179], v[52:55], v[112:115], v[176:179]\n"
      "buffer_load_dwordx4 %[wv0], %[srdW], %[sW] offen offset:0 lds\n"
      "v_mfma_f32_16x16x32_bf16 v[176:179], v[36:39], v[80:83], v[176:179]\n"
      "v_mfma_f32_16x16x32_bf16 v[180:183], v[36:39], v[84:87], v[180:183]\n"
      "v_mfma_f32_16x16x32_bf16 v[180:183], v[52:55], v[116:119], v[180:183]\n"
      "buffer_load_dwordx4 %[wv1], %[srdW], %[sW] offen offset:1024 lds\n"
      "v_mfma_f32_16x16x32_bf16 v[148:151], v[48:51], v[116:119], v[148:151]\n"
      "v_mfma_f32_16x16x32_bf16 v[148:151], v[32:35], v[84:87], v[148:151]\n"
      "v_mfma_f32_16x16x32_bf16 v[152:155], v[32:35], v[88:91], v[152:155]\n"
      "buffer_load_dwordx4 %[wv2], %[srdW], %[sW] offen offset:2048 lds\n"
      "v_mfma_f32_16x16x32_bf16 v[152:155], v[48:51], v[120:123], v[152:155]\n"
      "v_mfma_f32_16x16x32_bf16 v[184:187], v[52:55], v[120:123], v[184:187]\n"
      "v_mfma_f32_16x16x32_bf16 v[184:187], v[36:39], v[88:91], v[184:187]\n"
      "buffer_load_dwordx4 %[wv3], %[srdW], %[sW] offen offset:3072 lds\n"
      "v_mfma_f32_16x16x32_bf16 v[188:191], v[36:39], v[92:95], v[188:191]\n"
      "v_mfma_f32_16x16x32_bf16 v[188:191], v[52:55], v[124:127], v[188:191]\n"
      "v_mfma_f32_16x16x32_bf16 v[156:159], v[48:51], v[124:127], v[156:159]\n"
      "v_mfma_f32_16x16x32_bf16 v[156:159], v[32:35], v[92:95], v[156:159]\n"
      "s_waitcnt lgkmcnt(0)\n"
      "v_mfma_f32_16x16x32_bf16 v[192:195], v[40:43], v[64:67], v[192:195]\n"
      "s_waitcnt vmcnt(12)\n"
      "v_mfma_f32_16x16x32_bf16 v[192:195], v[56:59], v[96:99], v[192:195]\n"
      "s_barrier\n"
      "v_mfma_f32_16x16x32_bf16 v[224:227], v[60:63], v[96:99], v[224:227]\n"
      "ds_read_b64_tr_b16 v[32:33], %[wa0] offset:16384\n"
      "v_mfma_f32_16x16x32_bf16 v[224:227], v[44:47], v[64:67], v[224:227]\n"
      "ds_read_b128 v[64:67], %[xa0] offset:49152\n"
      "v_mfma_f32_16x16x32_bf16 v[228:231], v[44:47], v[68:71], v[228:231]\n"
      "ds_read_b128 v[96:99], %[xa1] offset:49152\n"
      "v_mfma_f32_16x16x32_bf16 v[228:231], v[60:63], v[100:103], v[228:231]\n"
      "ds_read_b64_tr_b16 v[34:35], %[wa0] offset:16896\n"
      "v_mfma_f32_16x16x32_bf16 v[196:199], v[56:59], v[100:103], v[196:199]\n"
      "ds_read_b64_tr_b16 v[36:37], %[wa1] offset:16384\n"
      "v_mfma_f32_16x16x32_bf16 v[196:199], v[40:43], v[68:71], v[196:199]\n"
      "ds_read_b128 v[68:71], %[xa0] offset:51200\n"
      "v_mfma_f32_16x16x32_bf16 v[200:203], v[40:43], v[72:75], v[200:203]\n"
      "ds_read_b128 v[100:103], %[xa1] offset:51200\n"
      "v_mfma_f32_16x16x32_bf16 v[200:203], v[56:59], v[104:107], v[200:203]\n"
      "ds_read_b64_tr_b16 v[38:39], %[wa1] offset:16896\n"
      "v_mfma_f32_16x16x32_bf16 v[232:235], v[60:63], v[104:107], v[232:235]\n"
      "s_waitcnt vmcnt(8)\n"
      "v_mfma_f32_16x16x32_bf16 v[232:235], v[44:47], v[72:75], v[232:235]\n"
      "ds_read_b128 v[72:75], %[xa0] offset:53248\n"
      "v_mfma_f32_16x16x32_bf16 v[236:239], v[44:47], v[76:79], v[236:239]\n"
      "ds_read_b128 v[104:107], %[xa1] offset:53248\n"
      "v_mfma_f32_16x16x32_bf16 v[236:239], v[60:63], v[108:111], v[236:239]\n"
      "ds_read_b64_tr_b16 v[48:49], %[wa0] offset:20480\n"
      "v_mfma_f32_16x16x32_bf16 v[204:207], v[56:59], v[108:111], v[204:207]\n"
      "ds_read_b64_tr_b16 v[50:51], %[wa0] offset:20992\n"
      "v_mfma_f32_16x16x32_bf16 v[204:207], v[40:43], v[76:79], v[204:207]\n"
      "ds_read_b128 v[76:79], %[xa0] offset:55296\n"
      "v_mfma_f32_16x16x32_bf16 v[208:211], v[40:43], v[80:83], v[208:211]\n"
      "ds_read_b128 v[108:111], %[xa1] offset:55296\n"
      "v_mfma_f32_16x16x32_bf16 v[208:211], v[56:59], v[112:115], v[208:211]\n"
      "ds_read_b64_tr_b16 v[52:53], %[wa1] offset:20480\n"
      "v_mfma_f32_16x16x32_bf16 v[240:243], v[60:63], v[112:115], v[240:243]\n"
      "ds_read_b64_tr_b16 v[54:55], %[wa1] offset:20992\n"
      "v_mfma_f32_16x16x32_bf16 v[240:243], v[44:47], v[80:83], v[240:243]\n"
      "ds_read_b128 v[80:83], %[xa0] offset:57344\n"
      "v_mfma_f32_16x16x32_bf16 v[244:247], v[44:47], v[84:87], v[244:247]\n"
      "ds_read_b128 v[112:115], %[xa1] offset:57344\n"
      "v_mfma_f32_16x16x32_bf16 v[244:247], v[60:63], v[116:119], v[244:247]\n"
      "v_mfma_f32_16x16x32_bf16 v[212:215], v[56:59], v[116:119], v[212:215]\n"
      "v_mfma_f32_16x16x32_bf16 v[212:215], v[40:43], v[84:87], v[212:215]\n"
      "ds_read_b128 v[84:87], %[xa0] offset:59392\n"
      "v_mfma_f32_16x16x32_bf16 v[216:219], v[40:43], v[88:91], v[216:219]\n"
      "ds_read_b128 v[116:119], %[xa1] offset:59392\n"
      "v_mfma_f32_16x16x32_bf16 v[216:219], v[56:59], v[120:123], v[216:219]\n"
      "v_mfma_f32_16x16x32_bf16 v[248:251], v[60:63], v[120:123], v[248:251]\n"
      "v_mfma_f32_16x16x32_bf16 v[248:251], v[44:47], v[88:91], v[248:251]\n"
      "ds_read_b128 v[88:91], %[xa0] offset:61440\n"
      "v_mfma_f32_16x16x32_bf16 v[252:255], v[44:47], v[92:95], v[252:255]\n"
      "ds_read_b128 v[120:123], %[xa1] offset:61440\n"
      "v_mfma_f32_16x16x32_bf16 v[252:255], v[60:63], v[124:127], v[252:255]\n"
      "v_mfma_f32_16x16x32_bf16 v[220:223], v[56:59], v[124:127], v[220:223]\n"
      "v_mfma_f32_16x16x32_bf16 v[220:223], v[40:43], v[92:95], v[220:223]\n"
      "ds_read_b128 v[92:95], %[xa0] offset:63488\n"
      "s_waitcnt lgkmcnt(7)\n"
      "v_mfma_f32_16x16x32_bf16 v[128:131], v[32:35], v[64:67], v[128:131]\n"
      "ds_read_b128 v[124:127], %[xa1] offset:63488\n"
      "v_mfma_f32_16x16x32_bf16 v[128:131], v[48:51], v[96:99], v[128:131]\n"
      "s_add_u32 m0, %[sWb], 8192\n"
      "v_mfma_f32_16x16x32_bf16 v[160:163], v[52:55], v[96:99], v[160:163]\n"
      "s_add_u32 %[sW], %[sW], %[d0]\n"
      "v_mfma_f32_16x16x32_bf16 v[160:163], v[36:39], v[64:67], v[160:163]\n"
      "ds_read_b64_tr_b16 v[40:41], %[wa2] offset:16384\n"
      "v_mfma_f32_16x16x32_bf16 v[164:167], v[36:39], v[68:71], v[164:167]\n"
      "buffer_load_dwordx4 %[wv0], %[srdW], %[sW] offen offset:0 lds\n"
      "v_mfma_f32_16x16x32_bf16 v[164:167], v[52:55], v[100:103], v[164:167]\n"
      "ds_read_b64_tr_b16 v[42:43], %[wa2] offset:16896\n"
      "v_mfma_f32_16x16x32_bf16 v[132:135], v[48:51], v[100:103], v[132:135]\n"
      "ds_read_b64_tr_b16 v[56:57], %[wa2] offset:20480\n"
      "v_mfma_f32_16x16x32_bf16 v[132:135], v[32:35], v[68:71], v[132:135]\n"
      "buffer_load_dwordx4 %[wv1], %[srdW], %[sW] offen offset:1024 lds\n"
      "v_mfma_f32_16x16x32_bf16 v[136:139], v[32:35], v[72:75], v[136:139]\n"
      "ds_read_b64_tr_b16 v[58:59], %[wa2] offset:20992\n"
      "v_mfma_f32_16x16x32_bf16 v[136:139], v[48:51], v[104:107], v[136:139]\n"
      "ds_read_b64_tr_b16 v[44:45], %[wa3] offset:16384\n"
      "v_mfma_f32_16x16x32_bf16 v[168:171], v[52:55], v[104:107], v[168:171]\n"
      "buffer_load_dwordx4 %[wv2], %[srdW], %[sW] offen offset:2048 lds\n"
      "v_mfma_f32_16x16x32_bf16 v[168:171], v[36:39], v[72:75], v[168:171]\n"
      "ds_read_b64_tr_b16 v[46:47], %[wa3] offset:16896\n"
      "v_mfma_f32_16x16x32_bf16 v[172:175], v[36:39], v[76:79], v[172:175]\n"
      "ds_read_b64_tr_b16 v[60:61], %[wa3] offset:20480\n"
      "v_mfma_f32_16x16x32_bf16 v[172:175], v[52:55], v[108:111], v[172:175]\n"
      "buffer_load_dwordx4 %[wv3], %[srdW], %[sW] offen offset:3072 lds\n"
      "v_mfma_f32_16x16x32_bf16 v[140:143], v[48:51], v[108:111], v[140:143]\n"
      "ds_read_b64_tr_b16 v[62:63], %[wa3] offset:20992\n"
      "v_mfma_f32_16x16x32_bf16 v[140:143], v[32:35], v[76:79], v[140:143]\n"
      "s_add_u32 m0, %[sWb], 12288\n"
      "s_waitcnt lgkmcnt(8)\n"
      "v_mfma_f32_16x16x32_bf16 v[144:147], v[32:35], v[80:83], v[144:147]\n"
      "s_add_u32 %[sW], %[sW], 0x8000\n"
      "v_mfma_f32_16x16x32_bf16 v[144:147], v[48:51], v[112:115], v[144:147]\n"
      "v_mfma_f32_16x16x32_bf16 v[176:179], v[52:55], v[112:115], v[176:179]\n"
      "buffer_load_dwordx4 %[wv0], %[srdW], %[sW] offen offset:0 lds\n"
      "v_mfma_f32_16x16x32_bf16 v[176:179], v[36:39], v[80:83], v[176:179]\n"
      "v_mfma_f32_16x16x32_bf16 v[180:183], v[36:39], v[84:87], v[180:183]\n"
      "v_mfma_f32_16x16x32_bf16 v[180:183], v[52:55], v[116:119], v[180:183]\n"
      "buffer_load_dwordx4 %[wv1], %[srdW], %[sW] offen offset:1024 lds\n"
      "v_mfma_f32_16x16x32_bf16 v[148:151], v[48:51], v[116:119], v[148:151]\n"
      "v_mfma_f32_16x16x32_bf16 v[148:151], v[32:35], v[84:87], v[148:151]\n"
      "v_mfma_f32_16x16x32_bf16 v[152:155], v[32:35], v[88:91], v[152:155]\n"
      "buffer_load_dwordx4 %[wv2], %[srdW], %[sW] offen offset:2048 lds\n"
      "v_mfma_f32_16x16x32_bf16 v[152:155], v[48:51], v[120:123], v[152:155]\n"
      "v_mfma_f32_16x16x32_bf16 v[184:187], v[52:55], v[120:123], v[184:187]\n"
      "v_mfma_f32_16x16x32_bf16 v[184:187], v[36:39], v[88:91], v[184:187]\n"
      "buffer_load_dwordx4 %[wv3], %[srdW], %[sW] offen offset:3072 lds\n"
      "v_mfma_f32_16x16x32_bf16 v[188:191], v[36:39], v[92:95], v[188:191]\n"
      "v_mfma_f32_16x16x32_bf16 v[188:191], v[52:55], v[124:127], v[188:191]\n"
      "v_mfma_f32_16x16x32_bf16 v[156:159], v[48:51], v[124:127], v[156:159]\n"
      "v_mfma_f32_16x16x32_bf16 v[156:159], v[32:35], v[92:95], v[156:159]\n"
      "s_waitcnt lgkmcnt(0)\n"
      "v_mfma_f32_16x16x32_bf16 v[192:195], v[40:43], v[64:67], v[192:195]\n"
      "s_waitcnt vmcnt(12)\n"
      "v_mfma_f32_16x16x32_bf16 v[192:195], v[56:59], v[96:99], v[192:195]\n"
      "s_barrier\n"
      "v_mfma_f32_16x16x32_bf16 v[224:227], v[60:63], v[96:99], v[224:227]\n"
      "ds_read_b64_tr_b16 v[32:33], %[wa0] offset:0\n"
      "v_mfma_f32_16x16x32_bf16 v[224:227], v[44:47], v[64:67], v[224:227]\n"
      "ds_read_b128 v[64:67], %[xa0] offset:0\n"
      "v_mfma_f32_16x16x32_bf16 v[228:231], v[44:47], v[68:71], v[228:231]\n"
      "ds_read_b128 v[96:99], %[xa1] offset:0\n"
      "v_mfma_f32_16x16x32_bf16 v[228:231], v[60:63], v[100:103], v[228:231]\n"
      "ds_read_b64_tr_b16 v[34:35], %[wa0] offset:512\n"
      "v_mfma_f32_16x16x32_bf16 v[196:199], v[56:59], v[100:103], v[196:199]\n"
      "ds_read_b64_tr_b16 v[36:37], %[wa1] offset:0\n"
      "v_mfma_f32_16x16x32_bf16 v[196:199], v[40:43], v[68:71], v[196:199]\n"
      "ds_read_b128 v[68:71], %[xa0] offset:2048\n"
      "v_mfma_f32_16x16x32_bf16 v[200:203], v[40:43], v[72:75], v[200:203]\n"
      "ds_read_b128 v[100:103], %[xa1] offset:2048\n"
      "v_mfma_f32_16x16x32_bf16 v[200:203], v[56:59], v[104:107], v[200:203]\n"
      "ds_read_b64_tr_b16 v[38:39], %[wa1] offset:512\n"
      "v_mfma_f32_16x16x32_bf16 v[232:235], v[60:63], v[104:107], v[232:235]\n"
      "s_waitcnt vmcnt(8)\n"
      "v_mfma_f32_16x16x32_bf16 v[232:235], v[44:47], v[72:75], v[232:235]\n"
      "ds_read_b128 v[72:75], %[xa0] offset:4096\n"
      "v_mfma_f32_16x16x32_bf16 v[236:239], v[44:47], v[76:79], v[236:239]\n"
      "ds_read_b128 v[104:107], %[xa1] offset:4096\n"
      "v_mfma_f32_16x16x32_bf16 v[236:239], v[60:63], v[108:111], v[236:239]\n"
      "ds_read_b64_tr_b16 v[48:49], %[wa0] offset:4096\n"
      "v_mfma_f32_16x16x32_bf16 v[204:207], v[56:59], v[108:111], v[204:207]\n"
      "ds_read_b64_tr_b16 v[50:51], %[wa0] offset:4608\n"
      "v_mfma_f32_16x16x32_bf16 v[204:207], v[40:43], v[76:79], v[204:207]\n"
      "ds_read_b128 v[76:79], %[xa0] offset:6144\n"
      "v_mfma_f32_16x16x32_bf16 v[208:211], v[40:43], v[80:83], v[208:211]\n"
      "ds_read_b128 v[108:111], %[xa1] offset:6144\n"
      "v_mfma_f32_16x16x32_bf16 v[208:211], v[56:59], v[112:115], v[208:211]\n"
      "ds_read_b64_tr_b16 v[52:53], %[wa1] offset:4096\n"
      "v_mfma_f32_16x16x32_bf16 v[240:243], v[60:63], v[112:115], v[240:243]\n"
      "ds_read_b64_tr_b16 v[54:55], %[wa1] offset:4608\n"
      "v_mfma_f32_16x16x32_bf16 v[240:243], v[44:47], v[80:83], v[240:243]\n"
      "ds_read_b128 v[80:83], %[xa0] offset:8192\n"
      "v_mfma_f32_16x16x32_bf16 v[244:247], v[44:47], v[84:87], v[244:247]\n"
      "ds_read_b128 v[112:115], %[xa1] offset:8192\n"
      "v_mfma_f32_16x16x32_bf16 v[244:247], v[60:63], v[116:119], v[244:247]\n"
      "v_mfma_f32_16x16x32_bf16 v[212:215], v[56:59], v[116:119], v[212:215]\n"
      "v_mfma_f32_16x16x32_bf16 v[212:215], v[40:43], v[84:87], v[212:215]\n"
      "ds_read_b128 v[84:87], %[xa0] offset:10240\n"
      "v_mfma_f32_16x16x32_bf16 v[216:219], v[40:43], v[88:91], v[216:219]\n"
      "ds_read_b128 v[116:119], %[xa1] offset:10240\n"
      "v_mfma_f32_16x16x32_bf16 v[216:219], v[56:59], v[120:123], v[216:219]\n"
      "v_mfma_f32_16x16x32_bf16 v[248:251], v[60:63], v[120:123], v[248:251]\n"
      "v_mfma_f32_16x16x32_bf16 v[248:251], v[44:47], v[88:91], v[248:251]\n"
      "ds_read_b128 v[88:91], %[xa0] offset:12288\n"
      "v_mfma_f32_16x16x32_bf16 v[252:255], v[44:47], v[92:95], v[252:255]\n"
      "ds_read_b128 v[120:123], %[xa1] offset:12288\n"
      "v_mfma_f32_16x16x32_bf16 v[252:255], v[60:63], v[124:127], v[252:255]\n"
      "v_mfma_f32_16x16x32_bf16 v[220:223], v[56:59], v[124:127], v[220:223]\n"
      "v_mfma_f32_16x16x32_bf16 v[220:223], v[40:43], v[92:95], v[220:223]\n"
      "ds_read_b128 v[92:95], %[xa0] offset:14336\n"
      "ds_read_b128 v[124:127], %[xa1] offset:14336\n"
      "s_mov_b32 %[sC], 3\n"
      "Lc0%=:\n"
      "s_waitcnt lgkmcnt(8)\n"
      "v_mfma_f32_16x16x32_bf16 v[128:131], v[32:35], v[64:67], v[128:131]\n"
      "s_add_u32 m0, %[sWb], 16384\n"
      "v_mfma_f32_16x16x32_bf16 v[128:131], v[48:51], v[96:99], v[128:131]\n"
      "s_add_u32 %[sW], %[sW], %[d1]\n"
      "v_mfma_f32_16x16x32_bf16 v[160:163], v[52:55], v[96:99], v[160:163]\n"
      "ds_read_b64_tr_b16 v[40:41], %[wa2] offset:0\n"
      "v_mfma_f32_16x16x32_bf16 v[160:163], v[36:39], v[64:67], v[160:163]\n"
      "buffer_load_dwordx4 %[wv0], %[srdW], %[sW] offen offset:0 lds\n"
      "v_mfma_f32_16x16x32_bf16 v[164:167], v[36:39], v[68:71], v[164:167]\n"
      "ds_read_b64_tr_b16 v[42:43], %[wa2] offset:512\n"
      "v_mfma_f32_16x16x32_bf16 v[164:167], v[52:55], v[100:103], v[164:167]\n"
      "ds_read_b64_tr_b16 v[56:57], %[wa2] offset:4096\n"
      "v_mfma_f32_16x16x32_bf16 v[132:135], v[48:51], v[100:103], v[132:135]\n"
      "buffer_load_dwordx4 %[wv1], %[srdW], %[sW] offen offset:1024 lds\n"
      "v_mfma_f32_16x16x32_bf16 v[132:135], v[32:35], v[68:71], v[132:135]\n"
      "ds_read_b64_tr_b16 v[58:59], %[wa2] offset:4608\n"
      "v_mfma_f32_16x16x32_bf16 v[136:139], v[32:35], v[72:75], v[136:139]\n"
      "ds_read_b64_tr_b16 v[44:45], %[wa3] offset:0\n"
      "v_mfma_f32_16x16x32_bf16 v[136:139], v[48:51], v[104:107], v[136:139]\n"
      "buffer_load_dwordx4 %[wv2], %[srdW], %[sW] offen offset:2048 lds\n"
      "v_mfma_f32_16x16x32_bf16 v[168:171], v[52:55], v[104:107], v[168:171]\n"
      "ds_read_b64_tr_b16 v[46:47], %[wa3] offset:512\n"
      "v_mfma_f32_16x16x32_bf16 v[168:171], v[36:39], v[72:75], v[168:171]\n"
      "ds_read_b64_tr_b16 v[60:61], %[wa3] offset:4096\n"
      "v_mfma_f32_16x16x32_bf16 v[172:175], v[36:39], v[76:79], v[172:175]\n"
      "buffer_load_dwordx4 %[wv3], %[srdW], %[sW] offen offset:3072 lds\n"
      "v_mfma_f32_16x16x32_bf16 v[172:175], v[52:55], v[108:111], v[172:175]\n"
      "ds_read_b64_tr_b16 v[62:63], %[wa3] offset:4608\n"
      "v_mfma_f32_16x16x32_bf16 v[140:143], v[48:51], v[108:111], v[140:143]\n"
      "s_add_u32 m0, %[sWb], 20480\n"
      "v_mfma_f32_16x16x32_bf16 v[140:143], v[32:35], v[76:79], v[140:143]\n"
      "s_add_u32 %[sW], %[sW], 0x8000\n"
      "s_waitcnt lgkmcnt(8)\n"
      "v_mfma_f32_16x16x32_bf16 v[144:147], v[32:35], v[80:83], v[144:147]\n"
      "v_mfma_f32_16x16x32_bf16 v[144:147], v[48:51], v[112:115], v[144:147]\n"
      "buffer_load_dwordx4 %[wv0], %[srdW], %[sW] offen offset:0 lds\n"
      "v_mfma_f32_16x16x32_bf16 v[176:179], v[52:55], v[112:115], v[176:179]\n"
      "v_mfma_f32_16x16x32_bf16 v[176:179], v[36:39], v[80:83], v[176:179]\n"
      "v_mfma_f32_16x16x32_bf16 v[180:183], v[36:39], v[84:87], v[180:183]\n"
      "buffer_load_dwordx4 %[wv1], %[srdW], %[sW] offen offset:1024 lds\n"
      "v_mfma_f32_16x16x32_bf16 v[180:183], v[52:55], v[116:119], v[180:183]\n"
      "v_mfma_f32_16x16x32_bf16 v[148:151], v[48:51], v[116:119], v[148:151]\n"
      "v_mfma_f32_16x16x32_bf16 v[148:151], v[32:35], v[84:87], v[148:151]\n"
      "buffer_load_dwordx4 %[wv2], %[srdW], %[sW] offen offset:2048 lds\n"
      "v_mfma_f32_16x16x32_bf16 v[152:155], v[32:35], v[88:91], v[152:155]\n"
      "v_mfma_f32_16x16x32_bf16 v[152:155], v[48:51], v[120:123], v[152:155]\n"
      "v_mfma_f32_16x16x32_bf16 v[184:187], v[52:55], v[120:123], v[184:187]\n"
      "buffer_load_dwordx4 %[wv3], %[srdW], %[sW] offen offset:3072 lds\n"
      "v_mfma_f32_16x16x32_bf16 v[184:187], v[36:39], v[88:91], v[184:187]\n"
      "v_mfma_f32_16x16x32_bf16 v[188:191], v[36:39], v[92:95], v[188:191]\n"
      "v_mfma_f32_16x16x32_bf16 v[188:191], v[52:55], v[124:127], v[188:191]\n"
      "v_mfma_f32_16x16x32_bf16 v[156:159], v[48:51], v[124:127], v[156:159]\n"
      "v_mfma_f32_16x16x32_bf16 v[156:159], v[32:35], v[92:95], v[156:159]\n"
      "s_waitcnt lgkmcnt(0)\n"
      "v_mfma_f32_16x16x32_bf16 v[192:195], v[40:43], v[64:67], v[192:195]\n"
      "s_waitcnt vmcnt(12)\n"
      "v_mfma_f32_16x16x32_bf16 v[192:195], v[56:59], v[96:99], v[192:195]\n"
      "s_barrier\n"
      "v_mfma_f32_16x16x32_bf16 v[224:227], v[60:63], v[96:99], v[224:227]\n"
      "ds_read_b64_tr_b16 v[32:33], %[wa0] offset:8192\n"
      "v_mfma_f32_16x16x32_bf16 v[224:227], v[44:47], v[64:67], v[224:227]\n"
      "ds_read_b128 v[64:67], %[xa0] offset:16384\n"
      "v_mfma_f32_16x16x32_bf16 v[228:231], v[44:47], v[68:71], v[228:231]\n"
      "ds_read_b128 v[96:99], %[xa1] offset:16384\n"
      "v_mfma_f32_16x16x32_bf16 v[228:231], v[60:63], v[100:103], v[228:231]\n"
      "ds_read_b64_tr_b16 v[34:35], %[wa0] offset:8704\n"
      "v_mfma_f32_16x16x32_bf16 v[196:199], v[56:59], v[100:103], v[196:199]\n"
      "ds_read_b64_tr_b16 v[36:37], %[wa1] offset:8192\n"
      "v_mfma_f32_16x16x32_bf16 v[196:199], v[40:43], v[68:71], v[196:199]\n"
      "ds_read_b128 v[68:71], %[xa0] offset:18432\n"
      "v_mfma_f32_16x16x32_bf16 v[200:203], v[40:43], v[72:75], v[200:203]\n"
      "ds_read_b128 v[100:103], %[xa1] offset:18432\n"
      "v_mfma_f32_16x16x32_bf16 v[200:203], v[56:59], v[104:107], v[200:203]\n"
      "ds_read_b64_tr_b16 v[38:39], %[wa1] offset:8704\n"
      "v_mfma_f32_16x16x32_bf16 v[232:235], v[60:63], v[104:107], v[232:235]\n"
      "s_waitcnt vmcnt(8)\n"
      "v_mfma_f32_16x16x32_bf16 v[232:235], v[44:47], v[72:75], v[232:235]\n"
      "ds_read_b128 v[72:75], %[xa0] offset:20480\n"
      "v_mfma_f32_16x16x32_bf16 v[236:239], v[44:47], v[76:79], v[236:239]\n"
      "ds_read_b128 v[104:107], %[xa1] offset:20480\n"
      "v_mfma_f32_16x16x32_bf16 v[236:239], v[60:63], v[108:111], v[236:239]\n"
      "ds_read_b64_tr_b16 v[48:49], %[wa0] offset:12288\n"
      "v_mfma_f32_16x16x32_bf16 v[204:207], v[56:59], v[108:111], v[204:207]\n"
      "ds_read_b64_tr_b16 v[50:51], %[wa0] offset:12800\n"
      "v_mfma_f32_16x16x32_bf16 v[204:207], v[40:43], v[76:79], v[204:207]\n"
      "ds_read_b128 v[76:79], %[xa0] offset:22528\n"
      "v_mfma_f32_16x16x32_bf16 v[208:211], v[40:43], v[80:83], v[208:211]\n"
      "ds_read_b128 v[108:111], %[xa1] offset:22528\n"
      "v_mfma_f32_16x16x32_bf16 v[208:211], v[56:59], v[112:115], v[208:211]\n"
      "ds_read_b64_tr_b16 v[52:53], %[wa1] offset:12288\n"
      "v_mfma_f32_16x16x32_bf16 v[240:243], v[60:63], v[112:115], v[240:243]\n"
      "ds_read_b64_tr_b16 v[54:55], %[wa1] offset:12800\n"
      "v_mfma_f32_16x16x32_bf16 v[240:243], v[44:47], v[80:83], v[240:243]\n"
      "ds_read_b128 v[80:83], %[xa0] offset:24576\n"
      "v_mfma_f32_16x16x32_bf16 v[244:247], v[44:47], v[84:87], v[244:247]\n"
      "ds_read_b128 v[112:115], %[xa1] offset:24576\n"
      "v_mfma_f32_16x16x32_bf16 v[244:247], v[60:63], v[116:119], v[244:247]\n"
      "v_mfma_f32_16x16x32_bf16 v[212:215], v[56:59], v[116:119], v[212:215]\n"
      "v_mfma_f32_16x16x32_bf16 v[212:215], v[40:43], v[84:87], v[212:215]\n"
      "ds_read_b128 v[84:87], %[xa0] offset:26624\n"
      "v_mfma_f32_16x16x32_bf16 v[216:219], v[40:43], v[88:91], v[216:219]\n"
      "ds_read_b128 v[116:119], %[xa1] offset:26624\n"
      "v_mfma_f32_16x16x32_bf16 v[216:219], v[56:59], v[120:123], v[216:219]\n"
      "v_mfma_f32_16x16x32_bf16 v[248:251], v[60:63], v[120:123], v[248:251]\n"
      "v_mfma_f32_16x16x32_bf16 v[248:251], v[44:47], v[88:91], v[248:251]\n"
      "ds_read_b128 v[88:91], %[xa0] offset:28672\n"
      "v_mfma_f32_16x16x32_bf16 v[252:255], v[44:47], v[92:95], v[252:255]\n"
      "ds_read_b128 v[120:123], %[xa1] offset:28672\n"
      "v_mfma_f32_16x16x32_bf16 v[252:255], v[60:63], v[124:127], v[252:255]\n"
      "v_mfma_f32_16x16x32_bf16 v[220:223], v[56:59], v[124:127], v[220:223]\n"
      "v_mfma_f32_16x16x32_bf16 v[220:223], v[40:43], v[92:95], v[220:223]\n"
      "ds_read_b128 v[92:95], %[xa0] offset:30720\n"
      "s_waitcnt lgkmcnt(7)\n"
      "v_mfma_f32_16x16x32_bf16 v[128:131], v[32:35], v[64:67], v[128:131]\n"
      "ds_read_b128 v[124:127], %[xa1] offset:30720\n"
      "v_mfma_f32_16x16x32_bf16 v[128:131], v[48:51], v[96:99], v[128:131]\n"
      "s_add_u32 m0, %[sWb], 0\n"
      "v_mfma_f32_16x16x32_bf16 v[160:163], v[52:55], v[96:99], v[160:163]\n"
      "s_add_u32 %[sW], %[sW], %[d0]\n"
      "v_mfma_f32_16x16x32_bf16 v[160:163], v[36:39], v[64:67], v[160:163]\n"
      "ds_read_b64_tr_b16 v[40:41], %[wa2] offset:8192\n"
      "v_mfma_f32_16x16x32_bf16 v[164:167], v[36:39], v[68:71], v[164:167]\n"
      "buffer_load_dwordx4 %[wv0], %[srdW], %[sW] offen offset:0 lds\n"
      "v_mfma_f32_16x16x32_bf16 v[164:167], v[52:55], v[100:103], v[164:167]\n"
      "ds_read_b64_tr_b16 v[42:43], %[wa2] offset:8704\n"
      "v_mfma_f32_16x16x32_bf16 v[132:135], v[48:51], v[100:103], v[132:135]\n"
      "ds_read_b64_tr_b16 v[56:57], %[wa2] offset:12288\n"
      "v_mfma_f32_16x16x32_bf16 v[132:135], v[32:35], v[68:71], v[132:135]\n"
      "buffer_load_dwordx4 %[wv1], %[srdW], %[sW] offen offset:1024 lds\n"
      "v_mfma_f32_16x16x32_bf16 v[136:139], v[32:35], v[72:75], v[136:139]\n"
      "ds_read_b64_tr_b16 v[58:59], %[wa2] offset:12800\n"
      "v_mfma_f32_16x16x32_bf16 v[136:139], v[48:51], v[104:107], v[136:139]\n"
      "ds_read_b64_tr_b16 v[44:45], %[wa3] offset:8192\n"
      "v_mfma_f32_16x16x32_bf16 v[168:171], v[52:55], v[104:107], v[168:171]\n"
      "buffer_load_dwordx4 %[wv2], %[srdW], %[sW] offen offset:2048 lds\n"
      "v_mfma_f32_16x16x32_bf16 v[168:171], v[36:39], v[72:75], v[168:171]\n"
      "ds_read_b64_tr_b16 v[46:47], %[wa3] offset:8704\n"
      "v_mfma_f32_16x16x32_bf16 v[172:175], v[36:39], v[76:79], v[172:175]\n"
      "ds_read_b64_tr_b16 v[60:61], %[wa3] offset:12288\n"
      "v_mfma_f32_16x16x32_bf16 v[172:175], v[52:55], v[108:111], v[172:175]\n"
      "buffer_load_dwordx4 %[wv3], %[srdW], %[sW] offen offset:3072 lds\n"
      "v_mfma_f32_16x16x32_bf16 v[140:143], v[48:51], v[108:111], v[140:143]\n"
      "ds_read_b64_tr_b16 v[62:63], %[wa3] offset:12800\n"
      "v_mfma_f32_16x16x32_bf16 v[140:143], v[32:35], v[76:79], v[140:143]\n"
      "s_add_u32 m0, %[sWb], 4096\n"
      "s_waitcnt lgkmcnt(8)\n"
      "v_mfma_f32_16x16x32_bf16 v[144:147], v[32:35], v[80:83], v[144:147]\n"
      "s_add_u32 %[sW], %[sW], 0x8000\n"
      "v_mfma_f32_16x16x32_bf16 v[144:147], v[48:51], v[112:115], v[144:147]\n"
      "v_mfma_f32_16x16x32_bf16 v[176:179], v[52:55], v[112:115], v[176:179]\n"
      "buffer_load_dwordx4 %[wv0], %[srdW], %[sW] offen offset:0 lds\n"
      "v_mfma_f32_16x16x32_bf16 v[176:179], v[36:39], v[80:83], v[176:179]\n"
      "v_mfma_f32_16x16x32_bf16 v[180:183], v[36:39], v[84:87], v[180:183]\n"
      "v_mfma_f32_16x16x32_bf16 v[180:183], v[52:55], v[116:119], v[180:183]\n"
      "buffer_load_dwordx4 %[wv1], %[srdW], %[sW] offen offset:1024 lds\n"
      "v_mfma_f32_16x16x32_bf16 v[148:151], v[48:51], v[116:119], v[148:151]\n"
      "v_mfma_f32_16x16x32_bf16 v[148:151], v[32:35], v[84:87], v[148:151]\n"
      "v_mfma_f32_16x16x32_bf16 v[152:155], v[32:35], v[88:91], v[152:155]\n"
      "buffer_load_dwordx4 %[wv2], %[srdW], %[sW] offen offset:2048 lds\n"
      "v_mfma_f32_16x16x32_bf16 v[152:155], v[48:51], v[120:123], v[152:155]\n"
      "v_mfma_f32_16x16x32_bf16 v[184:187], v[52:55], v[120:123], v[184:187]\n"
      "v_mfma_f32_16x16x32_bf16 v[184:187], v[36:39], v[88:91], v[184:187]\n"
      "buffer_load_dwordx4 %[wv3], %[srdW], %[sW] offen offset:3072 lds\n"
      "v_mfma_f32_16x16x32_bf16 v[188:191], v[36:39], v[92:95], v[188:191]\n"
      "v_mfma_f32_16x16x32_bf16 v[188:191], v[52:55], v[124:127], v[188:191]\n"
      "v_mfma_f32_16x16x32_bf16 v[156:159], v[48:51], v[124:127], v[156:159]\n"
      "v_mfma_f32_16x16x32_bf16 v[156:159], v[32:35], v[92:95], v[156:159]\n"
      "s_waitcnt lgkmcnt(0)\n"
      "v_mfma_f32_16x16x32_bf16 v[192:195], v[40:43], v[64:67], v[192:195]\n"
      "s_waitcnt vmcnt(12)\n"
      "v_mfma_f32_16x16x32_bf16 v[192:195], v[56:59], v[96:99], v[192:195]\n"
      "s_barrier\n"
      "v_mfma_f32_16x16x32_bf16 v[224:227], v[60:63], v[96:99], v[224:227]\n"
      "ds_read_b64_tr_b16 v[32:33], %[wa0] offset:16384\n"
      "v_mfma_f32_16x16x32_bf16 v[224:227], v[44:47], v[64:67], v[224:227]\n"
      "ds_read_b128 v[64:67], %[xa0] offset:32768\n"
      "v_mfma_f32_16x16x32_bf16 v[228:231], v[44:47], v[68:71], v[228:231]\n"
      "ds_read_b128 v[96:99], %[xa1] offset:32768\n"
      "v_mfma_f32_16x16x32_bf16 v[228:231], v[60:63], v[100:103], v[228:231]\n"
      "ds_read_b64_tr_b16 v[34:35], %[wa0] offset:16896\n"
      "v_mfma_f32_16x16x32_bf16 v[196:199], v[56:59], v[100:103], v[196:199]\n"
      "ds_read_b64_tr_b16 v[36:37], %[wa1] offset:16384\n"
      "v_mfma_f32_16x16x32_bf16 v[196:199], v[40:43], v[68:71], v[196:199]\n"
      "ds_read_b128 v[68:71], %[xa0] offset:34816\n"
      "v_mfma_f32_16x16x32_bf16 v[200:203], v[40:43], v[72:75], v[200:203]\n"
      "ds_read_b128 v[100:103], %[xa1] offset:34816\n"
      "v_mfma_f32_16x16x32_bf16 v[200:203], v[56:59], v[104:107], v[200:203]\n"
      "ds_read_b64_tr_b16 v[38:39], %[wa1] offset:16896\n"
      "v_mfma_f32_16x16x32_bf16 v[232:235], v[60:63], v[104:107], v[232:235]\n"
      "s_waitcnt vmcnt(8)\n"
      "v_mfma_f32_16x16x32_bf16 v[232:235], v[44:47], v[72:75], v[232:235]\n"
      "ds_read_b128 v[72:75], %[xa0] offset:36864\n"
      "v_mfma_f32_16x16x32_bf16 v[236:239], v[44:47], v[76:79], v[236:239]\n"
      "ds_read_b128 v[104:107], %[xa1] offset:36864\n"
      "v_mfma_f32_16x16x32_bf16 v[236:239], v[60:63], v[108:111], v[236:239]\n"
      "ds_read_b64_tr_b16 v[48:49], %[wa0] offset:20480\n"
      "v_mfma_f32_16x16x32_bf16 v[204:207], v[56:59], v[108:111], v[204:207]\n"
      "ds_read_b64_tr_b16 v[50:51], %[wa0] offset:20992\n"
      "v_mfma_f32_16x16x32_bf16 v[204:207], v[40:43], v[76:79], v[204:207]\n"
      "ds_read_b128 v[76:79], %[xa0] offset:38912\n"
      "v_mfma_f32_16x16x32_bf16 v[208:211], v[40:43], v[80:83], v[208:211]\n"
      "ds_read_b128 v[108:111], %[xa1] offset:38912\n"
      "v_mfma_f32_16x16x32_bf16 v[208:211], v[56:59], v[112:115], v[208:211]\n"
      "ds_read_b64_tr_b16 v[52:53], %[wa1] offset:20480\n"
      "v_mfma_f32_16x16x32_bf16 v[240:243], v[60:63], v[112:115], v[240:243]\n"
      "ds_read_b64_tr_b16 v[54:55], %[wa1] offset:20992\n"
      "v_mfma_f32_16x16x32_bf16 v[240:243], v[44:47], v[80:83], v[240:243]\n"
      "ds_read_b128 v[80:83], %[xa0] offset:40960\n"
      "v_mfma_f32_16x16x32_bf16 v[244:247], v[44:47], v[84:87], v[244:247]\n"
      "ds_read_b128 v[112:115], %[xa1] offset:40960\n"
      "v_mfma_f32_16x16x32_bf16 v[244:247], v[60:63], v[116:119], v[244:247]\n"
      "v_mfma_f32_16x16x32_bf16 v[212:215], v[56:59], v[116:119], v[212:215]\n"
      "v_mfma_f32_16x16x32_bf16 v[212:215], v[40:43], v[84:87], v[212:215]\n"
      "ds_read_b128 v[84:87], %[xa0] offset:43008\n"
      "v_mfma_f32_16x16x32_bf16 v[216:219], v[40:43], v[88:91], v[216:219]\n"
      "ds_read_b128 v[116:119], %[xa1] offset:43008\n"
      "v_mfma_f32_16x16x32_bf16 v[216:219], v[56:59], v[120:123], v[216:219]\n"
      "v_mfma_f32_16x16x32_bf16 v[248:251], v[60:63], v[120:123], v[248:251]\n"
      "v_mfma_f32_16x16x32_bf16 v[248:251], v[44:47], v[88:91], v[248:251]\n"
      "ds_read_b128 v[88:91], %[xa0] offset:45056\n"
      "v_mfma_f32_16x16x32_bf16 v[252:255], v[44:47], v[92:95], v[252:255]\n"
      "ds_read_b128 v[120:123], %[xa1] offset:45056\n"
      "v_mfma_f32_16x16x32_bf16 v[252:255], v[60:63], v[124:127], v[252:255]\n"
      "v_mfma_f32_16x16x32_bf16 v[220:223], v[56:59], v[124:127], v[220:223]\n"
      "v_mfma_f32_16x16x32_bf16 v[220:223], v[40:43], v[92:95], v[220:223]\n"
      "ds_read_b128 v[92:95], %[xa0] offset:47104\n"
      "s_waitcnt lgkmcnt(7)\n"
      "v_mfma_f32_16x16x32_bf16 v[128:131], v[32:35], v[64:67], v[128:131]\n"
      "ds_read_b128 v[124:127], %[xa1] offset:47104\n"
      "v_mfma_f32_16x16x32_bf16 v[128:131], v[48:51], v[96:99], v[128:131]\n"
      "s_add_u32 m0, %[sWb], 8192\n"
      "v_mfma_f32_16x16x32_bf16 v[160:163], v[52:55], v[96:99], v[160:163]\n"
      "s_add_u32 %[sW], %[sW], %[d1]\n"
      "v_mfma_f32_16x16x32_bf16 v[160:163], v[36:39], v[64:67], v[160:163]\n"
      "ds_read_b64_tr_b16 v[40:41], %[wa2] offset:16384\n"
      "v_mfma_f32_16x16x32_bf16 v[164:167], v[36:39], v[68:71], v[164:167]\n"
      "buffer_load_dwordx4 %[wv0], %[srdW], %[sW] offen offset:0 lds\n"
      "v_mfma_f32_16x16x32_bf16 v[164:167], v[52:55], v[100:103], v[164:167]\n"
      "ds_read_b64_tr_b16 v[42:43], %[wa2] offset:16896\n"
      "v_mfma_f32_16x16x32_bf16 v[132:135], v[48:51], v[100:103], v[132:135]\n"
      "ds_read_b64_tr_b16 v[56:57], %[wa2] offset:20480\n"
      "v_mfma_f32_16x16x32_bf16 v[132:135], v[32:35], v[68:71], v[132:135]\n"
      "buffer_load_dwordx4 %[wv1], %[srdW], %[sW] offen offset:1024 lds\n"
      "v_mfma_f32_16x16x32_bf16 v[136:139], v[32:35], v[72:75], v[136:139]\n"
      "ds_read_b64_tr_b16 v[58:59], %[wa2] offset:20992\n"
      "v_mfma_f32_16x16x32_bf16 v[136:139], v[48:51], v[104:107], v[136:139]\n"
      "ds_read_b64_tr_b16 v[44:45], %[wa3] offset:16384\n"
      "v_mfma_f32_16x16x32_bf16 v[168:171], v[52:55], v[104:107], v[168:171]\n"
      "buffer_load_dwordx4 %[wv2], %[srdW], %[sW] offen offset:2048 lds\n"
      "v_mfma_f32_16x16x32_bf16 v[168:171], v[36:39], v[72:75], v[168:171]\n"
      "ds_read_b64_tr_b16 v[46:47], %[wa3] offset:16896\n"
      "v_mfma_f32_16x16x32_bf16 v[172:175], v[36:39], v[76:79], v[172:175]\n"
      "ds_read_b64_tr_b16 v[60:61], %[wa3] offset:20480\n"
      "v_mfma_f32_16x16x32_bf16 v[172:175], v[52:55], v[108:111], v[172:175]\n"
      "buffer_load_dwordx4 %[wv3], %[srdW], %[sW] offen offset:3072 lds\n"
      "v_mfma_f32_16x16x32_bf16 v[140:143], v[48:51], v[108:111], v[140:143]\n"
      "ds_read_b64_tr_b16 v[62:63], %[wa3] offset:20992\n"
      "v_mfma_f32_16x16x32_bf16 v[140:143], v[32:35], v[76:79], v[140:143]\n"
      "s_add_u32 m0, %[sWb], 12288\n"
      "s_waitcnt lgkmcnt(8)\n"
      "v_mfma_f32_16x16x32_bf16 v[144:147], v[32:35], v[80:83], v[144:147]\n"
      "s_add_u32 %[sW], %[sW], 0x8000\n"
      "v_mfma_f32_16x16x32_bf16 v[144:147], v[48:51], v[112:115], v[144:147]\n"
      "v_mfma_f32_16x16x32_bf16 v[176:179], v[52:55], v[112:115], v[176:179]\n"
      "buffer_load_dwordx4 %[wv0], %[srdW], %[sW] offen offset:0 lds\n"
      "v_mfma_f32_16x16x32_bf16 v[176:179], v[36:39], v[80:83], v[176:179]\n"
      "v_mfma_f32_16x16x32_bf16 v[180:183], v[36:39], v[84:87], v[180:183]\n"
      "v_mfma_f32_16x16x32_bf16 v[180:183], v[52:55], v[116:119], v[180:183]\n"
      "buffer_load_dwordx4 %[wv1], %[srdW], %[sW] offen offset:1024 lds\n"
      "v_mfma_f32_16x16x32_bf16 v[148:151], v[48:51], v[116:119], v[148:151]\n"
      "v_mfma_f32_16x16x32_bf16 v[148:151], v[32:35], v[84:87], v[148:151]\n"
      "v_mfma_f32_16x16x32_bf16 v[152:155], v[32:35], v[88:91], v[152:155]\n"
      "buffer_load_dwordx4 %[wv2], %[srdW], %[sW] offen offset:2048 lds\n"
      "v_mfma_f32_16x16x32_bf16 v[152:155], v[48:51], v[120:123], v[152:155]\n"
      "v_mfma_f32_16x16x32_bf16 v[184:187], v[52:55], v[120:123], v[184:187]\n"
      "v_mfma_f32_16x16x32_bf16 v[184:187], v[36:39], v[88:91], v[184:187]\n"
      "buffer_load_dwordx4 %[wv3], %[srdW], %[sW] offen offset:3072 lds\n"
      "v_mfma_f32_16x16x32_bf16 v[188:191], v[36:39], v[92:95], v[188:191]\n"
      "v_mfma_f32_16x16x32_bf16 v[188:191], v[52:55], v[124:127], v[188:191]\n"
      "v_mfma_f32_16x16x32_bf16 v[156:159], v[48:51], v[124:127], v[156:159]\n"
      "v_mfma_f32_16x16x32_bf16 v[156:159], v[32:35], v[92:95], v[156:159]\n"
      "s_waitcnt lgkmcnt(0)\n"
      "v_mfma_f32_16x16x32_bf16 v[192:195], v[40:43], v[64:67], v[192:195]\n"
      "s_waitcnt vmcnt(12)\n"
      "v_mfma_f32_16x16x32_bf16 v[192:195], v[56:59], v[96:99], v[192:195]\n"
      "s_barrier\n"
      "v_mfma_f32_16x16x32_bf16 v[224:227], v[60:63], v[96:99], v[224:227]\n"
      "ds_read_b64_tr_b16 v[32:33], %[wa0] offset:0\n"
      "v_mfma_f32_16x16x32_bf16 v[224:227], v[44:47], v[64:67], v[224:227]\n"
      "ds_read_b128 v[64:67], %[xa0] offset:49152\n"
      "v_mfma_f32_16x16x32_bf16 v[228:231], v[44:47], v[68:71], v[228:231]\n"
      "ds_read_b128 v[96:99], %[xa1] offset:49152\n"
      "v_mfma_f32_16x16x32_bf16 v[228:231], v[60:63], v[100:103], v[228:231]\n"
      "ds_read_b64_tr_b16 v[34:35], %[wa0] offset:512\n"
      "v_mfma_f32_16x16x32_bf16 v[196:199], v[56:59], v[100:103], v[196:199]\n"
      "ds_read_b64_tr_b16 v[36:37], %[wa1] offset:0\n"
      "v_mfma_f32_16x16x32_bf16 v[196:199], v[40:43], v[68:71], v[196:199]\n"
      "ds_read_b128 v[68:71], %[xa0] offset:51200\n"
      "v_mfma_f32_16x16x32_bf16 v[200:203], v[40:43], v[72:75], v[200:203]\n"
      "ds_read_b128 v[100:103], %[xa1] offset:51200\n"
      "v_mfma_f32_16x16x32_bf16 v[200:203], v[56:59], v[104:107], v[200:203]\n"
      "ds_read_b64_tr_b16 v[38:39], %[wa1] offset:512\n"
      "v_mfma_f32_16x16x32_bf16 v[232:235], v[60:63], v[104:107], v[232:235]\n"
      "s_waitcnt vmcnt(8)\n"
      "v_mfma_f32_16x16x32_bf16 v[232:235], v[44:47], v[72:75], v[232:235]\n"
      "ds_read_b128 v[72:75], %[xa0] offset:53248\n"
      "v_mfma_f32_16x16x32_bf16 v[236:239], v[44:47], v[76:79], v[236:239]\n"
      "ds_read_b128 v[104:107], %[xa1] offset:53248\n"
      "v_mfma_f32_16x16x32_bf16 v[236:239], v[60:63], v[108:111], v[236:239]\n"
      "ds_read_b64_tr_b16 v[48:49], %[wa0] offset:4096\n"
      "v_mfma_f32_16x16x32_bf16 v[204:207], v[56:59], v[108:111], v[204:207]\n"
      "ds_read_b64_tr_b16 v[50:51], %[wa0] offset:4608\n"
      "v_mfma_f32_16x16x32_bf16 v[204:207], v[40:43], v[76:79], v[204:207]\n"
      "ds_read_b128 v[76:79], %[xa0] offset:55296\n"
      "v_mfma_f32_16x16x32_bf16 v[208:211], v[40:43], v[80:83], v[208:211]\n"
      "ds_read_b128 v[108:111], %[xa1] offset:55296\n"
      "v_mfma_f32_16x16x32_bf16 v[208:211], v[56:59], v[112:115], v[208:211]\n"
      "ds_read_b64_tr_b16 v[52:53], %[wa1] offset:4096\n"
      "v_mfma_f32_16x16x32_bf16 v[240:243], v[60:63], v[112:115], v[240:243]\n"
      "ds_read_b64_tr_b16 v[54:55], %[wa1] offset:4608\n"
      "v_mfma_f32_16x16x32_bf16 v[240:243], v[44:47], v[80:83], v[240:243]\n"
      "ds_read_b128 v[80:83], %[xa0] offset:57344\n"
      "v_mfma_f32_16x16x32_bf16 v[244:247], v[44:47], v[84:87], v[244:247]\n"
      "ds_read_b128 v[112:115], %[xa1] offset:57344\n"
      "v_mfma_f32_16x16x32_bf16 v[244:247], v[60:63], v[116:119], v[244:247]\n"
      "v_mfma_f32_16x16x32_bf16 v[212:215], v[56:59], v[116:119], v[212:215]\n"
      "v_mfma_f32_16x16x32_bf16 v[212:215], v[40:43], v[84:87], v[212:215]\n"
      "ds_read_b128 v[84:87], %[xa0] offset:59392\n"
      "v_mfma_f32_16x16x32_bf16 v[216:219], v[40:43], v[88:91], v[216:219]\n"
      "ds_read_b128 v[116:119], %[xa1] offset:59392\n"
      "v_mfma_f32_16x16x32_bf16 v[216:219], v[56:59], v[120:123], v[216:219]\n"
      "v_mfma_f32_16x16x32_bf16 v[248:251], v[60:63], v[120:123], v[248:251]\n"
      "v_mfma_f32_16x16x32_bf16 v[248:251], v[44:47], v[88:91], v[248:251]\n"
      "ds_read_b128 v[88:91], %[xa0] offset:61440\n"
      "v_mfma_f32_16x16x32_bf16 v[252:255], v[44:47], v[92:95], v[252:255]\n"
      "ds_read_b128 v[120:123], %[xa1] offset:61440\n"
      "v_mfma_f32_16x16x32_bf16 v[252:255], v[60:63], v[124:127], v[252:255]\n"
      "v_mfma_f32_16x16x32_bf16 v[220:223], v[56:59], v[124:127], v[220:223]\n"
      "v_mfma_f32_16x16x32_bf16 v[220:223], v[40:43], v[92:95], v[220:223]\n"
      "ds_read_b128 v[92:95], %[xa0] offset:63488\n"
      "s_waitcnt lgkmcnt(7)\n"
      "v_mfma_f32_16x16x32_bf16 v[128:131], v[32:35], v[64:67], v[128:131]\n"
      "ds_read_b128 v[124:127], %[xa1] offset:63488\n"
      "v_mfma_f32_16x16x32_bf16 v[128:131], v[48:51], v[96:99], v[128:131]\n"
      "s_add_u32 m0, %[sWb], 16384\n"
      "v_mfma_f32_16x16x32_bf16 v[160:163], v[52:55], v[96:99], v[160:163]\n"
      "s_add_u32 %[sW], %[sW], %[d0]\n"
      "v_mfma_f32_16x16x32_bf16 v[160:163], v[36:39], v[64:67], v[160:163]\n"
      "ds_read_b64_tr_b16 v[40:41], %[wa2] offset:0\n"
      "v_mfma_f32_16x16x32_bf16 v[164:167], v[36:39], v[68:71], v[164:167]\n"
      "buffer_load_dwordx4 %[wv0], %[srdW], %[sW] offen offset:0 lds\n"
      "v_mfma_f32_16x16x32_bf16 v[164:167], v[52:55], v[100:103], v[164:167]\n"
      "ds_read_b64_tr_b16 v[42:43], %[wa2] offset:512\n"
      "v_mfma_f32_16x16x32_bf16 v[132:135], v[48:51], v[100:103], v[132:135]\n"
      "ds_read_b64_tr_b16 v[56:57], %[wa2] offset:4096\n"
      "v_mfma_f32_16x16x32_bf16 v[132:135], v[32:35], v[68:71], v[132:135]\n"
      "buffer_load_dwordx4 %[wv1], %[srdW], %[sW] offen offset:1024 lds\n"
      "v_mfma_f32_16x16x32_bf16 v[136:139], v[32:35], v[72:75], v[136:139]\n"
      "ds_read_b64_tr_b16 v[58:59], %[wa2] offset:4608\n"
      "v_mfma_f32_16x16x32_bf16 v[136:139], v[48:51], v[104:107], v[136:139]\n"
      "ds_read_b64_tr_b16 v[44:45], %[wa3] offset:0\n"
      "v_mfma_f32_16x16x32_bf16 v[168:171], v[52:55], v[104:107], v[168:171]\n"
      "buffer_load_dwordx4 %[wv2], %[srdW], %[sW] offen offset:2048 lds\n"
      "v_mfma_f32_16x16x32_bf16 v[168:171], v[36:39], v[72:75], v[168:171]\n"
      "ds_read_b64_tr_b16 v[46:47], %[wa3] offset:512\n"
      "v_mfma_f32_16x16x32_bf16 v[172:175], v[36:39], v[76:79], v[172:175]\n"
      "ds_read_b64_tr_b16 v[60:61], %[wa3] offset:4096\n"
      "v_mfma_f32_16x16x32_bf16 v[172:175], v[52:55], v[108:111], v[172:175]\n"
      "buffer_load_dwordx4 %[wv3], %[srdW], %[sW] offen offset:3072 lds\n"
      "v_mfma_f32_16x16x32_bf16 v[140:143], v[48:51], v[108:111], v[140:143]\n"
      "ds_read_b64_tr_b16 v[62:63], %[wa3] offset:4608\n"
      "v_mfma_f32_16x16x32_bf16 v[140:143], v[32:35], v[76:79], v[140:143]\n"
      "s_add_u32 m0, %[sWb], 20480\n"
      "s_waitcnt lgkmcnt(8)\n"
      "v_mfma_f32_16x16x32_bf16 v[144:147], v[32:35], v[80:83], v[144:147]\n"
      "s_add_u32 %[sW], %[sW], 0x8000\n"
      "v_mfma_f32_16x16x32_bf16 v[144:147], v[48:51], v[112:115], v[144:147]\n"
      "v_mfma_f32_16x16x32_bf16 v[176:179], v[52:55], v[112:115], v[176:179]\n"
      "buffer_load_dwordx4 %[wv0], %[srdW], %[sW] offen offset:0 lds\n"
      "v_mfma_f32_16x16x32_bf16 v[176:179], v[36:39], v[80:83], v[176:179]\n"
      "v_mfma_f32_16x16x32_bf16 v[180:183], v[36:39], v[84:87], v[180:183]\n"
      "v_mfma_f32_16x16x32_bf16 v[180:183], v[52:55], v[116:119], v[180:183]\n"
      "buffer_load_dwordx4 %[wv1], %[srdW], %[sW] offen offset:1024 lds\n"
      "v_mfma_f32_16x16x32_bf16 v[148:151], v[48:51], v[116:119], v[148:151]\n"
      "v_mfma_f32_16x16x32_bf16 v[148:151], v[32:35], v[84:87], v[148:151]\n"
      "v_mfma_f32_16x16x32_bf16 v[152:155], v[32:35], v[88:91], v[152:155]\n"
      "buffer_load_dwordx4 %[wv2], %[srdW], %[sW] offen offset:2048 lds\n"
      "v_mfma_f32_16x16x32_bf16 v[152:155], v[48:51], v[120:123], v[152:155]\n"
      "v_mfma_f32_16x16x32_bf16 v[184:187], v[52:55], v[120:123], v[184:187]\n"
      "v_mfma_f32_16x16x32_bf16 v[184:187], v[36:39], v[88:91], v[184:187]\n"
      "buffer_load_dwordx4 %[wv3], %[srdW], %[sW] offen offset:3072 lds\n"
      "v_mfma_f32_16x16x32_bf16 v[188:191], v[36:39], v[92:95], v[188:191]\n"
      "v_mfma_f32_16x16x32_bf16 v[188:191], v[52:55], v[124:127], v[188:191]\n"
      "v_mfma_f32_16x16x32_bf16 v[156:159], v[48:51], v[124:127], v[156:159]\n"
      "v_mfma_f32_16x16x32_bf16 v[156:159], v[32:35], v[92:95], v[156:159]\n"
      "s_waitcnt lgkmcnt(0)\n"
      "v_mfma_f32_16x16x32_bf16 v[192:195], v[40:43], v[64:67], v[192:195]\n"
      "s_waitcnt vmcnt(12)\n"
      "v_mfma_f32_16x16x32_bf16 v[192:195], v[56:59], v[96:99], v[192:195]\n"
      "s_barrier\n"
      "v_mfma_f32_16x16x32_bf16 v[224:227], v[60:63], v[96:99], v[224:227]\n"
      "ds_read_b64_tr_b16 v[32:33], %[wa0] offset:8192\n"
      "v_mfma_f32_16x16x32_bf16 v[224:227], v[44:47], v[64:67], v[224:227]\n"
      "ds_read_b128 v[64:67], %[xa0] offset:0\n"
      "v_mfma_f32_16x16x32_bf16 v[228:231], v[44:47], v[68:71], v[228:231]\n"
      "ds_read_b128 v[96:99], %[xa1] offset:0\n"
      "v_mfma_f32_16x16x32_bf16 v[228:231], v[60:63], v[100:103], v[228:231]\n"
      "ds_read_b64_tr_b16 v[34:35], %[wa0] offset:8704\n"
      "v_mfma_f32_16x16x32_bf16 v[196:199], v[56:59], v[100:103], v[196:199]\n"
      "ds_read_b64_tr_b16 v[36:37], %[wa1] offset:8192\n"
      "v_mfma_f32_16x16x32_bf16 v[196:199], v[40:43], v[68:71], v[196:199]\n"
      "ds_read_b128 v[68:71], %[xa0] offset:2048\n"
      "v_mfma_f32_16x16x32_bf16 v[200:203], v[40:43], v[72:75], v[200:203]\n"
      "ds_read_b128 v[100:103], %[xa1] offset:2048\n"
      "v_mfma_f32_16x16x32_bf16 v[200:203], v[56:59], v[104:107], v[200:203]\n"
      "ds_read_b64_tr_b16 v[38:39], %[wa1] offset:8704\n"
      "v_mfma_f32_16x16x32_bf16 v[232:235], v[60:63], v[104:107], v[232:235]\n"
      "s_waitcnt vmcnt(8)\n"
      "v_mfma_f32_16x16x32_bf16 v[232:235], v[44:47], v[72:75], v[232:235]\n"
      "ds_read_b128 v[72:75], %[xa0] offset:4096\n"
      "v_mfma_f32_16x16x32_bf16 v[236:239], v[44:47], v[76:79], v[236:239]\n"
      "ds_read_b128 v[104:107], %[xa1] offset:4096\n"
      "v_mfma_f32_16x16x32_bf16 v[236:239], v[60:63], v[108:111], v[236:239]\n"
      "ds_read_b64_tr_b16 v[48:49], %[wa0] offset:12288\n"
      "v_mfma_f32_16x16x32_bf16 v[204:207], v[56:59], v[108:111], v[204:207]\n"
      "ds_read_b64_tr_b16 v[50:51], %[wa0] offset:12800\n"
      "v_mfma_f32_16x16x32_bf16 v[204:207], v[40:43], v[76:79], v[204:207]\n"
      "ds_read_b128 v[76:79], %[xa0] offset:6144\n"
      "v_mfma_f32_16x16x32_bf16 v[208:211], v[40:43], v[80:83], v[208:211]\n"
      "ds_read_b128 v[108:111], %[xa1] offset:6144\n"
      "v_mfma_f32_16x16x32_bf16 v[208:211], v[56:59], v[112:115], v[208:211]\n"
      "ds_read_b64_tr_b16 v[52:53], %[wa1] offset:12288\n"
      "v_mfma_f32_16x16x32_bf16 v[240:243], v[60:63], v[112:115], v[240:243]\n"
      "ds_read_b64_tr_b16 v[54:55], %[wa1] offset:12800\n"
      "v_mfma_f32_16x16x32_bf16 v[240:243], v[44:47], v[80:83], v[240:243]\n"
      "ds_read_b128 v[80:83], %[xa0] offset:8192\n"
      "v_mfma_f32_16x16x32_bf16 v[244:247], v[44:47], v[84:87], v[244:247]\n"
      "ds_read_b128 v[112:115], %[xa1] offset:8192\n"
      "v_mfma_f32_16x16x32_bf16 v[244:247], v[60:63], v[116:119], v[244:247]\n"
      "v_mfma_f32_16x16x32_bf16 v[212:215], v[56:59], v[116:119], v[212:215]\n"
      "v_mfma_f32_16x16x32_bf16 v[212:215], v[40:43], v[84:87], v[212:215]\n"
      "ds_read_b128 v[84:87], %[xa0] offset:10240\n"
      "v_mfma_f32_16x16x32_bf16 v[216:219], v[40:43], v[88:91], v[216:219]\n"
      "ds_read_b128 v[116:119], %[xa1] offset:10240\n"
      "v_mfma_f32_16x16x32_bf16 v[216:219], v[56:59], v[120:123], v[216:219]\n"
      "v_mfma_f32_16x16x32_bf16 v[248:251], v[60:63], v[120:123], v[248:251]\n"
      "v_mfma_f32_16x16x32_bf16 v[248:251], v[44:47], v[88:91], v[248:251]\n"
      "ds_read_b128 v[88:91], %[xa0] offset:12288\n"
      "v_mfma_f32_16x16x32_bf16 v[252:255], v[44:47], v[92:95], v[252:255]\n"
      "ds_read_b128 v[120:123], %[xa1] offset:12288\n"
      "v_mfma_f32_16x16x32_bf16 v[252:255], v[60:63], v[124:127], v[252:255]\n"
      "v_mfma_f32_16x16x32_bf16 v[220:223], v[56:59], v[124:127], v[220:223]\n"
      "v_mfma_f32_16x16x32_bf16 v[220:223], v[40:43], v[92:95], v[220:223]\n"
      "ds_read_b128 v[92:95], %[xa0] offset:14336\n"
      "s_waitcnt lgkmcnt(7)\n"
      "v_mfma_f32_16x16x32_bf16 v[128:131], v[32:35], v[64:67], v[128:131]\n"
      "ds_read_b128 v[124:127], %[xa1] offset:14336\n"
      "v_mfma_f32_16x16x32_bf16 v[128:131], v[48:51], v[96:99], v[128:131]\n"
      "s_add_u32 m0, %[sWb], 0\n"
      "v_mfma_f32_16x16x32_bf16 v[160:163], v[52:55], v[96:99], v[160:163]\n"
      "s_add_u32 %[sW], %[sW], %[d1]\n"
      "v_mfma_f32_16x16x32_bf16 v[160:163], v[36:39], v[64:67], v[160:163]\n"
      "ds_read_b64_tr_b16 v[40:41], %[wa2] offset:8192\n"
      "v_mfma_f32_16x16x32_bf16 v[164:167], v[36:39], v[68:71], v[164:167]\n"
      "buffer_load_dwordx4 %[wv0], %[srdW], %[sW] offen offset:0 lds\n"
      "v_mfma_f32_16x16x32_bf16 v[164:167], v[52:55], v[100:103], v[164:167]\n"
      "ds_read_b64_tr_b16 v[42:43], %[wa2] offset:8704\n"
      "v_mfma_f32_16x16x32_bf16 v[132:135], v[48:51], v[100:103], v[132:135]\n"
      "ds_read_b64_tr_b16 v[56:57], %[wa2] offset:12288\n"
      "v_mfma_f32_16x16x32_bf16 v[132:135], v[32:35], v[68:71], v[132:135]\n"
      "buffer_load_dwordx4 %[wv1], %[srdW], %[sW] offen offset:1024 lds\n"
      "v_mfma_f32_16x16x32_bf16 v[136:139], v[32:35], v[72:75], v[136:139]\n"
      "ds_read_b64_tr_b16 v[58:59], %[wa2] offset:12800\n"
      "v_mfma_f32_16x16x32_bf16 v[136:139], v[48:51], v[104:107], v[136:139]\n"
      "ds_read_b64_tr_b16 v[44:45], %[wa3] offset:8192\n"
      "v_mfma_f32_16x16x32_bf16 v[168:171], v[52:55], v[104:107], v[168:171]\n"
      "buffer_load_dwordx4 %[wv2], %[srdW], %[sW] offen offset:2048 lds\n"
      "v_mfma_f32_16x16x32_bf16 v[168:171], v[36:39], v[72:75], v[168:171]\n"
      "ds_read_b64_tr_b16 v[46:47], %[wa3] offset:8704\n"
      "v_mfma_f32_16x16x32_bf16 v[172:175], v[36:39], v[76:79], v[172:175]\n"
      "ds_read_b64_tr_b16 v[60:61], %[wa3] offset:12288\n"
      "v_mfma_f32_16x16x32_bf16 v[172:175], v[52:55], v[108:111], v[172:175]\n"
      "buffer_load_dwordx4 %[wv3], %[srdW], %[sW] offen offset:3072 lds\n"
      "v_mfma_f32_16x16x32_bf16 v[140:143], v[48:51], v[108:111], v[140:143]\n"
      "ds_read_b64_tr_b16 v[62:63], %[wa3] offset:12800\n"
      "v_mfma_f32_16x16x32_bf16 v[140:143], v[32:35], v[76:79], v[140:143]\n"
      "s_add_u32 m0, %[sWb], 4096\n"
      "s_waitcnt lgkmcnt(8)\n"
      "v_mfma_f32_16x16x32_bf16 v[144:147], v[32:35], v[80:83], v[144:147]\n"
      "s_add_u32 %[sW], %[sW], 0x8000\n"
      "v_mfma_f32_16x16x32_bf16 v[144:147], v[48:51], v[112:115], v[144:147]\n"
      "v_mfma_f32_16x16x32_bf16 v[176:179], v[52:55], v[112:115], v[176:179]\n"
      "buffer_load_dwordx4 %[wv0], %[srdW], %[sW] offen offset:0 lds\n"
      "v_mfma_f32_16x16x32_bf16 v[176:179], v[36:39], v[80:83], v[176:179]\n"
      "v_mfma_f32_16x16x32_bf16 v[180:183], v[36:39], v[84:87], v[180:183]\n"
      "v_mfma_f32_16x16x32_bf16 v[180:183], v[52:55], v[116:119], v[180:183]\n"
      "buffer_load_dwordx4 %[wv1], %[srdW], %[sW] offen offset:1024 lds\n"
      "v_mfma_f32_16x16x32_bf16 v[148:151], v[48:51], v[116:119], v[148:151]\n"
      "v_mfma_f32_16x16x32_bf16 v[148:151], v[32:35], v[84:87], v[148:151]\n"
      "v_mfma_f32_16x16x32_bf16 v[152:155], v[32:35], v[88:91], v[152:155]\n"
      "buffer_load_dwordx4 %[wv2], %[srdW], %[sW] offen offset:2048 lds\n"
      "v_mfma_f32_16x16x32_bf16 v[152:155], v[48:51], v[120:123], v[152:155]\n"
      "v_mfma_f32_16x16x32_bf16 v[184:187], v[52:55], v[120:123], v[184:187]\n"
      "v_mfma_f32_16x16x32_bf16 v[184:187], v[36:39], v[88:91], v[184:187]\n"
      "buffer_load_dwordx4 %[wv3], %[srdW], %[sW] offen offset:3072 lds\n"
      "v_mfma_f32_16x16x32_bf16 v[188:191], v[36:39], v[92:95], v[188:191]\n"
      "v_mfma_f32_16x16x32_bf16 v[188:191], v[52:55], v[124:127], v[188:191]\n"
      "v_mfma_f32_16x16x32_bf16 v[156:159], v[48:51], v[124:127], v[156:159]\n"
      "v_mfma_f32_16x16x32_bf16 v[156:159], v[32:35], v[92:95], v[156:159]\n"
      "s_waitcnt lgkmcnt(0)\n"
      "v_mfma_f32_16x16x32_bf16 v[192:195], v[40:43], v[64:67], v[192:195]\n"
      "s_waitcnt vmcnt(12)\n"
      "v_mfma_f32_16x16x32_bf16 v[192:195], v[56:59], v[96:99], v[192:195]\n"
      "s_barrier\n"
      "v_mfma_f32_16x16x32_bf16 v[224:227], v[60:63], v[96:99], v[224:227]\n"
      "ds_read_b64_tr_b16 v[32:33], %[wa0] offset:16384\n"
      "v_mfma_f32_16x16x32_bf16 v[224:227], v[44:47], v[64:67], v[224:227]\n"
      "ds_read_b128 v[64:67], %[xa0] offset:16384\n"
      "v_mfma_f32_16x16x32_bf16 v[228:231], v[44:47], v[68:71], v[228:231]\n"
      "ds_read_b128 v[96:99], %[xa1] offset:16384\n"
      "v_mfma_f32_16x16x32_bf16 v[228:231], v[60:63], v[100:103], v[228:231]\n"
      "ds_read_b64_tr_b16 v[34:35], %[wa0] offset:16896\n"
      "v_mfma_f32_16x16x32_bf16 v[196:199], v[56:59], v[100:103], v[196:199]\n"
      "ds_read_b64_tr_b16 v[36:37], %[wa1] offset:16384\n"
      "v_mfma_f32_16x16x32_bf16 v[196:199], v[40:43], v[68:71], v[196:199]\n"
      "ds_read_b128 v[68:71], %[xa0] offset:18432\n"
      "v_mfma_f32_16x16x32_bf16 v[200:203], v[40:43], v[72:75], v[200:203]\n"
      "ds_read_b128 v[100:103], %[xa1] offset:18432\n"
      "v_mfma_f32_16x16x32_bf16 v[200:203], v[56:59], v[104:107], v[200:203]\n"
      "ds_read_b64_tr_b16 v[38:39], %[wa1] offset:16896\n"
      "v_mfma_f32_16x16x32_bf16 v[232:235], v[60:63], v[104:107], v[232:235]\n"
      "s_waitcnt vmcnt(8)\n"
      "v_mfma_f32_16x16x32_bf16 v[232:235], v[44:47], v[72:75], v[232:235]\n"
      "ds_read_b128 v[72:75], %[xa0] offset:20480\n"
      "v_mfma_f32_16x16x32_bf16 v[236:239], v[44:47], v[76:79], v[236:239]\n"
      "ds_read_b128 v[104:107], %[xa1] offset:20480\n"
      "v_mfma_f32_16x16x32_bf16 v[236:239], v[60:63], v[108:111], v[236:239]\n"
      "ds_read_b64_tr_b16 v[48:49], %[wa0] offset:20480\n"
      "v_mfma_f32_16x16x32_bf16 v[204:207], v[56:59], v[108:111], v[204:207]\n"
      "ds_read_b64_tr_b16 v[50:51], %[wa0] offset:20992\n"
      "v_mfma_f32_16x16x32_bf16 v[204:207], v[40:43], v[76:79], v[204:207]\n"
      "ds_read_b128 v[76:79], %[xa0] offset:22528\n"
      "v_mfma_f32_16x16x32_bf16 v[208:211], v[40:43], v[80:83], v[208:211]\n"
      "ds_read_b128 v[108:111], %[xa1] offset:22528\n"
      "v_mfma_f32_16x16x32_bf16 v[208:211], v[56:59], v[112:115], v[208:211]\n"
      "ds_read_b64_tr_b16 v[52:53], %[wa1] offset:20480\n"
      "v_mfma_f32_16x16x32_bf16 v[240:243], v[60:63], v[112:115], v[240:243]\n"
      "ds_read_b64_tr_b16 v[54:55], %[wa1] offset:20992\n"
      "v_mfma_f32_16x16x32_bf16 v[240:243], v[44:47], v[80:83], v[240:243]\n"
      "ds_read_b128 v[80:83], %[xa0] offset:24576\n"
      "v_mfma_f32_16x16x32_bf16 v[244:247], v[44:47], v[84:87], v[244:247]\n"
      "ds_read_b128 v[112:115], %[xa1] offset:24576\n"
      "v_mfma_f32_16x16x32_bf16 v[244:247], v[60:63], v[116:119], v[244:247]\n"
      "v_mfma_f32_16x16x32_bf16 v[212:215], v[56:59], v[116:119], v[212:215]\n"
      "v_mfma_f32_16x16x32_bf16 v[212:215], v[40:43], v[84:87], v[212:215]\n"
      "ds_read_b128 v[84:87], %[xa0] offset:26624\n"
      "v_mfma_f32_16x16x32_bf16 v[216:219], v[40:43], v[88:91], v[216:219]\n"
      "ds_read_b128 v[116:119], %[xa1] offset:26624\n"
      "v_mfma_f32_16x16x32_bf16 v[216:219], v[56:59], v[120:123], v[216:219]\n"
      "v_mfma_f32_16x16x32_bf16 v[248:251], v[60:63], v[120:123], v[248:251]\n"
      "v_mfma_f32_16x16x32_bf16 v[248:251], v[44:47], v[88:91], v[248:251]\n"
      "ds_read_b128 v[88:91], %[xa0] offset:28672\n"
      "v_mfma_f32_16x16x32_bf16 v[252:255], v[44:47], v[92:95], v[252:255]\n"
      "ds_read_b128 v[120:123], %[xa1] offset:28672\n"
      "v_mfma_f32_16x16x32_bf16 v[252:255], v[60:63], v[124:127], v[252:255]\n"
      "v_mfma_f32_16x16x32_bf16 v[220:223], v[56:59], v[124:127], v[220:223]\n"
      "v_mfma_f32_16x16x32_bf16 v[220:223], v[40:43], v[92:95], v[220:223]\n"
      "ds_read_b128 v[92:95], %[xa0] offset:30720\n"
      "s_waitcnt lgkmcnt(7)\n"
      "v_mfma_f32_16x16x32_bf16 v[128:131], v[32:35], v[64:67], v[128:131]\n"
      "ds_read_b128 v[124:127], %[xa1] offset:30720\n"
      "v_mfma_f32_16x16x32_bf16 v[128:131], v[48:51], v[96:99], v[128:131]\n"
      "s_add_u32 m0, %[sWb], 8192\n"
      "v_mfma_f32_16x16x32_bf16 v[160:163], v[52:55], v[96:99], v[160:163]\n"
      "s_add_u32 %[sW], %[sW], %[d0]\n"
      "v_mfma_f32_16x16x32_bf16 v[160:163], v[36:39], v[64:67], v[160:163]\n"
      "ds_read_b64_tr_b16 v[40:41], %[wa2] offset:16384\n"
      "v_mfma_f32_16x16x32_bf16 v[164:167], v[36:39], v[68:71], v[164:167]\n"
      "buffer_load_dwordx4 %[wv0], %[srdW], %[sW] offen offset:0 lds\n"
      "v_mfma_f32_16x16x32_bf16 v[164:167], v[52:55], v[100:103], v[164:167]\n"
      "ds_read_b64_tr_b16 v[42:43], %[wa2] offset:16896\n"
      "v_mfma_f32_16x16x32_bf16 v[132:135], v[48:51], v[100:103], v[132:135]\n"
      "ds_read_b64_tr_b16 v[56:57], %[wa2] offset:20480\n"
      "v_mfma_f32_16x16x32_bf16 v[132:135], v[32:35], v[68:71], v[132:135]\n"
      "buffer_load_dwordx4 %[wv1], %[srdW], %[sW] offen offset:1024 lds\n"
      "v_mfma_f32_16x16x32_bf16 v[136:139], v[32:35], v[72:75], v[136:139]\n"
      "ds_read_b64_tr_b16 v[58:59], %[wa2] offset:20992\n"
      "v_mfma_f32_16x16x32_bf16 v[136:139], v[48:51], v[104:107], v[136:139]\n"
      "ds_read_b64_tr_b16 v[44:45], %[wa3] offset:16384\n"
      "v_mfma_f32_16x16x32_bf16 v[168:171], v[52:55], v[104:107], v[168:171]\n"
      "buffer_load_dwordx4 %[wv2], %[srdW], %[sW] offen offset:2048 lds\n"
      "v_mfma_f32_16x16x32_bf16 v[168:171], v[36:39], v[72:75], v[168:171]\n"
      "ds_read_b64_tr_b16 v[46:47], %[wa3] offset:16896\n"
      "v_mfma_f32_16x16x32_bf16 v[172:175], v[36:39], v[76:79], v[172:175]\n"
      "ds_read_b64_tr_b16 v[60:61], %[wa3] offset:20480\n"
      "v_mfma_f32_16x16x32_bf16 v[172:175], v[52:55], v[108:111], v[172:175]\n"
      "buffer_load_dwordx4 %[wv3], %[srdW], %[sW] offen offset:3072 lds\n"
      "v_mfma_f32_16x16x32_bf16 v[140:143], v[48:51], v[108:111], v[140:143]\n"
      "ds_read_b64_tr_b16 v[62:63], %[wa3] offset:20992\n"
      "v_mfma_f32_16x16x32_bf16 v[140:143], v[32:35], v[76:79], v[140:143]\n"
      "s_add_u32 m0, %[sWb], 12288\n"
      "s_waitcnt lgkmcnt(8)\n"
      "v_mfma_f32_16x16x32_bf16 v[144:147], v[32:35], v[80:83], v[144:147]\n"
      "s_add_u32 %[sW], %[sW], 0x8000\n"
      "v_mfma_f32_16x16x32_bf16 v[144:147], v[48:51], v[112:115], v[144:147]\n"
      "v_mfma_f32_16x16x32_bf16 v[176:179], v[52:55], v[112:115], v[176:179]\n"
      "buffer_load_dwordx4 %[wv0], %[srdW], %[sW] offen offset:0 lds\n"
      "v_mfma_f32_16x16x32_bf16 v[176:179], v[36:39], v[80:83], v[176:179]\n"
      "v_mfma_f32_16x16x32_bf16 v[180:183], v[36:39], v[84:87], v[180:183]\n"
      "v_mfma_f32_16x16x32_bf16 v[180:183], v[52:55], v[116:119], v[180:183]\n"
      "buffer_load_dwordx4 %[wv1], %[srdW], %[sW] offen offset:1024 lds\n"
      "v_mfma_f32_16x16x32_bf16 v[148:151], v[48:51], v[116:119], v[148:151]\n"
      "v_mfma_f32_16x16x32_bf16 v[148:151], v[32:35], v[84:87], v[148:151]\n"
      "v_mfma_f32_16x16x32_bf16 v[152:155], v[32:35], v[88:91], v[152:155]\n"
      "buffer_load_dwordx4 %[wv2], %[srdW], %[sW] offen offset:2048 lds\n"
      "v_mfma_f32_16x16x32_bf16 v[152:155], v[48:51], v[120:123], v[152:155]\n"
      "v_mfma_f32_16x16x32_bf16 v[184:187], v[52:55], v[120:123], v[184:187]\n"
      "v_mfma_f32_16x16x32_bf16 v[184:187], v[36:39], v[88:91], v[184:187]\n"
      "buffer_load_dwordx4 %[wv3], %[srdW], %[sW] offen offset:3072 lds\n"
      "v_mfma_f32_16x16x32_bf16 v[188:191], v[36:39], v[92:95], v[188:191]\n"
      "v_mfma_f32_16x16x32_bf16 v[188:191], v[52:55], v[124:127], v[188:191]\n"
      "v_mfma_f32_16x16x32_bf16 v[156:159], v[48:51], v[124:127], v[156:159]\n"
      "v_mfma_f32_16x16x32_bf16 v[156:159], v[32:35], v[92:95], v[156:159]\n"
      "s_waitcnt lgkmcnt(0)\n"
      "v_mfma_f32_16x16x32_bf16 v[192:195], v[40:43], v[64:67], v[192:195]\n"
      "s_waitcnt vmcnt(12)\n"
      "v_mfma_f32_16x16x32_bf16 v[192:195], v[56:59], v[96:99], v[192:195]\n"
      "s_barrier\n"
      "v_mfma_f32_16x16x32_bf16 v[224:227], v[60:63], v[96:99], v[224:227]\n"
      "ds_read_b64_tr_b16 v[32:33], %[wa0] offset:0\n"
      "v_mfma_f32_16x16x32_bf16 v[224:227], v[44:47], v[64:67], v[224:227]\n"
      "ds_read_b128 v[64:67], %[xa0] offset:32768\n"
      "v_mfma_f32_16x16x32_bf16 v[228:231], v[44:47], v[68:71], v[228:231]\n"
      "ds_read_b128 v[96:99], %[xa1] offset:32768\n"
      "v_mfma_f32_16x16x32_bf16 v[228:231], v[60:63], v[100:103], v[228:231]\n"
      "ds_read_b64_tr_b16 v[34:35], %[wa0] offset:512\n"
      "v_mfma_f32_16x16x32_bf16 v[196:199], v[56:59], v[100:103], v[196:199]\n"
      "ds_read_b64_tr_b16 v[36:37], %[wa1] offset:0\n"
      "v_mfma_f32_16x16x32_bf16 v[196:199], v[40:43], v[68:71], v[196:199]\n"
      "ds_read_b128 v[68:71], %[xa0] offset:34816\n"
      "v_mfma_f32_16x16x32_bf16 v[200:203], v[40:43], v[72:75], v[200:203]\n"
      "ds_read_b128 v[100:103], %[xa1] offset:34816\n"
      "v_mfma_f32_16x16x32_bf16 v[200:203], v[56:59], v[104:107], v[200:203]\n"
      "ds_read_b64_tr_b16 v[38:39], %[wa1] offset:512\n"
      "v_mfma_f32_16x16x32_bf16 v[232:235], v[60:63], v[104:107], v[232:235]\n"
      "s_waitcnt vmcnt(8)\n"
      "v_mfma_f32_16x16x32_bf16 v[232:235], v[44:47], v[72:75], v[232:235]\n"
      "ds_read_b128 v[72:75], %[xa0] offset:36864\n"
      "v_mfma_f32_16x16x32_bf16 v[236:239], v[44:47], v[76:79], v[236:239]\n"
      "ds_read_b128 v[104:107], %[xa1] offset:36864\n"
      "v_mfma_f32_16x16x32_bf16 v[236:239], v[60:63], v[108:111], v[236:239]\n"
      "ds_read_b64_tr_b16 v[48:49], %[wa0] offset:4096\n"
      "v_mfma_f32_16x16x32_bf16 v[204:207], v[56:59], v[108:111], v[204:207]\n"
      "ds_read_b64_tr_b16 v[50:51], %[wa0] offset:4608\n"
      "v_mfma_f32_16x16x32_bf16 v[204:207], v[40:43], v[76:79], v[204:207]\n"
      "ds_read_b128 v[76:79], %[xa0] offset:38912\n"
      "v_mfma_f32_16x16x32_bf16 v[208:211], v[40:43], v[80:83], v[208:211]\n"
      "ds_read_b128 v[108:111], %[xa1] offset:38912\n"
      "v_mfma_f32_16x16x32_bf16 v[208:211], v[56:59], v[112:115], v[208:211]\n"
      "ds_read_b64_tr_b16 v[52:53], %[wa1] offset:4096\n"
      "v_mfma_f32_16x16x32_bf16 v[240:243], v[60:63], v[112:115], v[240:243]\n"
      "ds_read_b64_tr_b16 v[54:55], %[wa1] offset:4608\n"
      "v_mfma_f32_16x16x32_bf16 v[240:243], v[44:47], v[80:83], v[240:243]\n"
      "ds_read_b128 v[80:83], %[xa0] offset:40960\n"
      "v_mfma_f32_16x16x32_bf16 v[244:247], v[44:47], v[84:87], v[244:247]\n"
      "ds_read_b128 v[112:115], %[xa1] offset:40960\n"
      "v_mfma_f32_16x16x32_bf16 v[244:247], v[60:63], v[116:119], v[244:247]\n"
      "v_mfma_f32_16x16x32_bf16 v[212:215], v[56:59], v[116:119], v[212:215]\n"
      "v_mfma_f32_16x16x32_bf16 v[212:215], v[40:43], v[84:87], v[212:215]\n"
      "ds_read_b128 v[84:87], %[xa0] offset:43008\n"
      "v_mfma_f32_16x16x32_bf16 v[216:219], v[40:43], v[88:91], v[216:219]\n"
      "ds_read_b128 v[116:119], %[xa1] offset:43008\n"
      "v_mfma_f32_16x16x32_bf16 v[216:219], v[56:59], v[120:123], v[216:219]\n"
      "v_mfma_f32_16x16x32_bf16 v[248:251], v[60:63], v[120:123], v[248:251]\n"
      "v_mfma_f32_16x16x32_bf16 v[248:251], v[44:47], v[88:91], v[248:251]\n"
      "ds_read_b128 v[88:91], %[xa0] offset:45056\n"
      "v_mfma_f32_16x16x32_bf16 v[252:255], v[44:47], v[92:95], v[252:255]\n"
      "ds_read_b128 v[120:123], %[xa1] offset:45056\n"
      "v_mfma_f32_16x16x32_bf16 v[252:255], v[60:63], v[124:127], v[252:255]\n"
      "v_mfma_f32_16x16x32_bf16 v[220:223], v[56:59], v[124:127], v[220:223]\n"
      "v_mfma_f32_16x16x32_bf16 v[220:223], v[40:43], v[92:95], v[220:223]\n"
      "ds_read_b128 v[92:95], %[xa0] offset:47104\n"
      "s_waitcnt lgkmcnt(7)\n"
      "v_mfma_f32_16x16x32_bf16 v[128:131], v[32:35], v[64:67], v[128:131]\n"
      "ds_read_b128 v[124:127], %[xa1] offset:47104\n"
      "v_mfma_f32_16x16x32_bf16 v[128:131], v[48:51], v[96:99], v[128:131]\n"
      "s_add_u32 m0, %[sWb], 16384\n"
      "v_mfma_f32_16x16x32_bf16 v[160:163], v[52:55], v[96:99], v[160:163]\n"
      "s_add_u32 %[sW], %[sW], %[d1]\n"
      "v_mfma_f32_16x16x32_bf16 v[160:163], v[36:39], v[64:67], v[160:163]\n"
      "ds_read_b64_tr_b16 v[40:41], %[wa2] offset:0\n"
      "v_mfma_f32_16x16x32_bf16 v[164:167], v[36:39], v[68:71], v[164:167]\n"
      "buffer_load_dwordx4 %[wv0], %[srdW], %[sW] offen offset:0 lds\n"
      "v_mfma_f32_16x16x32_bf16 v[164:167], v[52:55], v[100:103], v[164:167]\n"
      "ds_read_b64_tr_b16 v[42:43], %[wa2] offset:512\n"
      "v_mfma_f32_16x16x32_bf16 v[132:135], v[48:51], v[100:103], v[132:135]\n"
      "ds_read_b64_tr_b16 v[56:57], %[wa2] offset:4096\n"
      "v_mfma_f32_16x16x32_bf16 v[132:135], v[32:35], v[68:71], v[132:135]\n"
      "buffer_load_dwordx4 %[wv1], %[srdW], %[sW] offen offset:1024 lds\n"
      "v_mfma_f32_16x16x32_bf16 v[136:139], v[32:35], v[72:75], v[136:139]\n"
      "ds_read_b64_tr_b16 v[58:59], %[wa2] offset:4608\n"
      "v_mfma_f32_16x16x32_bf16 v[136:139], v[48:51], v[104:107], v[136:139]\n"
      "ds_read_b64_tr_b16 v[44:45], %[wa3] offset:0\n"
      "v_mfma_f32_16x16x32_bf16 v[168:171], v[52:55], v[104:107], v[168:171]\n"
      "buffer_load_dwordx4 %[wv2], %[srdW], %[sW] offen offset:2048 lds\n"
      "v_mfma_f32_16x16x32_bf16 v[168:171], v[36:39], v[72:75], v[168:171]\n"
      "ds_read_b64_tr_b16 v[46:47], %[wa3] offset:512\n"
      "v_mfma_f32_16x16x32_bf16 v[172:175], v[36:39], v[76:79], v[172:175]\n"
      "ds_read_b64_tr_b16 v[60:61], %[wa3] offset:4096\n"
      "v_mfma_f32_16x16x32_bf16 v[172:175], v[52:55], v[108:111], v[172:175]\n"
      "buffer_load_dwordx4 %[wv3], %[srdW], %[sW] offen offset:3072 lds\n"
      "v_mfma_f32_16x16x32_bf16 v[140:143], v[48:51], v[108:111], v[140:143]\n"
      "ds_read_b64_tr_b16 v[62:63], %[wa3] offset:4608\n"
      "v_mfma_f32_16x16x32_bf16 v[140:143], v[32:35], v[76:79], v[140:143]\n"
      "s_add_u32 m0, %[sWb], 20480\n"
      "s_waitcnt lgkmcnt(8)\n"
      "v_mfma_f32_16x16x32_bf16 v[144:147], v[32:35], v[80:83], v[144:147]\n"
      "s_add_u32 %[sW], %[sW], 0x8000\n"
      "v_mfma_f32_16x16x32_bf16 v[144:147], v[48:51], v[112:115], v[144:147]\n"
      "v_mfma_f32_16x16x32_bf16 v[176:179], v[52:55], v[112:115], v[176:179]\n"
      "buffer_load_dwordx4 %[wv0], %[srdW], %[sW] offen offset:0 lds\n"
      "v_mfma_f32_16x16x32_bf16 v[176:179], v[36:39], v[80:83], v[176:179]\n"
      "v_mfma_f32_16x16x32_bf16 v[180:183], v[36:39], v[84:87], v[180:183]\n"
      "v_mfma_f32_16x16x32_bf16 v[180:183], v[52:55], v[116:119], v[180:183]\n"
      "buffer_load_dwordx4 %[wv1], %[srdW], %[sW] offen offset:1024 lds\n"
      "v_mfma_f32_16x16x32_bf16 v[148:151], v[48:51], v[116:119], v[148:151]\n"
      "v_mfma_f32_16x16x32_bf16 v[148:151], v[32:35], v[84:87], v[148:151]\n"
      "v_mfma_f32_16x16x32_bf16 v[152:155], v[32:35], v[88:91], v[152:155]\n"
      "buffer_load_dwordx4 %[wv2], %[srdW], %[sW] offen offset:2048 lds\n"
      "v_mfma_f32_16x16x32_bf16 v[152:155], v[48:51], v[120:123], v[152:155]\n"
      "v_mfma_f32_16x16x32_bf16 v[184:187], v[52:55], v[120:123], v[184:187]\n"
      "v_mfma_f32_16x16x32_bf16 v[184:187], v[36:39], v[88:91], v[184:187]\n"
      "buffer_load_dwordx4 %[wv3], %[srdW], %[sW] offen offset:3072 lds\n"
      "v_mfma_f32_16x16x32_bf16 v[188:191], v[36:39], v[92:95], v[188:191]\n"
      "v_mfma_f32_16x16x32_bf16 v[188:191], v[52:55], v[124:127], v[188:191]\n"
      "v_mfma_f32_16x16x32_bf16 v[156:159], v[48:51], v[124:127], v[156:159]\n"
      "v_mfma_f32_16x16x32_bf16 v[156:159], v[32:35], v[92:95], v[156:159]\n"
      "s_waitcnt lgkmcnt(0)\n"
      "v_mfma_f32_16x16x32_bf16 v[192:195], v[40:43], v[64:67], v[192:195]\n"
      "s_waitcnt vmcnt(12)\n"
      "v_mfma_f32_16x16x32_bf16 v[192:195], v[56:59], v[96:99], v[192:195]\n"
      "s_barrier\n"
      "v_mfma_f32_16x16x32_bf16 v[224:227], v[60:63], v[96:99], v[224:227]\n"
      "ds_read_b64_tr_b16 v[32:33], %[wa0] offset:8192\n"
      "v_mfma_f32_16x16x32_bf16 v[224:227], v[44:47], v[64:67], v[224:227]\n"
      "ds_read_b128 v[64:67], %[xa0] offset:49152\n"
      "v_mfma_f32_16x16x32_bf16 v[228:231], v[44:47], v[68:71], v[228:231]\n"
      "ds_read_b128 v[96:99], %[xa1] offset:49152\n"
      "v_mfma_f32_16x16x32_bf16 v[228:231], v[60:63], v[100:103], v[228:231]\n"
      "ds_read_b64_tr_b16 v[34:35], %[wa0] offset:8704\n"
      "v_mfma_f32_16x16x32_bf16 v[196:199], v[56:59], v[100:103], v[196:199]\n"
      "ds_read_b64_tr_b16 v[36:37], %[wa1] offset:8192\n"
      "v_mfma_f32_16x16x32_bf16 v[196:199], v[40:43], v[68:71], v[196:199]\n"
      "ds_read_b128 v[68:71], %[xa0] offset:51200\n"
      "v_mfma_f32_16x16x32_bf16 v[200:203], v[40:43], v[72:75], v[200:203]\n"
      "ds_read_b128 v[100:103], %[xa1] offset:51200\n"
      "v_mfma_f32_16x16x32_bf16 v[200:203], v[56:59], v[104:107], v[200:203]\n"
      "ds_read_b64_tr_b16 v[38:39], %[wa1] offset:8704\n"
      "v_mfma_f32_16x16x32_bf16 v[232:235], v[60:63], v[104:107], v[232:235]\n"
      "s_waitcnt vmcnt(8)\n"
      "v_mfma_f32_16x16x32_bf16 v[232:235], v[44:47], v[72:75], v[232:235]\n"
      "ds_read_b128 v[72:75], %[xa0] offset:53248\n"
      "v_mfma_f32_16x16x32_bf16 v[236:239], v[44:47], v[76:79], v[236:239]\n"
      "ds_read_b128 v[104:107], %[xa1] offset:53248\n"
      "v_mfma_f32_16x16x32_bf16 v[236:239], v[60:63], v[108:111], v[236:239]\n"
      "ds_read_b64_tr_b16 v[48:49], %[wa0] offset:12288\n"
      "v_mfma_f32_16x16x32_bf16 v[204:207], v[56:59], v[108:111], v[204:207]\n"
      "ds_read_b64_tr_b16 v[50:51], %[wa0] offset:12800\n"
      "v_mfma_f32_16x16x32_bf16 v[204:207], v[40:43], v[76:79], v[204:207]\n"
      "ds_read_b128 v[76:79], %[xa0] offset:55296\n"
      "v_mfma_f32_16x16x32_bf16 v[208:211], v[40:43], v[80:83], v[208:211]\n"
      "ds_read_b128 v[108:111], %[xa1] offset:55296\n"
      "v_mfma_f32_16x16x32_bf16 v[208:211], v[56:59], v[112:115], v[208:211]\n"
      "ds_read_b64_tr_b16 v[52:53], %[wa1] offset:12288\n"
      "v_mfma_f32_16x16x32_bf16 v[240:243], v[60:63], v[112:115], v[240:243]\n"
      "ds_read_b64_tr_b16 v[54:55], %[wa1] offset:12800\n"
      "v_mfma_f32_16x16x32_bf16 v[240:243], v[44:47], v[80:83], v[240:243]\n"
      "ds_read_b128 v[80:83], %[xa0] offset:57344\n"
      "v_mfma_f32_16x16x32_bf16 v[244:247], v[44:47], v[84:87], v[244:247]\n"
      "ds_read_b128 v[112:115], %[xa1] offset:57344\n"
      "v_mfma_f32_16x16x32_bf16 v[244:247], v[60:63], v[116:119], v[244:247]\n"
      "v_mfma_f32_16x16x32_bf16 v[212:215], v[56:59], v[116:119], v[212:215]\n"
      "v_mfma_f32_16x16x32_bf16 v[212:215], v[40:43], v[84:87], v[212:215]\n"
      "ds_read_b128 v[84:87], %[xa0] offset:59392\n"
      "v_mfma_f32_16x16x32_bf16 v[216:219], v[40:43], v[88:91], v[216:219]\n"
      "ds_read_b128 v[116:119], %[xa1] offset:59392\n"
      "v_mfma_f32_16x16x32_bf16 v[216:219], v[56:59], v[120:123], v[216:219]\n"
      "v_mfma_f32_16x16x32_bf16 v[248:251], v[60:63], v[120:123], v[248:251]\n"
      "v_mfma_f32_16x16x32_bf16 v[248:251], v[44:47], v[88:91], v[248:251]\n"
      "ds_read_b128 v[88:91], %[xa0] offset:61440\n"
      "v_mfma_f32_16x16x32_bf16 v[252:255], v[44:47], v[92:95], v[252:255]\n"
      "ds_read_b128 v[120:123], %[xa1] offset:61440\n"
      "v_mfma_f32_16x16x32_bf16 v[252:255], v[60:63], v[124:127], v[252:255]\n"
      "v_mfma_f32_16x16x32_bf16 v[220:223], v[56:59], v[124:127], v[220:223]\n"
      "v_mfma_f32_16x16x32_bf16 v[220:223], v[40:43], v[92:95], v[220:223]\n"
      "ds_read_b128 v[92:95], %[xa0] offset:63488\n"
      "s_waitcnt lgkmcnt(7)\n"
      "v_mfma_f32_16x16x32_bf16 v[128:131], v[32:35], v[64:67], v[128:131]\n"
      "ds_read_b128 v[124:127], %[xa1] offset:63488\n"
      "v_mfma_f32_16x16x32_bf16 v[128:131], v[48:51], v[96:99], v[128:131]\n"
      "s_add_u32 m0, %[sWb], 0\n"
      "v_mfma_f32_16x16x32_bf16 v[160:163], v[52:55], v[96:99], v[160:163]\n"
      "s_add_u32 %[sW], %[sW], %[d0]\n"
      "v_mfma_f32_16x16x32_bf16 v[160:163], v[36:39], v[64:67], v[160:163]\n"
      "ds_read_b64_tr_b16 v[40:41], %[wa2] offset:8192\n"
      "v_mfma_f32_16x16x32_bf16 v[164:167], v[36:39], v[68:71], v[164:167]\n"
      "buffer_load_dwordx4 %[wv0], %[srdW], %[sW] offen offset:0 lds\n"
      "v_mfma_f32_16x16x32_bf16 v[164:167], v[52:55], v[100:103], v[164:167]\n"
      "ds_read_b64_tr_b16 v[42:43], %[wa2] offset:8704\n"
      "v_mfma_f32_16x16x32_bf16 v[132:135], v[48:51], v[100:103], v[132:135]\n"
      "ds_read_b64_tr_b16 v[56:57], %[wa2] offset:12288\n"
      "v_mfma_f32_16x16x32_bf16 v[132:135], v[32:35], v[68:71], v[132:135]\n"
      "buffer_load_dwordx4 %[wv1], %[srdW], %[sW] offen offset:1024 lds\n"
      "v_mfma_f32_16x16x32_bf16 v[136:139], v[32:35], v[72:75], v[136:139]\n"
      "ds_read_b64_tr_b16 v[58:59], %[wa2] offset:12800\n"
      "v_mfma_f32_16x16x32_bf16 v[136:139], v[48:51], v[104:107], v[136:139]\n"
      "ds_read_b64_tr_b16 v[44:45], %[wa3] offset:8192\n"
      "v_mfma_f32_16x16x32_bf16 v[168:171], v[52:55], v[104:107], v[168:171]\n"
      "buffer_load_dwordx4 %[wv2], %[srdW], %[sW] offen offset:2048 lds\n"
      "v_mfma_f32_16x16x32_bf16 v[168:171], v[36:39], v[72:75], v[168:171]\n"
      "ds_read_b64_tr_b16 v[46:47], %[wa3] offset:8704\n"
      "v_mfma_f32_16x16x32_bf16 v[172:175], v[36:39], v[76:79], v[172:175]\n"
      "ds_read_b64_tr_b16 v[60:61], %[wa3] offset:12288\n"
      "v_mfma_f32_16x16x32_bf16 v[172:175], v[52:55], v[108:111], v[172:175]\n"
      "buffer_load_dwordx4 %[wv3], %[srdW], %[sW] offen offset:3072 lds\n"
      "v_mfma_f32_16x16x32_bf16 v[140:143], v[48:51], v[108:111], v[140:143]\n"
      "ds_read_b64_tr_b16 v[62:63], %[wa3] offset:12800\n"
      "v_mfma_f32_16x16x32_bf16 v[140:143], v[32:35], v[76:79], v[140:143]\n"
      "s_add_u32 m0, %[sWb], 4096\n"
      "s_waitcnt lgkmcnt(8)\n"
      "v_mfma_f32_16x16x32_bf16 v[144:147], v[32:35], v[80:83], v[144:147]\n"
      "s_add_u32 %[sW], %[sW], 0x8000\n"
      "v_mfma_f32_16x16x32_bf16 v[144:147], v[48:51], v[112:115], v[144:147]\n"
      "v_mfma_f32_16x16x32_bf16 v[176:179], v[52:55], v[112:115], v[176:179]\n"
      "buffer_load_dwordx4 %[wv0], %[srdW], %[sW] offen offset:0 lds\n"
      "v_mfma_f32_16x16x32_bf16 v[176:179], v[36:39], v[80:83], v[176:179]\n"
      "v_mfma_f32_16x16x32_bf16 v[180:183], v[36:39], v[84:87], v[180:183]\n"
      "v_mfma_f32_16x16x32_bf16 v[180:183], v[52:55], v[116:119], v[180:183]\n"
      "buffer_load_dwordx4 %[wv1], %[srdW], %[sW] offen offset:1024 lds\n"
      "v_mfma_f32_16x16x32_bf16 v[148:151], v[48:51], v[116:119], v[148:151]\n"
      "v_mfma_f32_16x16x32_bf16 v[148:151], v[32:35], v[84:87], v[148:151]\n"
      "v_mfma_f32_16x16x32_bf16 v[152:155], v[32:35], v[88:91], v[152:155]\n"
      "buffer_load_dwordx4 %[wv2], %[srdW], %[sW] offen offset:2048 lds\n"
      "v_mfma_f32_16x16x32_bf16 v[152:155], v[48:51], v[120:123], v[152:155]\n"
      "v_mfma_f32_16x16x32_bf16 v[184:187], v[52:55], v[120:123], v[184:187]\n"
      "v_mfma_f32_16x16x32_bf16 v[184:187], v[36:39], v[88:91], v[184:187]\n"
      "buffer_load_dwordx4 %[wv3], %[srdW], %[sW] offen offset:3072 lds\n"
      "v_mfma_f32_16x16x32_bf16 v[188:191], v[36:39], v[92:95], v[188:191]\n"
      "v_mfma_f32_16x16x32_bf16 v[188:191], v[52:55], v[124:127], v[188:191]\n"
      "v_mfma_f32_16x16x32_bf16 v[156:159], v[48:51], v[124:127], v[156:159]\n"
      "v_mfma_f32_16x16x32_bf16 v[156:159], v[32:35], v[92:95], v[156:159]\n"
      "s_waitcnt lgkmcnt(0)\n"
      "v_mfma_f32_16x16x32_bf16 v[192:195], v[40:43], v[64:67], v[192:195]\n"
      "s_waitcnt vmcnt(12)\n"
      "v_mfma_f32_16x16x32_bf16 v[192:195], v[56:59], v[96:99], v[192:195]\n"
      "s_barrier\n"
      "v_mfma_f32_16x16x32_bf16 v[224:227], v[60:63], v[96:99], v[224:227]\n"
      "ds_read_b64_tr_b16 v[32:33], %[wa0] offset:16384\n"
      "v_mfma_f32_16x16x32_bf16 v[224:227], v[44:47], v[64:67], v[224:227]\n"
      "ds_read_b128 v[64:67], %[xa0] offset:0\n"
      "v_mfma_f32_16x16x32_bf16 v[228:231], v[44:47], v[68:71], v[228:231]\n"
      "ds_read_b128 v[96:99], %[xa1] offset:0\n"
      "v_mfma_f32_16x16x32_bf16 v[228:231], v[60:63], v[100:103], v[228:231]\n"
      "ds_read_b64_tr_b16 v[34:35], %[wa0] offset:16896\n"
      "v_mfma_f32_16x16x32_bf16 v[196:199], v[56:59], v[100:103], v[196:199]\n"
      "ds_read_b64_tr_b16 v[36:37], %[wa1] offset:16384\n"
      "v_mfma_f32_16x16x32_bf16 v[196:199], v[40:43], v[68:71], v[196:199]\n"
      "ds_read_b128 v[68:71], %[xa0] offset:2048\n"
      "v_mfma_f32_16x16x32_bf16 v[200:203], v[40:43], v[72:75], v[200:203]\n"
      "ds_read_b128 v[100:103], %[xa1] offset:2048\n"
      "v_mfma_f32_16x16x32_bf16 v[200:203], v[56:59], v[104:107], v[200:203]\n"
      "ds_read_b64_tr_b16 v[38:39], %[wa1] offset:16896\n"
      "v_mfma_f32_16x16x32_bf16 v[232:235], v[60:63], v[104:107], v[232:235]\n"
      "s_waitcnt vmcnt(8)\n"
      "v_mfma_f32_16x16x32_bf16 v[232:235], v[44:47], v[72:75], v[232:235]\n"
      "ds_read_b128 v[72:75], %[xa0] offset:4096\n"
      "v_mfma_f32_16x16x32_bf16 v[236:239], v[44:47], v[76:79], v[236:239]\n"
      "ds_read_b128 v[104:107], %[xa1] offset:4096\n"
      "v_mfma_f32_16x16x32_bf16 v[236:239], v[60:63], v[108:111], v[236:239]\n"
      "ds_read_b64_tr_b16 v[48:49], %[wa0] offset:20480\n"
      "v_mfma_f32_16x16x32_bf16 v[204:207], v[56:59], v[108:111], v[204:207]\n"
      "ds_read_b64_tr_b16 v[50:51], %[wa0] offset:20992\n"
      "v_mfma_f32_16x16x32_bf16 v[204:207], v[40:43], v[76:79], v[204:207]\n"
      "ds_read_b128 v[76:79], %[xa0] offset:6144\n"
      "v_mfma_f32_16x16x32_bf16 v[208:211], v[40:43], v[80:83], v[208:211]\n"
      "ds_read_b128 v[108:111], %[xa1] offset:6144\n"
      "v_mfma_f32_16x16x32_bf16 v[208:211], v[56:59], v[112:115], v[208:211]\n"
      "ds_read_b64_tr_b16 v[52:53], %[wa1] offset:20480\n"
      "v_mfma_f32_16x16x32_bf16 v[240:243], v[60:63], v[112:115], v[240:243]\n"
      "ds_read_b64_tr_b16 v[54:55], %[wa1] offset:20992\n"
      "v_mfma_f32_16x16x32_bf16 v[240:243], v[44:47], v[80:83], v[240:243]\n"
      "ds_read_b128 v[80:83], %[xa0] offset:8192\n"
      "v_mfma_f32_16x16x32_bf16 v[244:247], v[44:47], v[84:87], v[244:247]\n"
      "ds_read_b128 v[112:115], %[xa1] offset:8192\n"
      "v_mfma_f32_16x16x32_bf16 v[244:247], v[60:63], v[116:119], v[244:247]\n"
      "v_mfma_f32_16x16x32_bf16 v[212:215], v[56:59], v[116:119], v[212:215]\n"
      "v_mfma_f32_16x16x32_bf16 v[212:215], v[40:43], v[84:87], v[212:215]\n"
      "ds_read_b128 v[84:87], %[xa0] offset:10240\n"
      "v_mfma_f32_16x16x32_bf16 v[216:219], v[40:43], v[88:91], v[216:219]\n"
      "ds_read_b128 v[116:119], %[xa1] offset:10240\n"
      "v_mfma_f32_16x16x32_bf16 v[216:219], v[56:59], v[120:123], v[216:219]\n"
      "v_mfma_f32_16x16x32_bf16 v[248:251], v[60:63], v[120:123], v[248:251]\n"
      "v_mfma_f32_16x16x32_bf16 v[248:251], v[44:47], v[88:91], v[248:251]\n"
      "ds_read_b128 v[88:91], %[xa0] offset:12288\n"
      "v_mfma_f32_16x16x32_bf16 v[252:255], v[44:47], v[92:95], v[252:255]\n"
      "ds_read_b128 v[120:123], %[xa1] offset:12288\n"
      "v_mfma_f32_16x16x32_bf16 v[252:255], v[60:63], v[124:127], v[252:255]\n"
      "v_mfma_f32_16x16x32_bf16 v[220:223], v[56:59], v[124:127], v[220:223]\n"
      "v_mfma_f32_16x16x32_bf16 v[220:223], v[40:43], v[92:95], v[220:223]\n"
      "ds_read_b128 v[92:95], %[xa0] offset:14336\n"
      "s_waitcnt lgkmcnt(7)\n"
      "v_mfma_f32_16x16x32_bf16 v[128:131], v[32:35], v[64:67], v[128:131]\n"
      "ds_read_b128 v[124:127], %[xa1] offset:14336\n"
      "v_mfma_f32_16x16x32_bf16 v[128:131], v[48:51], v[96:99], v[128:131]\n"
      "s_add_u32 m0, %[sWb], 8192\n"
      "v_mfma_f32_16x16x32_bf16 v[160:163], v[52:55], v[96:99], v[160:163]\n"
      "s_add_u32 %[sW], %[sW], %[d1]\n"
      "v_mfma_f32_16x16x32_bf16 v[160:163], v[36:39], v[64:67], v[160:163]\n"
      "ds_read_b64_tr_b16 v[40:41], %[wa2] offset:16384\n"
      "v_mfma_f32_16x16x32_bf16 v[164:167], v[36:39], v[68:71], v[164:167]\n"
      "buffer_load_dwordx4 %[wv0], %[srdW], %[sW] offen offset:0 lds\n"
      "v_mfma_f32_16x16x32_bf16 v[164:167], v[52:55], v[100:103], v[164:167]\n"
      "ds_read_b64_tr_b16 v[42:43], %[wa2] offset:16896\n"
      "v_mfma_f32_16x16x32_bf16 v[132:135], v[48:51], v[100:103], v[132:135]\n"
      "ds_read_b64_tr_b16 v[56:57], %[wa2] offset:20480\n"
      "v_mfma_f32_16x16x32_bf16 v[132:135], v[32:35], v[68:71], v[132:135]\n"
      "buffer_load_dwordx4 %[wv1], %[srdW], %[sW] offen offset:1024 lds\n"
      "v_mfma_f32_16x16x32_bf16 v[136:139], v[32:35], v[72:75], v[136:139]\n"
      "ds_read_b64_tr_b16 v[58:59], %[wa2] offset:20992\n"
      "v_mfma_f32_16x16x32_bf16 v[136:139], v[48:51], v[104:107], v[136:139]\n"
      "ds_read_b64_tr_b16 v[44:45], %[wa3] offset:16384\n"
      "v_mfma_f32_16x16x32_bf16 v[168:171], v[52:55], v[104:107], v[168:171]\n"
      "buffer_load_dwordx4 %[wv2], %[srdW], %[sW] offen offset:2048 lds\n"
      "v_mfma_f32_16x16x32_bf16 v[168:171], v[36:39], v[72:75], v[168:171]\n"
      "ds_read_b64_tr_b16 v[46:47], %[wa3] offset:16896\n"
      "v_mfma_f32_16x16x32_bf16 v[172:175], v[36:39], v[76:79], v[172:175]\n"
      "ds_read_b64_tr_b16 v[60:61], %[wa3] offset:20480\n"
      "v_mfma_f32_16x16x32_bf16 v[172:175], v[52:55], v[108:111], v[172:175]\n"
      "buffer_load_dwordx4 %[wv3], %[srdW], %[sW] offen offset:3072 lds\n"
      "v_mfma_f32_16x16x32_bf16 v[140:143], v[48:51], v[108:111], v[140:143]\n"
      "ds_read_b64_tr_b16 v[62:63], %[wa3] offset:20992\n"
      "v_mfma_f32_16x16x32_bf16 v[140:143], v[32:35], v[76:79], v[140:143]\n"
      "s_add_u32 m0, %[sWb], 12288\n"
      "s_waitcnt lgkmcnt(8)\n"
      "v_mfma_f32_16x16x32_bf16 v[144:147], v[32:35], v[80:83], v[144:147]\n"
      "s_add_u32 %[sW], %[sW], 0x8000\n"
      "v_mfma_f32_16x16x32_bf16 v[144:147], v[48:51], v[112:115], v[144:147]\n"
      "v_mfma_f32_16x16x32_bf16 v[176:179], v[52:55], v[112:115], v[176:179]\n"
      "buffer_load_dwordx4 %[wv0], %[srdW], %[sW] offen offset:0 lds\n"
      "v_mfma_f32_16x16x32_bf16 v[176:179], v[36:39], v[80:83], v[176:179]\n"
      "v_mfma_f32_16x16x32_bf16 v[180:183], v[36:39], v[84:87], v[180:183]\n"
      "v_mfma_f32_16x16x32_bf16 v[180:183], v[52:55], v[116:119], v[180:183]\n"
      "buffer_load_dwordx4 %[wv1], %[srdW], %[sW] offen offset:1024 lds\n"
      "v_mfma_f32_16x16x32_bf16 v[148:151], v[48:51], v[116:119], v[148:151]\n"
      "v_mfma_f32_16x16x32_bf16 v[148:151], v[32:35], v[84:87], v[148:151]\n"
      "v_mfma_f32_16x16x32_bf16 v[152:155], v[32:35], v[88:91], v[152:155]\n"
      "buffer_load_dwordx4 %[wv2], %[srdW], %[sW] offen offset:2048 lds\n"
      "v_mfma_f32_16x16x32_bf16 v[152:155], v[48:51], v[120:123], v[152:155]\n"
      "v_mfma_f32_16x16x32_bf16 v[184:187], v[52:55], v[120:123], v[184:187]\n"
      "v_mfma_f32_16x16x32_bf16 v[184:187], v[36:39], v[88:91], v[184:187]\n"
      "buffer_load_dwordx4 %[wv3], %[srdW], %[sW] offen offset:3072 lds\n"
      "v_mfma_f32_16x16x32_bf16 v[188:191], v[36:39], v[92:95], v[188:191]\n"
      "v_mfma_f32_16x16x32_bf16 v[188:191], v[52:55], v[124:127], v[188:191]\n"
      "v_mfma_f32_16x16x32_bf16 v[156:159], v[48:51], v[124:127], v[156:159]\n"
      "v_mfma_f32_16x16x32_bf16 v[156:159], v[32:35], v[92:95], v[156:159]\n"
      "s_waitcnt lgkmcnt(0)\n"
      "v_mfma_f32_16x16x32_bf16 v[192:195], v[40:43], v[64:67], v[192:195]\n"
      "s_waitcnt vmcnt(12)\n"
      "v_mfma_f32_16x16x32_bf16 v[192:195], v[56:59], v[96:99], v[192:195]\n"
      "s_barrier\n"
      "v_mfma_f32_16x16x32_bf16 v[224:227], v[60:63], v[96:99], v[224:227]\n"
      "ds_read_b64_tr_b16 v[32:33], %[wa0] offset:0\n"
      "v_mfma_f32_16x16x32_bf16 v[224:227], v[44:47], v[64:67], v[224:227]\n"
      "ds_read_b128 v[64:67], %[xa0] offset:16384\n"
      "v_mfma_f32_16x16x32_bf16 v[228:231], v[44:47], v[68:71], v[228:231]\n"
      "ds_read_b128 v[96:99], %[xa1] offset:16384\n"
      "v_mfma_f32_16x16x32_bf16 v[228:231], v[60:63], v[100:103], v[228:231]\n"
      "ds_read_b64_tr_b16 v[34:35], %[wa0] offset:512\n"
      "v_mfma_f32_16x16x32_bf16 v[196:199], v[56:59], v[100:103], v[196:199]\n"
      "ds_read_b64_tr_b16 v[36:37], %[wa1] offset:0\n"
      "v_mfma_f32_16x16x32_bf16 v[196:199], v[40:43], v[68:71], v[196:199]\n"
      "ds_read_b128 v[68:71], %[xa0] offset:18432\n"
      "v_mfma_f32_16x16x32_bf16 v[200:203], v[40:43], v[72:75], v[200:203]\n"
      "ds_read_b128 v[100:103], %[xa1] offset:18432\n"
      "v_mfma_f32_16x16x32_bf16 v[200:203], v[56:59], v[104:107], v[200:203]\n"
      "ds_read_b64_tr_b16 v[38:39], %[wa1] offset:512\n"
      "v_mfma_f32_16x16x32_bf16 v[232:235], v[60:63], v[104:107], v[232:235]\n"
      "s_waitcnt vmcnt(8)\n"
      "v_mfma_f32_16x16x32_bf16 v[232:235], v[44:47], v[72:75], v[232:235]\n"
      "ds_read_b128 v[72:75], %[xa0] offset:20480\n"
      "v_mfma_f32_16x16x32_bf16 v[236:239], v[44:47], v[76:79], v[236:239]\n"
      "ds_read_b128 v[104:107], %[xa1] offset:20480\n"
      "v_mfma_f32_16x16x32_bf16 v[236:239], v[60:63], v[108:111], v[236:239]\n"
      "ds_read_b64_tr_b16 v[48:49], %[wa0] offset:4096\n"
      "v_mfma_f32_16x16x32_bf16 v[204:207], v[56:59], v[108:111], v[204:207]\n"
      "ds_read_b64_tr_b16 v[50:51], %[wa0] offset:4608\n"
      "v_mfma_f32_16x16x32_bf16 v[204:207], v[40:43], v[76:79], v[204:207]\n"
      "ds_read_b128 v[76:79], %[xa0] offset:22528\n"
      "v_mfma_f32_16x16x32_bf16 v[208:211], v[40:43], v[80:83], v[208:211]\n"
      "ds_read_b128 v[108:111], %[xa1] offset:22528\n"
      "v_mfma_f32_16x16x32_bf16 v[208:211], v[56:59], v[112:115], v[208:211]\n"
      "ds_read_b64_tr_b16 v[52:53], %[wa1] offset:4096\n"
      "v_mfma_f32_16x16x32_bf16 v[240:243], v[60:63], v[112:115], v[240:243]\n"
      "ds_read_b64_tr_b16 v[54:55], %[wa1] offset:4608\n"
      "v_mfma_f32_16x16x32_bf16 v[240:243], v[44:47], v[80:83], v[240:243]\n"
      "ds_read_b128 v[80:83], %[xa0] offset:24576\n"
      "v_mfma_f32_16x16x32_bf16 v[244:247], v[44:47], v[84:87], v[244:247]\n"
      "ds_read_b128 v[112:115], %[xa1] offset:24576\n"
      "v_mfma_f32_16x16x32_bf16 v[244:247], v[60:63], v[116:119], v[244:247]\n"
      "v_mfma_f32_16x16x32_bf16 v[212:215], v[56:59], v[116:119], v[212:215]\n"
      "v_mfma_f32_16x16x32_bf16 v[212:215], v[40:43], v[84:87], v[212:215]\n"
      "ds_read_b128 v[84:87], %[xa0] offset:26624\n"
      "v_mfma_f32_16x16x32_bf16 v[216:219], v[40:43], v[88:91], v[216:219]\n"
      "ds_read_b128 v[116:119], %[xa1] offset:26624\n"
      "v_mfma_f32_16x16x32_bf16 v[216:219], v[56:59], v[120:123], v[216:219]\n"
      "v_mfma_f32_16x16x32_bf16 v[248:251], v[60:63], v[120:123], v[248:251]\n"
      "v_mfma_f32_16x16x32_bf16 v[248:251], v[44:47], v[88:91], v[248:251]\n"
      "ds_read_b128 v[88:91], %[xa0] offset:28672\n"
      "v_mfma_f32_16x16x32_bf16 v[252:255], v[44:47], v[92:95], v[252:255]\n"
      "ds_read_b128 v[120:123], %[xa1] offset:28672\n"
      "v_mfma_f32_16x16x32_bf16 v[252:255], v[60:63], v[124:127], v[252:255]\n"
      "v_mfma_f32_16x16x32_bf16 v[220:223], v[56:59], v[124:127], v[220:223]\n"
      "v_mfma_f32_16x16x32_bf16 v[220:223], v[40:43], v[92:95], v[220:223]\n"
      "ds_read_b128 v[92:95], %[xa0] offset:30720\n"
      "s_waitcnt lgkmcnt(7)\n"
      "v_mfma_f32_16x16x32_bf16 v[128:131], v[32:35], v[64:67], v[128:131]\n"
      "ds_read_b128 v[124:127], %[xa1] offset:30720\n"
      "v_mfma_f32_16x16x32_bf16 v[128:131], v[48:51], v[96:99], v[128:131]\n"
      "s_add_u32 m0, %[sWb], 16384\n"
      "v_mfma_f32_16x16x32_bf16 v[160:163], v[52:55], v[96:99], v[160:163]\n"
      "s_add_u32 %[sW], %[sW], %[d0]\n"
      "v_mfma_f32_16x16x32_bf16 v[160:163], v[36:39], v[64:67], v[160:163]\n"
      "ds_read_b64_tr_b16 v[40:41], %[wa2] offset:0\n"
      "v_mfma_f32_16x16x32_bf16 v[164:167], v[36:39], v[68:71], v[164:167]\n"
      "buffer_load_dwordx4 %[wv0], %[srdW], %[sW] offen offset:0 lds\n"
      "v_mfma_f32_16x16x32_bf16 v[164:167], v[52:55], v[100:103], v[164:167]\n"
      "ds_read_b64_tr_b16 v[42:43], %[wa2] offset:512\n"
      "v_mfma_f32_16x16x32_bf16 v[132:135], v[48:51], v[100:103], v[132:135]\n"
      "ds_read_b64_tr_b16 v[56:57], %[wa2] offset:4096\n"
      "v_mfma_f32_16x16x32_bf16 v[132:135], v[32:35], v[68:71], v[132:135]\n"
      "buffer_load_dwordx4 %[wv1], %[srdW], %[sW] offen offset:1024 lds\n"
      "v_mfma_f32_16x16x32_bf16 v[136:139], v[32:35], v[72:75], v[136:139]\n"
      "ds_read_b64_tr_b16 v[58:59], %[wa2] offset:4608\n"
      "v_mfma_f32_16x16x32_bf16 v[136:139], v[48:51], v[104:107], v[136:139]\n"
      "ds_read_b64_tr_b16 v[44:45], %[wa3] offset:0\n"
      "v_mfma_f32_16x16x32_bf16 v[168:171], v[52:55], v[104:107], v[168:171]\n"
      "buffer_load_dwordx4 %[wv2], %[srdW], %[sW] offen offset:2048 lds\n"
      "v_mfma_f32_16x16x32_bf16 v[168:171], v[36:39], v[72:75], v[168:171]\n"
      "ds_read_b64_tr_b16 v[46:47], %[wa3] offset:512\n"
      "v_mfma_f32_16x16x32_bf16 v[172:175], v[36:39], v[76:79], v[172:175]\n"
      "ds_read_b64_tr_b16 v[60:61], %[wa3] offset:4096\n"
      "v_mfma_f32_16x16x32_bf16 v[172:175], v[52:55], v[108:111], v[172:175]\n"
      "buffer_load_dwordx4 %[wv3], %[srdW], %[sW] offen offset:3072 lds\n"
      "v_mfma_f32_16x16x32_bf16 v[140:143], v[48:51], v[108:111], v[140:143]\n"
      "ds_read_b64_tr_b16 v[62:63], %[wa3] offset:4608\n"
      "v_mfma_f32_16x16x32_bf16 v[140:143], v[32:35], v[76:79], v[140:143]\n"
      "s_add_u32 m0, %[sWb], 20480\n"
      "s_waitcnt lgkmcnt(8)\n"
      "v_mfma_f32_16x16x32_bf16 v[144:147], v[32:35], v[80:83], v[144:147]\n"
      "s_add_u32 %[sW], %[sW], 0x8000\n"
      "v_mfma_f32_16x16x32_bf16 v[144:147], v[48:51], v[112:115], v[144:147]\n"
      "v_mfma_f32_16x16x32_bf16 v[176:179], v[52:55], v[112:115], v[176:179]\n"
      "buffer_load_dwordx4 %[wv0], %[srdW], %[sW] offen offset:0 lds\n"
      "v_mfma_f32_16x16x32_bf16 v[176:179], v[36:39], v[80:83], v[176:179]\n"
      "v_mfma_f32_16x16x32_bf16 v[180:183], v[36:39], v[84:87], v[180:183]\n"
      "v_mfma_f32_16x16x32_bf16 v[180:183], v[52:55], v[116:119], v[180:183]\n"
      "buffer_load_dwordx4 %[wv1], %[srdW], %[sW] offen offset:1024 lds\n"
      "v_mfma_f32_16x16x32_bf16 v[148:151], v[48:51], v[116:119], v[148:151]\n"
      "v_mfma_f32_16x16x32_bf16 v[148:151], v[32:35], v[84:87], v[148:151]\n"
      "v_mfma_f32_16x16x32_bf16 v[152:155], v[32:35], v[88:91], v[152:155]\n"
      "buffer_load_dwordx4 %[wv2], %[srdW], %[sW] offen offset:2048 lds\n"
      "v_mfma_f32_16x16x32_bf16 v[152:155], v[48:51], v[120:123], v[152:155]\n"
      "v_mfma_f32_16x16x32_bf16 v[184:187], v[52:55], v[120:123], v[184:187]\n"
      "v_mfma_f32_16x16x32_bf16 v[184:187], v[36:39], v[88:91], v[184:187]\n"
      "buffer_load_dwordx4 %[wv3], %[srdW], %[sW] offen offset:3072 lds\n"
      "v_mfma_f32_16x16x32_bf16 v[188:191], v[36:39], v[92:95], v[188:191]\n"
      "v_mfma_f32_16x16x32_bf16 v[188:191], v[52:55], v[124:127], v[188:191]\n"
      "v_mfma_f32_16x16x32_bf16 v[156:159], v[48:51], v[124:127], v[156:159]\n"
      "v_mfma_f32_16x16x32_bf16 v[156:159], v[32:35], v[92:95], v[156:159]\n"
      "s_waitcnt lgkmcnt(0)\n"
      "v_mfma_f32_16x16x32_bf16 v[192:195], v[40:43], v[64:67], v[192:195]\n"
      "s_waitcnt vmcnt(12)\n"
      "v_mfma_f32_16x16x32_bf16 v[192:195], v[56:59], v[96:99], v[192:195]\n"
      "s_barrier\n"
      "v_mfma_f32_16x16x32_bf16 v[224:227], v[60:63], v[96:99], v[224:227]\n"
      "ds_read_b64_tr_b16 v[32:33], %[wa0] offset:8192\n"
      "v_mfma_f32_16x16x32_bf16 v[224:227], v[44:47], v[64:67], v[224:227]\n"
      "ds_read_b128 v[64:67], %[xa0] offset:32768\n"
      "v_mfma_f32_16x16x32_bf16 v[228:231], v[44:47], v[68:71], v[228:231]\n"
      "ds_read_b128 v[96:99], %[xa1] offset:32768\n"
      "v_mfma_f32_16x16x32_bf16 v[228:231], v[60:63], v[100:103], v[228:231]\n"
      "ds_read_b64_tr_b16 v[34:35], %[wa0] offset:8704\n"
      "v_mfma_f32_16x16x32_bf16 v[196:199], v[56:59], v[100:103], v[196:199]\n"
      "ds_read_b64_tr_b16 v[36:37], %[wa1] offset:8192\n"
      "v_mfma_f32_16x16x32_bf16 v[196:199], v[40:43], v[68:71], v[196:199]\n"
      "ds_read_b128 v[68:71], %[xa0] offset:34816\n"
      "v_mfma_f32_16x16x32_bf16 v[200:203], v[40:43], v[72:75], v[200:203]\n"
      "ds_read_b128 v[100:103], %[xa1] offset:34816\n"
      "v_mfma_f32_16x16x32_bf16 v[200:203], v[56:59], v[104:107], v[200:203]\n"
      "ds_read_b64_tr_b16 v[38:39], %[wa1] offset:8704\n"
      "v_mfma_f32_16x16x32_bf16 v[232:235], v[60:63], v[104:107], v[232:235]\n"
      "s_waitcnt vmcnt(8)\n"
      "v_mfma_f32_16x16x32_bf16 v[232:235], v[44:47], v[72:75], v[232:235]\n"
      "ds_read_b128 v[72:75], %[xa0] offset:36864\n"
      "v_mfma_f32_16x16x32_bf16 v[236:239], v[44:47], v[76:79], v[236:239]\n"
      "ds_read_b128 v[104:107], %[xa1] offset:36864\n"
      "v_mfma_f32_16x16x32_bf16 v[236:239], v[60:63], v[108:111], v[236:239]\n"
      "ds_read_b64_tr_b16 v[48:49], %[wa0] offset:12288\n"
      "v_mfma_f32_16x16x32_bf16 v[204:207], v[56:59], v[108:111], v[204:207]\n"
      "ds_read_b64_tr_b16 v[50:51], %[wa0] offset:12800\n"
      "v_mfma_f32_16x16x32_bf16 v[204:207], v[40:43], v[76:79], v[204:207]\n"
      "ds_read_b128 v[76:79], %[xa0] offset:38912\n"
      "v_mfma_f32_16x16x32_bf16 v[208:211], v[40:43], v[80:83], v[208:211]\n"
      "ds_read_b128 v[108:111], %[xa1] offset:38912\n"
      "v_mfma_f32_16x16x32_bf16 v[208:211], v[56:59], v[112:115], v[208:211]\n"
      "ds_read_b64_tr_b16 v[52:53], %[wa1] offset:12288\n"
      "v_mfma_f32_16x16x32_bf16 v[240:243], v[60:63], v[112:115], v[240:243]\n"
      "ds_read_b64_tr_b16 v[54:55], %[wa1] offset:12800\n"
      "v_mfma_f32_16x16x32_bf16 v[240:243], v[44:47], v[80:83], v[240:243]\n"
      "ds_read_b128 v[80:83], %[xa0] offset:40960\n"
      "v_mfma_f32_16x16x32_bf16 v[244:247], v[44:47], v[84:87], v[244:247]\n"
      "ds_read_b128 v[112:115], %[xa1] offset:40960\n"
      "v_mfma_f32_16x16x32_bf16 v[244:247], v[60:63], v[116:119], v[244:247]\n"
      "v_mfma_f32_16x16x32_bf16 v[212:215], v[56:59], v[116:119], v[212:215]\n"
      "v_mfma_f32_16x16x32_bf16 v[212:215], v[40:43], v[84:87], v[212:215]\n"
      "ds_read_b128 v[84:87], %[xa0] offset:43008\n"
      "v_mfma_f32_16x16x32_bf16 v[216:219], v[40:43], v[88:91], v[216:219]\n"
      "ds_read_b128 v[116:119], %[xa1] offset:43008\n"
      "v_mfma_f32_16x16x32_bf16 v[216:219], v[56:59], v[120:123], v[216:219]\n"
      "v_mfma_f32_16x16x32_bf16 v[248:251], v[60:63], v[120:123], v[248:251]\n"
      "v_mfma_f32_16x16x32_bf16 v[248:251], v[44:47], v[88:91], v[248:251]\n"
      "ds_read_b128 v[88:91], %[xa0] offset:45056\n"
      "v_mfma_f32_16x16x32_bf16 v[252:255], v[44:47], v[92:95], v[252:255]\n"
      "ds_read_b128 v[120:123], %[xa1] offset:45056\n"
      "v_mfma_f32_16x16x32_bf16 v[252:255], v[60:63], v[124:127], v[252:255]\n"
      "v_mfma_f32_16x16x32_bf16 v[220:223], v[56:59], v[124:127], v[220:223]\n"
      "v_mfma_f32_16x16x32_bf16 v[220:223], v[40:43], v[92:95], v[220:223]\n"
      "ds_read_b128 v[92:95], %[xa0] offset:47104\n"
      "s_waitcnt lgkmcnt(7)\n"
      "v_mfma_f32_16x16x32_bf16 v[128:131], v[32:35], v[64:67], v[128:131]\n"
      "ds_read_b128 v[124:127], %[xa1] offset:47104\n"
      "v_mfma_f32_16x16x32_bf16 v[128:131], v[48:51], v[96:99], v[128:131]\n"
      "s_add_u32 m0, %[sWb], 0\n"
      "v_mfma_f32_16x16x32_bf16 v[160:163], v[52:55], v[96:99], v[160:163]\n"
      "s_add_u32 %[sW], %[sW], %[d1]\n"
      "v_mfma_f32_16x16x32_bf16 v[160:163], v[36:39], v[64:67], v[160:163]\n"
      "ds_read_b64_tr_b16 v[40:41], %[wa2] offset:8192\n"
      "v_mfma_f32_16x16x32_bf16 v[164:167], v[36:39], v[68:71], v[164:167]\n"
      "buffer_load_dwordx4 %[wv0], %[srdW], %[sW] offen offset:0 lds\n"
      "v_mfma_f32_16x16x32_bf16 v[164:167], v[52:55], v[100:103], v[164:167]\n"
      "ds_read_b64_tr_b16 v[42:43], %[wa2] offset:8704\n"
      "v_mfma_f32_16x16x32_bf16 v[132:135], v[48:51], v[100:103], v[132:135]\n"
      "ds_read_b64_tr_b16 v[56:57], %[wa2] offset:12288\n"
      "v_mfma_f32_16x16x32_bf16 v[132:135], v[32:35], v[68:71], v[132:135]\n"
      "buffer_load_dwordx4 %[wv1], %[srdW], %[sW] offen offset:1024 lds\n"
      "v_mfma_f32_16x16x32_bf16 v[136:139], v[32:35], v[72:75], v[136:139]\n"
      "ds_read_b64_tr_b16 v[58:59], %[wa2] offset:12800\n"
      "v_mfma_f32_16x16x32_bf16 v[136:139], v[48:51], v[104:107], v[136:139]\n"
      "ds_read_b64_tr_b16 v[44:45], %[wa3] offset:8192\n"
      "v_mfma_f32_16x16x32_bf16 v[168:171], v[52:55], v[104:107], v[168:171]\n"
      "buffer_load_dwordx4 %[wv2], %[srdW], %[sW] offen offset:2048 lds\n"
      "v_mfma_f32_16x16x32_bf16 v[168:171], v[36:39], v[72:75], v[168:171]\n"
      "ds_read_b64_tr_b16 v[46:47], %[wa3] offset:8704\n"
      "v_mfma_f32_16x16x32_bf16 v[172:175], v[36:39], v[76:79], v[172:175]\n"
      "ds_read_b64_tr_b16 v[60:61], %[wa3] offset:12288\n"
      "v_mfma_f32_16x16x32_bf16 v[172:175], v[52:55], v[108:111], v[172:175]\n"
      "buffer_load_dwordx4 %[wv3], %[srdW], %[sW] offen offset:3072 lds\n"
      "v_mfma_f32_16x16x32_bf16 v[140:143], v[48:51], v[108:111], v[140:143]\n"
      "ds_read_b64_tr_b16 v[62:63], %[wa3] offset:12800\n"
      "v_mfma_f32_16x16x32_bf16 v[140:143], v[32:35], v[76:79], v[140:143]\n"
      "s_add_u32 m0, %[sWb], 4096\n"
      "s_waitcnt lgkmcnt(8)\n"
      "v_mfma_f32_16x16x32_bf16 v[144:147], v[32:35], v[80:83], v[144:147]\n"
      "s_add_u32 %[sW], %[sW], 0x8000\n"
      "v_mfma_f32_16x16x32_bf16 v[144:147], v[48:51], v[112:115], v[144:147]\n"
      "v_mfma_f32_16x16x32_bf16 v[176:179], v[52:55], v[112:115], v[176:179]\n"
      "buffer_load_dwordx4 %[wv0], %[srdW], %[sW] offen offset:0 lds\n"
      "v_mfma_f32_16x16x32_bf16 v[176:179], v[36:39], v[80:83], v[176:179]\n"
      "v_mfma_f32_16x16x32_bf16 v[180:183], v[36:39], v[84:87], v[180:183]\n"
      "v_mfma_f32_16x16x32_bf16 v[180:183], v[52:55], v[116:119], v[180:183]\n"
      "buffer_load_dwordx4 %[wv1], %[srdW], %[sW] offen offset:1024 lds\n"
      "v_mfma_f32_16x16x32_bf16 v[148:151], v[48:51], v[116:119], v[148:151]\n"
      "v_mfma_f32_16x16x32_bf16 v[148:151], v[32:35], v[84:87], v[148:151]\n"
      "v_mfma_f32_16x16x32_bf16 v[152:155], v[32:35], v[88:91], v[152:155]\n"
      "buffer_load_dwordx4 %[wv2], %[srdW], %[sW] offen offset:2048 lds\n"
      "v_mfma_f32_16x16x32_bf16 v[152:155], v[48:51], v[120:123], v[152:155]\n"
      "v_mfma_f32_16x16x32_bf16 v[184:187], v[52:55], v[120:123], v[184:187]\n"
      "v_mfma_f32_16x16x32_bf16 v[184:187], v[36:39], v[88:91], v[184:187]\n"
      "buffer_load_dwordx4 %[wv3], %[srdW], %[sW] offen offset:3072 lds\n"
      "v_mfma_f32_16x16x32_bf16 v[188:191], v[36:39], v[92:95], v[188:191]\n"
      "v_mfma_f32_16x16x32_bf16 v[188:191], v[52:55], v[124:127], v[188:191]\n"
      "v_mfma_f32_16x16x32_bf16 v[156:159], v[48:51], v[124:127], v[156:159]\n"
      "v_mfma_f32_16x16x32_bf16 v[156:159], v[32:35], v[92:95], v[156:159]\n"
      "s_waitcnt lgkmcnt(0)\n"
      "v_mfma_f32_16x16x32_bf16 v[192:195], v[40:43], v[64:67], v[192:195]\n"
      "s_waitcnt vmcnt(12)\n"
      "v_mfma_f32_16x16x32_bf16 v[192:195], v[56:59], v[96:99], v[192:195]\n"
      "s_barrier\n"
      "v_mfma_f32_16x16x32_bf16 v[224:227], v[60:63], v[96:99], v[224:227]\n"
      "ds_read_b64_tr_b16 v[32:33], %[wa0] offset:16384\n"
      "v_mfma_f32_16x16x32_bf16 v[224:227], v[44:47], v[64:67], v[224:227]\n"
      "ds_read_b128 v[64:67], %[xa0] offset:49152\n"
      "v_mfma_f32_16x16x32_bf16 v[228:231], v[44:47], v[68:71], v[228:231]\n"
      "ds_read_b128 v[96:99], %[xa1] offset:49152\n"
      "v_mfma_f32_16x16x32_bf16 v[228:231], v[60:63], v[100:103], v[228:231]\n"
      "ds_read_b64_tr_b16 v[34:35], %[wa0] offset:16896\n"
      "v_mfma_f32_16x16x32_bf16 v[196:199], v[56:59], v[100:103], v[196:199]\n"
      "ds_read_b64_tr_b16 v[36:37], %[wa1] offset:16384\n"
      "v_mfma_f32_16x16x32_bf16 v[196:199], v[40:43], v[68:71], v[196:199]\n"
      "ds_read_b128 v[68:71], %[xa0] offset:51200\n"
      "v_mfma_f32_16x16x32_bf16 v[200:203], v[40:43], v[72:75], v[200:203]\n"
      "ds_read_b128 v[100:103], %[xa1] offset:51200\n"
      "v_mfma_f32_16x16x32_bf16 v[200:203], v[56:59], v[104:107], v[200:203]\n"
      "ds_read_b64_tr_b16 v[38:39], %[wa1] offset:16896\n"
      "v_mfma_f32_16x16x32_bf16 v[232:235], v[60:63], v[104:107], v[232:235]\n"
      "s_waitcnt vmcnt(8)\n"
      "v_mfma_f32_16x16x32_bf16 v[232:235], v[44:47], v[72:75], v[232:235]\n"
      "ds_read_b128 v[72:75], %[xa0] offset:53248\n"
      "v_mfma_f32_16x16x32_bf16 v[236:239], v[44:47], v[76:79], v[236:239]\n"
      "ds_read_b128 v[104:107], %[xa1] offset:53248\n"
      "v_mfma_f32_16x16x32_bf16 v[236:239], v[60:63], v[108:111], v[236:239]\n"
      "ds_read_b64_tr_b16 v[48:49], %[wa0] offset:20480\n"
      "v_mfma_f32_16x16x32_bf16 v[204:207], v[56:59], v[108:111], v[204:207]\n"
      "ds_read_b64_tr_b16 v[50:51], %[wa0] offset:20992\n"
      "v_mfma_f32_16x16x32_bf16 v[204:207], v[40:43], v[76:79], v[204:207]\n"
      "ds_read_b128 v[76:79], %[xa0] offset:55296\n"
      "v_mfma_f32_16x16x32_bf16 v[208:211], v[40:43], v[80:83], v[208:211]\n"
      "ds_read_b128 v[108:111], %[xa1] offset:55296\n"
      "v_mfma_f32_16x16x32_bf16 v[208:211], v[56:59], v[112:115], v[208:211]\n"
      "ds_read_b64_tr_b16 v[52:53], %[wa1] offset:20480\n"
      "v_mfma_f32_16x16x32_bf16 v[240:243], v[60:63], v[112:115], v[240:243]\n"
      "ds_read_b64_tr_b16 v[54:55], %[wa1] offset:20992\n"
      "v_mfma_f32_16x16x32_bf16 v[240:243], v[44:47], v[80:83], v[240:243]\n"
      "ds_read_b128 v[80:83], %[xa0] offset:57344\n"
      "v_mfma_f32_16x16x32_bf16 v[244:247], v[44:47], v[84:87], v[244:247]\n"
      "ds_read_b128 v[112:115], %[xa1] offset:57344\n"
      "v_mfma_f32_16x16x32_bf16 v[244:247], v[60:63], v[116:119], v[244:247]\n"
      "v_mfma_f32_16x16x32_bf16 v[212:215], v[56:59], v[116:119], v[212:215]\n"
      "v_mfma_f32_16x16x32_bf16 v[212:215], v[40:43], v[84:87], v[212:215]\n"
      "ds_read_b128 v[84:87], %[xa0] offset:59392\n"
      "v_mfma_f32_16x16x32_bf16 v[216:219], v[40:43], v[88:91], v[216:219]\n"
      "ds_read_b128 v[116:119], %[xa1] offset:59392\n"
      "v_mfma_f32_16x16x32_bf16 v[216:219], v[56:59], v[120:123], v[216:219]\n"
      "v_mfma_f32_16x16x32_bf16 v[248:251], v[60:63], v[120:123], v[248:251]\n"
      "v_mfma_f32_16x16x32_bf16 v[248:251], v[44:47], v[88:91], v[248:251]\n"
      "ds_read_b128 v[88:91], %[xa0] offset:61440\n"
      "v_mfma_f32_16x16x32_bf16 v[252:255], v[44:47], v[92:95], v[252:255]\n"
      "ds_read_b128 v[120:123], %[xa1] offset:61440\n"
      "v_mfma_f32_16x16x32_bf16 v[252:255], v[60:63], v[124:127], v[252:255]\n"
      "v_mfma_f32_16x16x32_bf16 v[220:223], v[56:59], v[124:127], v[220:223]\n"
      "v_mfma_f32_16x16x32_bf16 v[220:223], v[40:43], v[92:95], v[220:223]\n"
      "ds_read_b128 v[92:95], %[xa0] offset:63488\n"
      "s_waitcnt lgkmcnt(7)\n"
      "v_mfma_f32_16x16x32_bf16 v[128:131], v[32:35], v[64:67], v[128:131]\n"
      "ds_read_b128 v[124:127], %[xa1] offset:63488\n"
      "v_mfma_f32_16x16x32_bf16 v[128:131], v[48:51], v[96:99], v[128:131]\n"
      "s_add_u32 m0, %[sWb], 8192\n"
      "v_mfma_f32_16x16x32_bf16 v[160:163], v[52:55], v[96:99], v[160:163]\n"
      "s_add_u32 %[sW], %[sW], %[d0]\n"
      "v_mfma_f32_16x16x32_bf16 v[160:163], v[36:39], v[64:67], v[160:163]\n"
      "ds_read_b64_tr_b16 v[40:41], %[wa2] offset:16384\n"
      "v_mfma_f32_16x16x32_bf16 v[164:167], v[36:39], v[68:71], v[164:167]\n"
      "buffer_load_dwordx4 %[wv0], %[srdW], %[sW] offen offset:0 lds\n"
      "v_mfma_f32_16x16x32_bf16 v[164:167], v[52:55], v[100:103], v[164:167]\n"
      "ds_read_b64_tr_b16 v[42:43], %[wa2] offset:16896\n"
      "v_mfma_f32_16x16x32_bf16 v[132:135], v[48:51], v[100:103], v[132:135]\n"
      "ds_read_b64_tr_b16 v[56:57], %[wa2] offset:20480\n"
      "v_mfma_f32_16x16x32_bf16 v[132:135], v[32:35], v[68:71], v[132:135]\n"
      "buffer_load_dwordx4 %[wv1], %[srdW], %[sW] offen offset:1024 lds\n"
      "v_mfma_f32_16x16x32_bf16 v[136:139], v[32:35], v[72:75], v[136:139]\n"
      "ds_read_b64_tr_b16 v[58:59], %[wa2] offset:20992\n"
      "v_mfma_f32_16x16x32_bf16 v[136:139], v[48:51], v[104:107], v[136:139]\n"
      "ds_read_b64_tr_b16 v[44:45], %[wa3] offset:16384\n"
      "v_mfma_f32_16x16x32_bf16 v[168:171], v[52:55], v[104:107], v[168:171]\n"
      "buffer_load_dwordx4 %[wv2], %[srdW], %[sW] offen offset:2048 lds\n"
      "v_mfma_f32_16x16x32_bf16 v[168:171], v[36:39], v[72:75], v[168:171]\n"
      "ds_read_b64_tr_b16 v[46:47], %[wa3] offset:16896\n"
      "v_mfma_f32_16x16x32_bf16 v[172:175], v[36:39], v[76:79], v[172:175]\n"
      "ds_read_b64_tr_b16 v[60:61], %[wa3] offset:20480\n"
      "v_mfma_f32_16x16x32_bf16 v[172:175], v[52:55], v[108:111], v[172:175]\n"
      "buffer_load_dwordx4 %[wv3], %[srdW], %[sW] offen offset:3072 lds\n"
      "v_mfma_f32_16x16x32_bf16 v[140:143], v[48:51], v[108:111], v[140:143]\n"
      "ds_read_b64_tr_b16 v[62:63], %[wa3] offset:20992\n"
      "v_mfma_f32_16x16x32_bf16 v[140:143], v[32:35], v[76:79], v[140:143]\n"
      "s_add_u32 m0, %[sWb], 12288\n"
      "s_waitcnt lgkmcnt(8)\n"
      "v_mfma_f32_16x16x32_bf16 v[144:147], v[32:35], v[80:83], v[144:147]\n"
      "s_add_u32 %[sW], %[sW], 0x8000\n"
      "v_mfma_f32_16x16x32_bf16 v[144:147], v[48:51], v[112:115], v[144:147]\n"
      "v_mfma_f32_16x16x32_bf16 v[176:179], v[52:55], v[112:115], v[176:179]\n"
      "buffer_load_dwordx4 %[wv0], %[srdW], %[sW] offen offset:0 lds\n"
      "v_mfma_f32_16x16x32_bf16 v[176:179], v[36:39], v[80:83], v[176:179]\n"
      "v_mfma_f32_16x16x32_bf16 v[180:183], v[36:39], v[84:87], v[180:183]\n"
      "v_mfma_f32_16x16x32_bf16 v[180:183], v[52:55], v[116:119], v[180:183]\n"
      "buffer_load_dwordx4 %[wv1], %[srdW], %[sW] offen offset:1024 lds\n"
      "v_mfma_f32_16x16x32_bf16 v[148:151], v[48:51], v[116:119], v[148:151]\n"
      "v_mfma_f32_16x16x32_bf16 v[148:151], v[32:35], v[84:87], v[148:151]\n"
      "v_mfma_f32_16x16x32_bf16 v[152:155], v[32:35], v[88:91], v[152:155]\n"
      "buffer_load_dwordx4 %[wv2], %[srdW], %[sW] offen offset:2048 lds\n"
      "v_mfma_f32_16x16x32_bf16 v[152:155], v[48:51], v[120:123], v[152:155]\n"
      "v_mfma_f32_16x16x32_bf16 v[184:187], v[52:55], v[120:123], v[184:187]\n"
      "v_mfma_f32_16x16x32_bf16 v[184:187], v[36:39], v[88:91], v[184:187]\n"
      "buffer_load_dwordx4 %[wv3], %[srdW], %[sW] offen offset:3072 lds\n"
      "v_mfma_f32_16x16x32_bf16 v[188:191], v[36:39], v[92:95], v[188:191]\n"
      "v_mfma_f32_16x16x32_bf16 v[188:191], v[52:55], v[124:127], v[188:191]\n"
      "v_mfma_f32_16x16x32_bf16 v[156:159], v[48:51], v[124:127], v[156:159]\n"
      "v_mfma_f32_16x16x32_bf16 v[156:159], v[32:35], v[92:95], v[156:159]\n"
      "s_waitcnt lgkmcnt(0)\n"
      "v_mfma_f32_16x16x32_bf16 v[192:195], v[40:43], v[64:67], v[192:195]\n"
      "s_waitcnt vmcnt(12)\n"
      "v_mfma_f32_16x16x32_bf16 v[192:195], v[56:59], v[96:99], v[192:195]\n"
      "s_barrier\n"
      "v_mfma_f32_16x16x32_bf16 v[224:227], v[60:63], v[96:99], v[224:227]\n"
      "ds_read_b64_tr_b16 v[32:33], %[wa0] offset:0\n"
      "v_mfma_f32_16x16x32_bf16 v[224:227], v[44:47], v[64:67], v[224:227]\n"
      "ds_read_b128 v[64:67], %[xa0] offset:0\n"
      "v_mfma_f32_16x16x32_bf16 v[228:231], v[44:47], v[68:71], v[228:231]\n"
      "ds_read_b128 v[96:99], %[xa1] offset:0\n"
      "v_mfma_f32_16x16x32_bf16 v[228:231], v[60:63], v[100:103], v[228:231]\n"
      "ds_read_b64_tr_b16 v[34:35], %[wa0] offset:512\n"
      "v_mfma_f32_16x16x32_bf16 v[196:199], v[56:59], v[100:103], v[196:199]\n"
      "ds_read_b64_tr_b16 v[36:37], %[wa1] offset:0\n"
      "v_mfma_f32_16x16x32_bf16 v[196:199], v[40:43], v[68:71], v[196:199]\n"
      "ds_read_b128 v[68:71], %[xa0] offset:2048\n"
      "v_mfma_f32_16x16x32_bf16 v[200:203], v[40:43], v[72:75], v[200:203]\n"
      "ds_read_b128 v[100:103], %[xa1] offset:2048\n"
      "v_mfma_f32_16x16x32_bf16 v[200:203], v[56:59], v[104:107], v[200:203]\n"
      "ds_read_b64_tr_b16 v[38:39], %[wa1] offset:512\n"
      "v_mfma_f32_16x16x32_bf16 v[232:235], v[60:63], v[104:107], v[232:235]\n"
      "s_waitcnt vmcnt(8)\n"
      "v_mfma_f32_16x16x32_bf16 v[232:235], v[44:47], v[72:75], v[232:235]\n"
      "ds_read_b128 v[72:75], %[xa0] offset:4096\n"
      "v_mfma_f32_16x16x32_bf16 v[236:239], v[44:47], v[76:79], v[236:239]\n"
      "ds_read_b128 v[104:107], %[xa1] offset:4096\n"
      "v_mfma_f32_16x16x32_bf16 v[236:239], v[60:63], v[108:111], v[236:239]\n"
      "ds_read_b64_tr_b16 v[48:49], %[wa0] offset:4096\n"
      "v_mfma_f32_16x16x32_bf16 v[204:207], v[56:59], v[108:111], v[204:207]\n"
      "ds_read_b64_tr_b16 v[50:51], %[wa0] offset:4608\n"
      "v_mfma_f32_16x16x32_bf16 v[204:207], v[40:43], v[76:79], v[204:207]\n"
      "ds_read_b128 v[76:79], %[xa0] offset:6144\n"
      "v_mfma_f32_16x16x32_bf16 v[208:211], v[40:43], v[80:83], v[208:211]\n"
      "ds_read_b128 v[108:111], %[xa1] offset:6144\n"
      "v_mfma_f32_16x16x32_bf16 v[208:211], v[56:59], v[112:115], v[208:211]\n"
      "ds_read_b64_tr_b16 v[52:53], %[wa1] offset:4096\n"
      "v_mfma_f32_16x16x32_bf16 v[240:243], v[60:63], v[112:115], v[240:243]\n"
      "ds_read_b64_tr_b16 v[54:55], %[wa1] offset:4608\n"
      "v_mfma_f32_16x16x32_bf16 v[240:243], v[44:47], v[80:83], v[240:243]\n"
      "ds_read_b128 v[80:83], %[xa0] offset:8192\n"
      "v_mfma_f32_16x16x32_bf16 v[244:247], v[44:47], v[84:87], v[244:247]\n"
      "ds_read_b128 v[112:115], %[xa1] offset:8192\n"
      "v_mfma_f32_16x16x32_bf16 v[244:247], v[60:63], v[116:119], v[244:247]\n"
      "v_mfma_f32_16x16x32_bf16 v[212:215], v[56:59], v[116:119], v[212:215]\n"
      "v_mfma_f32_16x16x32_bf16 v[212:215], v[40:43], v[84:87], v[212:215]\n"
      "ds_read_b128 v[84:87], %[xa0] offset:10240\n"
      "v_mfma_f32_16x16x32_bf16 v[216:219], v[40:43], v[88:91], v[216:219]\n"
      "ds_read_b128 v[116:119], %[xa1] offset:10240\n"
      "v_mfma_f32_16x16x32_bf16 v[216:219], v[56:59], v[120:123], v[216:219]\n"
      "v_mfma_f32_16x16x32_bf16 v[248:251], v[60:63], v[120:123], v[248:251]\n"
      "v_mfma_f32_16x16x32_bf16 v[248:251], v[44:47], v[88:91], v[248:251]\n"
      "ds_read_b128 v[88:91], %[xa0] offset:12288\n"
      "v_mfma_f32_16x16x32_bf16 v[252:255], v[44:47], v[92:95], v[252:255]\n"
      "ds_read_b128 v[120:123], %[xa1] offset:12288\n"
      "v_mfma_f32_16x16x32_bf16 v[252:255], v[60:63], v[124:127], v[252:255]\n"
      "v_mfma_f32_16x16x32_bf16 v[220:223], v[56:59], v[124:127], v[220:223]\n"
      "v_mfma_f32_16x16x32_bf16 v[220:223], v[40:43], v[92:95], v[220:223]\n"
      "ds_read_b128 v[92:95], %[xa0] offset:14336\n"
      "ds_read_b128 v[124:127], %[xa1] offset:14336\n"
      "s_sub_u32 %[sC], %[sC], 1\n"
      "s_cmp_lg_u32 %[sC], 0\n"
      "s_cbranch_scc1 Lc0%=\n"
      "s_waitcnt lgkmcnt(8)\n"
      "v_mfma_f32_16x16x32_bf16 v[128:131], v[32:35], v[64:67], v[128:131]\n"
      "s_add_u32 m0, %[sWb], 16384\n"
      "v_mfma_f32_16x16x32_bf16 v[128:131], v[48:51], v[96:99], v[128:131]\n"
      "s_add_u32 %[sW], %[sW], %[d1]\n"
      "v_mfma_f32_16x16x32_bf16 v[160:163], v[52:55], v[96:99], v[160:163]\n"
      "ds_read_b64_tr_b16 v[40:41], %[wa2] offset:0\n"
      "v_mfma_f32_16x16x32_bf16 v[160:163], v[36:39], v[64:67], v[160:163]\n"
      "buffer_load_dwordx4 %[wv0], %[srdW], %[sW] offen offset:0 lds\n"
      "v_mfma_f32_16x16x32_bf16 v[164:167], v[36:39], v[68:71], v[164:167]\n"
      "ds_read_b64_tr_b16 v[42:43], %[wa2] offset:512\n"
      "v_mfma_f32_16x16x32_bf16 v[164:167], v[52:55], v[100:103], v[164:167]\n"
      "ds_read_b64_tr_b16 v[56:57], %[wa2] offset:4096\n"
      "v_mfma_f32_16x16x32_bf16 v[132:135], v[48:51], v[100:103], v[132:135]\n"
      "buffer_load_dwordx4 %[wv1], %[srdW], %[sW] offen offset:1024 lds\n"
      "v_mfma_f32_16x16x32_bf16 v[132:135], v[32:35], v[68:71], v[132:135]\n"
      "ds_read_b64_tr_b16 v[58:59], %[wa2] offset:4608\n"
      "v_mfma_f32_16x16x32_bf16 v[136:139], v[32:35], v[72:75], v[136:139]\n"
      "ds_read_b64_tr_b16 v[44:45], %[wa3] offset:0\n"
      "v_mfma_f32_16x16x32_bf16 v[136:139], v[48:51], v[104:107], v[136:139]\n"
      "buffer_load_dwordx4 %[wv2], %[srdW], %[sW] offen offset:2048 lds\n"
      "v_mfma_f32_16x16x32_bf16 v[168:171], v[52:55], v[104:107], v[168:171]\n"
      "ds_read_b64_tr_b16 v[46:47], %[wa3] offset:512\n"
      "v_mfma_f32_16x16x32_bf16 v[168:171], v[36:39], v[72:75], v[168:171]\n"
      "ds_read_b64_tr_b16 v[60:61], %[wa3] offset:4096\n"
      "v_mfma_f32_16x16x32_bf16 v[172:175], v[36:39], v[76:79], v[172:175]\n"
      "buffer_load_dwordx4 %[wv3], %[srdW], %[sW] offen offset:3072 lds\n"
      "v_mfma_f32_16x16x32_bf16 v[172:175], v[52:55], v[108:111], v[172:175]\n"
      "ds_read_b64_tr_b16 v[62:63], %[wa3] offset:4608\n"
      "v_mfma_f32_16x16x32_bf16 v[140:143], v[48:51], v[108:111], v[140:143]\n"
      "s_add_u32 m0, %[sWb], 20480\n"
      "v_mfma_f32_16x16x32_bf16 v[140:143], v[32:35], v[76:79], v[140:143]\n"
      "s_add_u32 %[sW], %[sW], 0x8000\n"
      "s_waitcnt lgkmcnt(8)\n"
      "v_mfma_f32_16x16x32_bf16 v[144:147], v[32:35], v[80:83], v[144:147]\n"
      "v_mfma_f32_16x16x32_bf16 v[144:147], v[48:51], v[112:115], v[144:147]\n"
      "buffer_load_dwordx4 %[wv0], %[srdW], %[sW] offen offset:0 lds\n"
      "v_mfma_f32_16x16x32_bf16 v[176:179], v[52:55], v[112:115], v[176:179]\n"
      "v_mfma_f32_16x16x32_bf16 v[176:179], v[36:39], v[80:83], v[176:179]\n"
      "v_mfma_f32_16x16x32_bf16 v[180:183], v[36:39], v[84:87], v[180:183]\n"
      "buffer_load_dwordx4 %[wv1], %[srdW], %[sW] offen offset:1024 lds\n"
      "v_mfma_f32_16x16x32_bf16 v[180:183], v[52:55], v[116:119], v[180:183]\n"
      "v_mfma_f32_16x16x32_bf16 v[148:151], v[48:51], v[116:119], v[148:151]\n"
      "v_mfma_f32_16x16x32_bf16 v[148:151], v[32:35], v[84:87], v[148:151]\n"
      "buffer_load_dwordx4 %[wv2], %[srdW], %[sW] offen offset:2048 lds\n"
      "v_mfma_f32_16x16x32_bf16 v[152:155], v[32:35], v[88:91], v[152:155]\n"
      "v_mfma_f32_16x16x32_bf16 v[152:155], v[48:51], v[120:123], v[152:155]\n"
      "v_mfma_f32_16x16x32_bf16 v[184:187], v[52:55], v[120:123], v[184:187]\n"
      "buffer_load_dwordx4 %[wv3], %[srdW], %[sW] offen offset:3072 lds\n"
      "v_mfma_f32_16x16x32_bf16 v[184:187], v[36:39], v[88:91], v[184:187]\n"
      "v_mfma_f32_16x16x32_bf16 v[188:191], v[36:39], v[92:95], v[188:191]\n"
      "v_mfma_f32_16x16x32_bf16 v[188:191], v[52:55], v[124:127], v[188:191]\n"
      "v_mfma_f32_16x16x32_bf16 v[156:159], v[48:51], v[124:127], v[156:159]\n"
      "v_mfma_f32_16x16x32_bf16 v[156:159], v[32:35], v[92:95], v[156:159]\n"
      "s_waitcnt lgkmcnt(0)\n"
      "v_mfma_f32_16x16x32_bf16 v[192:195], v[40:43], v[64:67], v[192:195]\n"
      "s_waitcnt vmcnt(12)\n"
      "v_mfma_f32_16x16x32_bf16 v[192:195], v[56:59], v[96:99], v[192:195]\n"
      "s_barrier\n"
      "v_mfma_f32_16x16x32_bf16 v[224:227], v[60:63], v[96:99], v[224:227]\n"
      "ds_read_b64_tr_b16 v[32:33], %[wa0] offset:8192\n"
      "v_mfma_f32_16x16x32_bf16 v[224:227], v[44:47], v[64:67], v[224:227]\n"
      "ds_read_b128 v[64:67], %[xa0] offset:16384\n"
      "v_mfma_f32_16x16x32_bf16 v[228:231], v[44:47], v[68:71], v[228:231]\n"
      "ds_read_b128 v[96:99], %[xa1] offset:16384\n"
      "v_mfma_f32_16x16x32_bf16 v[228:231], v[60:63], v[100:103], v[228:231]\n"
      "ds_read_b64_tr_b16 v[34:35], %[wa0] offset:8704\n"
      "v_mfma_f32_16x16x32_bf16 v[196:199], v[56:59], v[100:103], v[196:199]\n"
      "ds_read_b64_tr_b16 v[36:37], %[wa1] offset:8192\n"
      "v_mfma_f32_16x16x32_bf16 v[196:199], v[40:43], v[68:71], v[196:199]\n"
      "ds_read_b128 v[68:71], %[xa0] offset:18432\n"
      "v_mfma_f32_16x16x32_bf16 v[200:203], v[40:43], v[72:75], v[200:203]\n"
      "ds_read_b128 v[100:103], %[xa1] offset:18432\n"
      "v_mfma_f32_16x16x32_bf16 v[200:203], v[56:59], v[104:107], v[200:203]\n"
      "ds_read_b64_tr_b16 v[38:39], %[wa1] offset:8704\n"
      "v_mfma_f32_16x16x32_bf16 v[232:235], v[60:63], v[104:107], v[232:235]\n"
      "s_waitcnt vmcnt(8)\n"
      "v_mfma_f32_16x16x32_bf16 v[232:235], v[44:47], v[72:75], v[232:235]\n"
      "ds_read_b128 v[72:75], %[xa0] offset:20480\n"
      "v_mfma_f32_16x16x32_bf16 v[236:239], v[44:47], v[76:79], v[236:239]\n"
      "ds_read_b128 v[104:107], %[xa1] offset:20480\n"
      "v_mfma_f32_16x16x32_bf16 v[236:239], v[60:63], v[108:111], v[236:239]\n"
      "ds_read_b64_tr_b16 v[48:49], %[wa0] offset:12288\n"
      "v_mfma_f32_16x16x32_bf16 v[204:207], v[56:59], v[108:111], v[204:207]\n"
      "ds_read_b64_tr_b16 v[50:51], %[wa0] offset:12800\n"
      "v_mfma_f32_16x16x32_bf16 v[204:207], v[40:43], v[76:79], v[204:207]\n"
      "ds_read_b128 v[76:79], %[xa0] offset:22528\n"
      "v_mfma_f32_16x16x32_bf16 v[208:211], v[40:43], v[80:83], v[208:211]\n"
      "ds_read_b128 v[108:111], %[xa1] offset:22528\n"
      "v_mfma_f32_16x16x32_bf16 v[208:211], v[56:59], v[112:115], v[208:211]\n"
      "ds_read_b64_tr_b16 v[52:53], %[wa1] offset:12288\n"
      "v_mfma_f32_16x16x32_bf16 v[240:243], v[60:63], v[112:115], v[240:243]\n"
      "ds_read_b64_tr_b16 v[54:55], %[wa1] offset:12800\n"
      "v_mfma_f32_16x16x32_bf16 v[240:243], v[44:47], v[80:83], v[240:243]\n"
      "ds_read_b128 v[80:83], %[xa0] offset:24576\n"
      "v_mfma_f32_16x16x32_bf16 v[244:247], v[44:47], v[84:87], v[244:247]\n"
      "ds_read_b128 v[112:115], %[xa1] offset:24576\n"
      "v_mfma_f32_16x16x32_bf16 v[244:247], v[60:63], v[116:119], v[244:247]\n"
      "v_mfma_f32_16x16x32_bf16 v[212:215], v[56:59], v[116:119], v[212:215]\n"
      "v_mfma_f32_16x16x32_bf16 v[212:215], v[40:43], v[84:87], v[212:215]\n"
      "ds_read_b128 v[84:87], %[xa0] offset:26624\n"
      "v_mfma_f32_16x16x32_bf16 v[216:219], v[40:43], v[88:91], v[216:219]\n"
      "ds_read_b128 v[116:119], %[xa1] offset:26624\n"
      "v_mfma_f32_16x16x32_bf16 v[216:219], v[56:59], v[120:123], v[216:219]\n"
      "v_mfma_f32_16x16x32_bf16 v[248:251], v[60:63], v[120:123], v[248:251]\n"
      "v_mfma_f32_16x16x32_bf16 v[248:251], v[44:47], v[88:91], v[248:251]\n"
      "ds_read_b128 v[88:91], %[xa0] offset:28672\n"
      "v_mfma_f32_16x16x32_bf16 v[252:255], v[44:47], v[92:95], v[252:255]\n"
      "ds_read_b128 v[120:123], %[xa1] offset:28672\n"
      "v_mfma_f32_16x16x32_bf16 v[252:255], v[60:63], v[124:127], v[252:255]\n"
      "v_mfma_f32_16x16x32_bf16 v[220:223], v[56:59], v[124:127], v[220:223]\n"
      "v_mfma_f32_16x16x32_bf16 v[220:223], v[40:43], v[92:95], v[220:223]\n"
      "ds_read_b128 v[92:95], %[xa0] offset:30720\n"
      "s_waitcnt lgkmcnt(7)\n"
      "v_mfma_f32_16x16x32_bf16 v[128:131], v[32:35], v[64:67], v[128:131]\n"
      "ds_read_b128 v[124:127], %[xa1] offset:30720\n"
      "v_mfma_f32_16x16x32_bf16 v[128:131], v[48:51], v[96:99], v[128:131]\n"
      "s_add_u32 m0, %[sWb], 0\n"
      "v_mfma_f32_16x16x32_bf16 v[160:163], v[52:55], v[96:99], v[160:163]\n"
      "s_add_u32 %[sW], %[sW], %[d0]\n"
      "v_mfma_f32_16x16x32_bf16 v[160:163], v[36:39], v[64:67], v[160:163]\n"
      "ds_read_b64_tr_b16 v[40:41], %[wa2] offset:8192\n"
      "v_mfma_f32_16x16x32_bf16 v[164:167], v[36:39], v[68:71], v[164:167]\n"
      "buffer_load_dwordx4 %[wv0], %[srdW], %[sW] offen offset:0 lds\n"
      "v_mfma_f32_16x16x32_bf16 v[164:167], v[52:55], v[100:103], v[164:167]\n"
      "ds_read_b64_tr_b16 v[42:43], %[wa2] offset:8704\n"
      "v_mfma_f32_16x16x32_bf16 v[132:135], v[48:51], v[100:103], v[132:135]\n"
      "ds_read_b64_tr_b16 v[56:57], %[wa2] offset:12288\n"
      "v_mfma_f32_16x16x32_bf16 v[132:135], v[32:35], v[68:71], v[132:135]\n"
      "buffer_load_dwordx4 %[wv1], %[srdW], %[sW] offen offset:1024 lds\n"
      "v_mfma_f32_16x16x32_bf16 v[136:139], v[32:35], v[72:75], v[136:139]\n"
      "ds_read_b64_tr_b16 v[58:59], %[wa2] offset:12800\n"
      "v_mfma_f32_16x16x32_bf16 v[136:139], v[48:51], v[104:107], v[136:139]\n"
      "ds_read_b64_tr_b16 v[44:45], %[wa3] offset:8192\n"
      "v_mfma_f32_16x16x32_bf16 v[168:171], v[52:55], v[104:107], v[168:171]\n"
      "buffer_load_dwordx4 %[wv2], %[srdW], %[sW] offen offset:2048 lds\n"
      "v_mfma_f32_16x16x32_bf16 v[168:171], v[36:39], v[72:75], v[168:171]\n"
      "ds_read_b64_tr_b16 v[46:47], %[wa3] offset:8704\n"
      "v_mfma_f32_16x16x32_bf16 v[172:175], v[36:39], v[76:79], v[172:175]\n"
      "ds_read_b64_tr_b16 v[60:61], %[wa3] offset:12288\n"
      "v_mfma_f32_16x16x32_bf16 v[172:175], v[52:55], v[108:111], v[172:175]\n"
      "buffer_load_dwordx4 %[wv3], %[srdW], %[sW] offen offset:3072 lds\n"
      "v_mfma_f32_16x16x32_bf16 v[140:143], v[48:51], v[108:111], v[140:143]\n"
      "ds_read_b64_tr_b16 v[62:63], %[wa3] offset:12800\n"
      "v_mfma_f32_16x16x32_bf16 v[140:143], v[32:35], v[76:79], v[140:143]\n"
      "s_add_u32 m0, %[sWb], 4096\n"
      "s_waitcnt lgkmcnt(8)\n"
      "v_mfma_f32_16x16x32_bf16 v[144:147], v[32:35], v[80:83], v[144:147]\n"
      "s_add_u32 %[sW], %[sW], 0x8000\n"
      "v_mfma_f32_16x16x32_bf16 v[144:147], v[48:51], v[112:115], v[144:147]\n"
      "v_mfma_f32_16x16x32_bf16 v[176:179], v[52:55], v[112:115], v[176:179]\n"
      "buffer_load_dwordx4 %[wv0], %[srdW], %[sW] offen offset:0 lds\n"
      "v_mfma_f32_16x16x32_bf16 v[176:179], v[36:39], v[80:83], v[176:179]\n"
      "v_mfma_f32_16x16x32_bf16 v[180:183], v[36:39], v[84:87], v[180:183]\n"
      "v_mfma_f32_16x16x32_bf16 v[180:183], v[52:55], v[116:119], v[180:183]\n"
      "buffer_load_dwordx4 %[wv1], %[srdW], %[sW] offen offset:1024 lds\n"
      "v_mfma_f32_16x16x32_bf16 v[148:151], v[48:51], v[116:119], v[148:151]\n"
      "v_mfma_f32_16x16x32_bf16 v[148:151], v[32:35], v[84:87], v[148:151]\n"
      "v_mfma_f32_16x16x32_bf16 v[152:155], v[32:35], v[88:91], v[152:155]\n"
      "buffer_load_dwordx4 %[wv2], %[srdW], %[sW] offen offset:2048 lds\n"
      "v_mfma_f32_16x16x32_bf16 v[152:155], v[48:51], v[120:123], v[152:155]\n"
      "v_mfma_f32_16x16x32_bf16 v[184:187], v[52:55], v[120:123], v[184:187]\n"
      "v_mfma_f32_16x16x32_bf16 v[184:187], v[36:39], v[88:91], v[184:187]\n"
      "buffer_load_dwordx4 %[wv3], %[srdW], %[sW] offen offset:3072 lds\n"
      "v_mfma_f32_16x16x32_bf16 v[188:191], v[36:39], v[92:95], v[188:191]\n"
      "v_mfma_f32_16x16x32_bf16 v[188:191], v[52:55], v[124:127], v[188:191]\n"
      "v_mfma_f32_16x16x32_bf16 v[156:159], v[48:51], v[124:127], v[156:159]\n"
      "v_mfma_f32_16x16x32_bf16 v[156:159], v[32:35], v[92:95], v[156:159]\n"
      "s_waitcnt lgkmcnt(0)\n"
      "v_mfma_f32_16x16x32_bf16 v[192:195], v[40:43], v[64:67], v[192:195]\n"
      "s_waitcnt vmcnt(12)\n"
      "v_mfma_f32_16x16x32_bf16 v[192:195], v[56:59], v[96:99], v[192:195]\n"
      "s_barrier\n"
      "v_mfma_f32_16x16x32_bf16 v[224:227], v[60:63], v[96:99], v[224:227]\n"
      "ds_read_b64_tr_b16 v[32:33], %[wa0] offset:16384\n"
      "v_mfma_f32_16x16x32_bf16 v[224:227], v[44:47], v[64:67], v[224:227]\n"
      "ds_read_b128 v[64:67], %[xa0] offset:32768\n"
      "v_mfma_f32_16x16x32_bf16 v[228:231], v[44:47], v[68:71], v[228:231]\n"
      "ds_read_b128 v[96:99], %[xa1] offset:32768\n"
      "v_mfma_f32_16x16x32_bf16 v[228:231], v[60:63], v[100:103], v[228:231]\n"
      "ds_read_b64_tr_b16 v[34:35], %[wa0] offset:16896\n"
      "v_mfma_f32_16x16x32_bf16 v[196:199], v[56:59], v[100:103], v[196:199]\n"
      "ds_read_b64_tr_b16 v[36:37], %[wa1] offset:16384\n"
      "v_mfma_f32_16x16x32_bf16 v[196:199], v[40:43], v[68:71], v[196:199]\n"
      "ds_read_b128 v[68:71], %[xa0] offset:34816\n"
      "v_mfma_f32_16x16x32_bf16 v[200:203], v[40:43], v[72:75], v[200:203]\n"
      "ds_read_b128 v[100:103], %[xa1] offset:34816\n"
      "v_mfma_f32_16x16x32_bf16 v[200:203], v[56:59], v[104:107], v[200:203]\n"
      "ds_read_b64_tr_b16 v[38:39], %[wa1] offset:16896\n"
      "v_mfma_f32_16x16x32_bf16 v[232:235], v[60:63], v[104:107], v[232:235]\n"
      "s_waitcnt vmcnt(8)\n"
      "v_mfma_f32_16x16x32_bf16 v[232:235], v[44:47], v[72:75], v[232:235]\n"
      "ds_read_b128 v[72:75], %[xa0] offset:36864\n"
      "v_mfma_f32_16x16x32_bf16 v[236:239], v[44:47], v[76:79], v[236:239]\n"
      "ds_read_b128 v[104:107], %[xa1] offset:36864\n"
      "v_mfma_f32_16x16x32_bf16 v[236:239], v[60:63], v[108:111], v[236:239]\n"
      "ds_read_b64_tr_b16 v[48:49], %[wa0] offset:20480\n"
      "v_mfma_f32_16x16x32_bf16 v[204:207], v[56:59], v[108:111], v[204:207]\n"
      "ds_read_b64_tr_b16 v[50:51], %[wa0] offset:20992\n"
      "v_mfma_f32_16x16x32_bf16 v[204:207], v[40:43], v[76:79], v[204:207]\n"
      "ds_read_b128 v[76:79], %[xa0] offset:38912\n"
      "v_mfma_f32_16x16x32_bf16 v[208:211], v[40:43], v[80:83], v[208:211]\n"
      "ds_read_b128 v[108:111], %[xa1] offset:38912\n"
      "v_mfma_f32_16x16x32_bf16 v[208:211], v[56:59], v[112:115], v[208:211]\n"
      "ds_read_b64_tr_b16 v[52:53], %[wa1] offset:20480\n"
      "v_mfma_f32_16x16x32_bf16 v[240:243], v[60:63], v[112:115], v[240:243]\n"
      "ds_read_b64_tr_b16 v[54:55], %[wa1] offset:20992\n"
      "v_mfma_f32_16x16x32_bf16 v[240:243], v[44:47], v[80:83], v[240:243]\n"
      "ds_read_b128 v[80:83], %[xa0] offset:40960\n"
      "v_mfma_f32_16x16x32_bf16 v[244:247], v[44:47], v[84:87], v[244:247]\n"
      "ds_read_b128 v[112:115], %[xa1] offset:40960\n"
      "v_mfma_f32_16x16x32_bf16 v[244:247], v[60:63], v[116:119], v[244:247]\n"
      "v_mfma_f32_16x16x32_bf16 v[212:215], v[56:59], v[116:119], v[212:215]\n"
      "v_mfma_f32_16x16x32_bf16 v[212:215], v[40:43], v[84:87], v[212:215]\n"
      "ds_read_b128 v[84:87], %[xa0] offset:43008\n"
      "v_mfma_f32_16x16x32_bf16 v[216:219], v[40:43], v[88:91], v[216:219]\n"
      "ds_read_b128 v[116:119], %[xa1] offset:43008\n"
      "v_mfma_f32_16x16x32_bf16 v[216:219], v[56:59], v[120:123], v[216:219]\n"
      "v_mfma_f32_16x16x32_bf16 v[248:251], v[60:63], v[120:123], v[248:251]\n"
      "v_mfma_f32_16x16x32_bf16 v[248:251], v[44:47], v[88:91], v[248:251]\n"
      "ds_read_b128 v[88:91], %[xa0] offset:45056\n"
      "v_mfma_f32_16x16x32_bf16 v[252:255], v[44:47], v[92:95], v[252:255]\n"
      "ds_read_b128 v[120:123], %[xa1] offset:45056\n"
      "v_mfma_f32_16x16x32_bf16 v[252:255], v[60:63], v[124:127], v[252:255]\n"
      "v_mfma_f32_16x16x32_bf16 v[220:223], v[56:59], v[124:127], v[220:223]\n"
      "v_mfma_f32_16x16x32_bf16 v[220:223], v[40:43], v[92:95], v[220:223]\n"
      "ds_read_b128 v[92:95], %[xa0] offset:47104\n"
      "s_waitcnt lgkmcnt(7)\n"
      "v_mfma_f32_16x16x32_bf16 v[128:131], v[32:35], v[64:67], v[128:131]\n"
      "ds_read_b128 v[124:127], %[xa1] offset:47104\n"
      "v_mfma_f32_16x16x32_bf16 v[128:131], v[48:51], v[96:99], v[128:131]\n"
      "global_load_dwordx2 v[24:25], %[vb], %[pw2] offset:0\n"
      "v_mfma_f32_16x16x32_bf16 v[160:163], v[52:55], v[96:99], v[160:163]\n"
      "global_load_dwordx2 v[26:27], %[vb], %[pw2] offset:32\n"
      "v_mfma_f32_16x16x32_bf16 v[160:163], v[36:39], v[64:67], v[160:163]\n"
      "ds_read_b64_tr_b16 v[40:41], %[wa2] offset:16384\n"
      "v_mfma_f32_16x16x32_bf16 v[164:167], v[36:39], v[68:71], v[164:167]\n"
      "global_load_dwordx2 v[28:29], %[vb], %[pw2] offset:64\n"
      "v_mfma_f32_16x16x32_bf16 v[164:167], v[52:55], v[100:103], v[164:167]\n"
      "ds_read_b64_tr_b16 v[42:43], %[wa2] offset:16896\n"
      "v_mfma_f32_16x16x32_bf16 v[132:135], v[48:51], v[100:103], v[132:135]\n"
      "ds_read_b64_tr_b16 v[56:57], %[wa2] offset:20480\n"
      "v_mfma_f32_16x16x32_bf16 v[132:135], v[32:35], v[68:71], v[132:135]\n"
      "global_load_dwordx2 v[30:31], %[vb], %[pw2] offset:96\n"
      "v_mfma_f32_16x16x32_bf16 v[136:139], v[32:35], v[72:75], v[136:139]\n"
      "ds_read_b64_tr_b16 v[58:59], %[wa2] offset:20992\n"
      "v_mfma_f32_16x16x32_bf16 v[136:139], v[48:51], v[104:107], v[136:139]\n"
      "ds_read_b64_tr_b16 v[44:45], %[wa3] offset:16384\n"
      "v_mfma_f32_16x16x32_bf16 v[168:171], v[52:55], v[104:107], v[168:171]\n"
      "v_mfma_f32_16x16x32_bf16 v[168:171], v[36:39], v[72:75], v[168:171]\n"
      "ds_read_b64_tr_b16 v[46:47], %[wa3] offset:16896\n"
      "v_mfma_f32_16x16x32_bf16 v[172:175], v[36:39], v[76:79], v[172:175]\n"
      "ds_read_b64_tr_b16 v[60:61], %[wa3] offset:20480\n"
      "v_mfma_f32_16x16x32_bf16 v[172:175], v[52:55], v[108:111], v[172:175]\n"
      "v_mfma_f32_16x16x32_bf16 v[140:143], v[48:51], v[108:111], v[140:143]\n"
      "ds_read_b64_tr_b16 v[62:63], %[wa3] offset:20992\n"
      "v_mfma_f32_16x16x32_bf16 v[140:143], v[32:35], v[76:79], v[140:143]\n"
      "s_waitcnt lgkmcnt(8)\n"
      "v_mfma_f32_16x16x32_bf16 v[144:147], v[32:35], v[80:83], v[144:147]\n"
      "v_mfma_f32_16x16x32_bf16 v[144:147], v[48:51], v[112:115], v[144:147]\n"
      "v_mfma_f32_16x16x32_bf16 v[176:179], v[52:55], v[112:115], v[176:179]\n"
      "v_mfma_f32_16x16x32_bf16 v[176:179], v[36:39], v[80:83], v[176:179]\n"
      "v_mfma_f32_16x16x32_bf16 v[180:183], v[36:39], v[84:87], v[180:183]\n"
      "v_mfma_f32_16x16x32_bf16 v[180:183], v[52:55], v[116:119], v[180:183]\n"
      "v_mfma_f32_16x16x32_bf16 v[148:151], v[48:51], v[116:119], v[148:151]\n"
      "v_mfma_f32_16x16x32_bf16 v[148:151], v[32:35], v[84:87], v[148:151]\n"
      "v_mfma_f32_16x16x32_bf16 v[152:155], v[32:35], v[88:91], v[152:155]\n"
      "v_mfma_f32_16x16x32_bf16 v[152:155], v[48:51], v[120:123], v[152:155]\n"
      "v_mfma_f32_16x16x32_bf16 v[184:187], v[52:55], v[120:123], v[184:187]\n"
      "v_mfma_f32_16x16x32_bf16 v[184:187], v[36:39], v[88:91], v[184:187]\n"
      "v_mfma_f32_16x16x32_bf16 v[188:191], v[36:39], v[92:95], v[188:191]\n"
      "v_mfma_f32_16x16x32_bf16 v[188:191], v[52:55], v[124:127], v[188:191]\n"
      "v_mfma_f32_16x16x32_bf16 v[156:159], v[48:51], v[124:127], v[156:159]\n"
      "v_mfma_f32_16x16x32_bf16 v[156:159], v[32:35], v[92:95], v[156:159]\n"
      "s_waitcnt lgkmcnt(0)\n"
      "v_mfma_f32_16x16x32_bf16 v[192:195], v[40:43], v[64:67], v[192:195]\n"
      "s_waitcnt vmcnt(8)\n"
      "v_mfma_f32_16x16x32_bf16 v[192:195], v[56:59], v[96:99], v[192:195]\n"
      "s_barrier\n"
      "v_mfma_f32_16x16x32_bf16 v[224:227], v[60:63], v[96:99], v[224:227]\n"
      "ds_read_b64_tr_b16 v[32:33], %[wa0] offset:0\n"
      "v_mfma_f32_16x16x32_bf16 v[224:227], v[44:47], v[64:67], v[224:227]\n"
      "ds_read_b128 v[64:67], %[xa0] offset:49152\n"
      "v_mfma_f32_16x16x32_bf16 v[228:231], v[44:47], v[68:71], v[228:231]\n"
      "ds_read_b128 v[96:99], %[xa1] offset:49152\n"
      "v_mfma_f32_16x16x32_bf16 v[228:231], v[60:63], v[100:103], v[228:231]\n"
      "ds_read_b64_tr_b16 v[34:35], %[wa0] offset:512\n"
      "v_mfma_f32_16x16x32_bf16 v[196:199], v[56:59], v[100:103], v[196:199]\n"
      "ds_read_b64_tr_b16 v[36:37], %[wa1] offset:0\n"
      "v_mfma_f32_16x16x32_bf16 v[196:199], v[40:43], v[68:71], v[196:199]\n"
      "ds_read_b128 v[68:71], %[xa0] offset:51200\n"
      "v_mfma_f32_16x16x32_bf16 v[200:203], v[40:43], v[72:75], v[200:203]\n"
      "ds_read_b128 v[100:103], %[xa1] offset:51200\n"
      "v_mfma_f32_16x16x32_bf16 v[200:203], v[56:59], v[104:107], v[200:203]\n"
      "ds_read_b64_tr_b16 v[38:39], %[wa1] offset:512\n"
      "v_mfma_f32_16x16x32_bf16 v[232:235], v[60:63], v[104:107], v[232:235]\n"
      "s_waitcnt vmcnt(4)\n"
      "v_mfma_f32_16x16x32_bf16 v[232:235], v[44:47], v[72:75], v[232:235]\n"
      "ds_read_b128 v[72:75], %[xa0] offset:53248\n"
      "v_mfma_f32_16x16x32_bf16 v[236:239], v[44:47], v[76:79], v[236:239]\n"
      "ds_read_b128 v[104:107], %[xa1] offset:53248\n"
      "v_mfma_f32_16x16x32_bf16 v[236:239], v[60:63], v[108:111], v[236:239]\n"
      "ds_read_b64_tr_b16 v[48:49], %[wa0] offset:4096\n"
      "v_mfma_f32_16x16x32_bf16 v[204:207], v[56:59], v[108:111], v[204:207]\n"
      "ds_read_b64_tr_b16 v[50:51], %[wa0] offset:4608\n"
      "v_mfma_f32_16x16x32_bf16 v[204:207], v[40:43], v[76:79], v[204:207]\n"
      "ds_read_b128 v[76:79], %[xa0] offset:55296\n"
      "v_mfma_f32_16x16x32_bf16 v[208:211], v[40:43], v[80:83], v[208:211]\n"
      "ds_read_b128 v[108:111], %[xa1] offset:55296\n"
      "v_mfma_f32_16x16x32_bf16 v[208:211], v[56:59], v[112:115], v[208:211]\n"
      "ds_read_b64_tr_b16 v[52:53], %[wa1] offset:4096\n"
      "v_mfma_f32_16x16x32_bf16 v[240:243], v[60:63], v[112:115], v[240:243]\n"
      "ds_read_b64_tr_b16 v[54:55], %[wa1] offset:4608\n"
      "v_mfma_f32_16x16x32_bf16 v[240:243], v[44:47], v[80:83], v[240:243]\n"
      "ds_read_b128 v[80:83], %[xa0] offset:57344\n"
      "v_mfma_f32_16x16x32_bf16 v[244:247], v[44:47], v[84:87], v[244:247]\n"
      "ds_read_b128 v[112:115], %[xa1] offset:57344\n"
      "v_mfma_f32_16x16x32_bf16 v[244:247], v[60:63], v[116:119], v[244:247]\n"
      "v_mfma_f32_16x16x32_bf16 v[212:215], v[56:59], v[116:119], v[212:215]\n"
      "v_mfma_f32_16x16x32_bf16 v[212:215], v[40:43], v[84:87], v[212:215]\n"
      "ds_read_b128 v[84:87], %[xa0] offset:59392\n"
      "v_mfma_f32_16x16x32_bf16 v[216:219], v[40:43], v[88:91], v[216:219]\n"
      "ds_read_b128 v[116:119], %[xa1] offset:59392\n"
      "v_mfma_f32_16x16x32_bf16 v[216:219], v[56:59], v[120:123], v[216:219]\n"
      "v_mfma_f32_16x16x32_bf16 v[248:251], v[60:63], v[120:123], v[248:251]\n"
      "v_mfma_f32_16x16x32_bf16 v[248:251], v[44:47], v[88:91], v[248:251]\n"
      "ds_read_b128 v[88:91], %[xa0] offset:61440\n"
      "v_mfma_f32_16x16x32_bf16 v[252:255], v[44:47], v[92:95], v[252:255]\n"
      "ds_read_b128 v[120:123], %[xa1] offset:61440\n"
      "v_mfma_f32_16x16x32_bf16 v[252:255], v[60:63], v[124:127], v[252:255]\n"
      "v_mfma_f32_16x16x32_bf16 v[220:223], v[56:59], v[124:127], v[220:223]\n"
      "v_mfma_f32_16x16x32_bf16 v[220:223], v[40:43], v[92:95], v[220:223]\n"
      "ds_read_b128 v[92:95], %[xa0] offset:63488\n"
      "s_waitcnt lgkmcnt(7)\n"
      "v_mfma_f32_16x16x32_bf16 v[128:131], v[32:35], v[64:67], v[128:131]\n"
      "ds_read_b128 v[124:127], %[xa1] offset:63488\n"
      "v_mfma_f32_16x16x32_bf16 v[128:131], v[48:51], v[96:99], v[128:131]\n"
      "v_mfma_f32_16x16x32_bf16 v[160:163], v[52:55], v[96:99], v[160:163]\n"
      "v_mfma_f32_16x16x32_bf16 v[160:163], v[36:39], v[64:67], v[160:163]\n"
      "ds_read_b64_tr_b16 v[40:41], %[wa2] offset:0\n"
      "v_mfma_f32_16x16x32_bf16 v[164:167], v[36:39], v[68:71], v[164:167]\n"
      "v_mfma_f32_16x16x32_bf16 v[164:167], v[52:55], v[100:103], v[164:167]\n"
      "ds_read_b64_tr_b16 v[42:43], %[wa2] offset:512\n"
      "v_mfma_f32_16x16x32_bf16 v[132:135], v[48:51], v[100:103], v[132:135]\n"
      "ds_read_b64_tr_b16 v[56:57], %[wa2] offset:4096\n"
      "v_mfma_f32_16x16x32_bf16 v[132:135], v[32:35], v[68:71], v[132:135]\n"
      "v_mfma_f32_16x16x32_bf16 v[136:139], v[32:35], v[72:75], v[136:139]\n"
      "ds_read_b64_tr_b16 v[58:59], %[wa2] offset:4608\n"
      "v_mfma_f32_16x16x32_bf16 v[136:139], v[48:51], v[104:107], v[136:139]\n"
      "ds_read_b64_tr_b16 v[44:45], %[wa3] offset:0\n"
      "v_mfma_f32_16x16x32_bf16 v[168:171], v[52:55], v[104:107], v[168:171]\n"
      "v_mfma_f32_16x16x32_bf16 v[168:171], v[36:39], v[72:75], v[168:171]\n"
      "v_cvt_pk_bf16_f32 v128, v128, v129\n"
      "ds_read_b64_tr_b16 v[46:47], %[wa3] offset:512\n"
      "v_mfma_f32_16x16x32_bf16 v[172:175], v[36:39], v[76:79], v[172:175]\n"
      "v_cvt_pk_bf16_f32 v129, v130, v131\n"
      "ds_read_b64_tr_b16 v[60:61], %[wa3] offset:4096\n"
      "v_mfma_f32_16x16x32_bf16 v[172:175], v[52:55], v[108:111], v[172:175]\n"
      "v_cvt_pk_bf16_f32 v130, v160, v161\n"
      "v_cvt_pk_bf16_f32 v131, v162, v163\n"
      "v_mfma_f32_16x16x32_bf16 v[140:143], v[48:51], v[108:111], v[140:143]\n"
      "v_pk_max_i16 v128, v128, 0\n"
      "ds_read_b64_tr_b16 v[62:63], %[wa3] offset:4608\n"
      "v_mfma_f32_16x16x32_bf16 v[140:143], v[32:35], v[76:79], v[140:143]\n"
      "v_pk_max_i16 v129, v129, 0\n"
      "v_pk_max_i16 v130, v130, 0\n"
      "s_waitcnt lgkmcnt(8)\n"
      "v_mfma_f32_16x16x32_bf16 v[144:147], v[32:35], v[80:83], v[144:147]\n"
      "v_pk_max_i16 v131, v131, 0\n"
      "v_cvt_pk_bf16_f32 v132, v132, v133\n"
      "v_mfma_f32_16x16x32_bf16 v[144:147], v[48:51], v[112:115], v[144:147]\n"
      "v_cvt_pk_bf16_f32 v133, v134, v135\n"
      "v_cvt_pk_bf16_f32 v134, v164, v165\n"
      "v_mfma_f32_16x16x32_bf16 v[176:179], v[52:55], v[112:115], v[176:179]\n"
      "v_cvt_pk_bf16_f32 v135, v166, v167\n"
      "v_pk_max_i16 v132, v132, 0\n"
      "v_mfma_f32_16x16x32_bf16 v[176:179], v[36:39], v[80:83], v[176:179]\n"
      "v_pk_max_i16 v133, v133, 0\n"
      "v_pk_max_i16 v134, v134, 0\n"
      "v_mfma_f32_16x16x32_bf16 v[180:183], v[36:39], v[84:87], v[180:183]\n"
      "v_pk_max_i16 v135, v135, 0\n"
      "v_cvt_pk_bf16_f32 v136, v136, v137\n"
      "v_mfma_f32_16x16x32_bf16 v[180:183], v[52:55], v[116:119], v[180:183]\n"
      "v_cvt_pk_bf16_f32 v137, v138, v139\n"
      "v_cvt_pk_bf16_f32 v138, v168, v169\n"
      "v_mfma_f32_16x16x32_bf16 v[148:151], v[48:51], v[116:119], v[148:151]\n"
      "v_cvt_pk_bf16_f32 v139, v170, v171\n"
      "v_pk_max_i16 v136, v136, 0\n"
      "v_mfma_f32_16x16x32_bf16 v[148:151], v[32:35], v[84:87], v[148:151]\n"
      "v_pk_max_i16 v137, v137, 0\n"
      "v_pk_max_i16 v138, v138, 0\n"
      "v_mfma_f32_16x16x32_bf16 v[152:155], v[32:35], v[88:91], v[152:155]\n"
      "v_pk_max_i16 v139, v139, 0\n"
      "v_cvt_pk_bf16_f32 v140, v140, v141\n"
      "v_mfma_f32_16x16x32_bf16 v[152:155], v[48:51], v[120:123], v[152:155]\n"
      "v_cvt_pk_bf16_f32 v141, v142, v143\n"
      "v_cvt_pk_bf16_f32 v142, v172, v173\n"
      "v_mfma_f32_16x16x32_bf16 v[184:187], v[52:55], v[120:123], v[184:187]\n"
      "v_cvt_pk_bf16_f32 v143, v174, v175\n"
      "v_pk_max_i16 v140, v140, 0\n"
      "v_mfma_f32_16x16x32_bf16 v[184:187], v[36:39], v[88:91], v[184:187]\n"
      "v_pk_max_i16 v141, v141, 0\n"
      "v_pk_max_i16 v142, v142, 0\n"
      "v_mfma_f32_16x16x32_bf16 v[188:191], v[36:39], v[92:95], v[188:191]\n"
      "v_pk_max_i16 v143, v143, 0\n"
      "v_cvt_pk_bf16_f32 v144, v144, v145\n"
      "v_mfma_f32_16x16x32_bf16 v[188:191], v[52:55], v[124:127], v[188:191]\n"
      "v_cvt_pk_bf16_f32 v145, v146, v147\n"
      "v_cvt_pk_bf16_f32 v146, v176, v177\n"
      "v_mfma_f32_16x16x32_bf16 v[156:159], v[48:51], v[124:127], v[156:159]\n"
      "v_cvt_pk_bf16_f32 v147, v178, v179\n"
      "v_pk_max_i16 v144, v144, 0\n"
      "v_mfma_f32_16x16x32_bf16 v[156:159], v[32:35], v[92:95], v[156:159]\n"
      "v_pk_max_i16 v145, v145, 0\n"
      "v_pk_max_i16 v146, v146, 0\n"
      "s_waitcnt lgkmcnt(0)\n"
      "v_mfma_f32_16x16x32_bf16 v[192:195], v[40:43], v[64:67], v[192:195]\n"
      "v_pk_max_i16 v147, v147, 0\n"
      "v_cvt_pk_bf16_f32 v148, v148, v149\n"
      "v_mfma_f32_16x16x32_bf16 v[192:195], v[56:59], v[96:99], v[192:195]\n"
      "v_cvt_pk_bf16_f32 v149, v150, v151\n"
      "v_cvt_pk_bf16_f32 v150, v180, v181\n"
      "v_mfma_f32_16x16x32_bf16 v[224:227], v[60:63], v[96:99], v[224:227]\n"
      "v_cvt_pk_bf16_f32 v151, v182, v183\n"
      "v_pk_max_i16 v148, v148, 0\n"
      "v_mfma_f32_16x16x32_bf16 v[224:227], v[44:47], v[64:67], v[224:227]\n"
      "v_pk_max_i16 v149, v149, 0\n"
      "v_pk_max_i16 v150, v150, 0\n"
      "v_mfma_f32_16x16x32_bf16 v[228:231], v[44:47], v[68:71], v[228:231]\n"
      "v_pk_max_i16 v151, v151, 0\n"
      "v_cvt_pk_bf16_f32 v152, v152, v153\n"
      "v_mfma_f32_16x16x32_bf16 v[228:231], v[60:63], v[100:103], v[228:231]\n"
      "v_cvt_pk_bf16_f32 v153, v154, v155\n"
      "v_cvt_pk_bf16_f32 v154, v184, v185\n"
      "v_mfma_f32_16x16x32_bf16 v[196:199], v[56:59], v[100:103], v[196:199]\n"
      "v_cvt_pk_bf16_f32 v155, v186, v187\n"
      "v_pk_max_i16 v152, v152, 0\n"
      "v_mfma_f32_16x16x32_bf16 v[196:199], v[40:43], v[68:71], v[196:199]\n"
      "v_pk_max_i16 v153, v153, 0\n"
      "v_pk_max_i16 v154, v154, 0\n"
      "v_mfma_f32_16x16x32_bf16 v[200:203], v[40:43], v[72:75], v[200:203]\n"
      "v_pk_max_i16 v155, v155, 0\n"
      "v_cvt_pk_bf16_f32 v156, v156, v157\n"
      "v_mfma_f32_16x16x32_bf16 v[200:203], v[56:59], v[104:107], v[200:203]\n"
      "v_cvt_pk_bf16_f32 v157, v158, v159\n"
      "v_cvt_pk_bf16_f32 v158, v188, v189\n"
      "v_mfma_f32_16x16x32_bf16 v[232:235], v[60:63], v[104:107], v[232:235]\n"
      "v_cvt_pk_bf16_f32 v159, v190, v191\n"
      "v_pk_max_i16 v156, v156, 0\n"
      "v_mfma_f32_16x16x32_bf16 v[232:235], v[44:47], v[72:75], v[232:235]\n"
      "v_pk_max_i16 v157, v157, 0\n"
      "v_pk_max_i16 v158, v158, 0\n"
      "v_mfma_f32_16x16x32_bf16 v[236:239], v[44:47], v[76:79], v[236:239]\n"
      "v_pk_max_i16 v159, v159, 0\n"
      "v_cvt_pk_bf16_f32 v192, v192, v193\n"
      "v_mfma_f32_16x16x32_bf16 v[236:239], v[60:63], v[108:111], v[236:239]\n"
      "v_cvt_pk_bf16_f32 v193, v194, v195\n"
      "v_cvt_pk_bf16_f32 v194, v224, v225\n"
      "v_mfma_f32_16x16x32_bf16 v[204:207], v[56:59], v[108:111], v[204:207]\n"
      "v_cvt_pk_bf16_f32 v195, v226, v227\n"
      "v_pk_max_i16 v192, v192, 0\n"
      "v_mfma_f32_16x16x32_bf16 v[204:207], v[40:43], v[76:79], v[204:207]\n"
      "v_pk_max_i16 v193, v193, 0\n"
      "v_pk_max_i16 v194, v194, 0\n"
      "v_mfma_f32_16x16x32_bf16 v[208:211], v[40:43], v[80:83], v[208:211]\n"
      "v_pk_max_i16 v195, v195, 0\n"
      "v_cvt_pk_bf16_f32 v196, v196, v197\n"
      "v_mfma_f32_16x16x32_bf16 v[208:211], v[56:59], v[112:115], v[208:211]\n"
      "v_cvt_pk_bf16_f32 v197, v198, v199\n"
      "v_cvt_pk_bf16_f32 v198, v228, v229\n"
      "v_mfma_f32_16x16x32_bf16 v[240:243], v[60:63], v[112:115], v[240:243]\n"
      "v_cvt_pk_bf16_f32 v199, v230, v231\n"
      "v_pk_max_i16 v196, v196, 0\n"
      "v_mfma_f32_16x16x32_bf16 v[240:243], v[44:47], v[80:83], v[240:243]\n"
      "v_pk_max_i16 v197, v197, 0\n"
      "v_pk_max_i16 v198, v198, 0\n"
      "v_mfma_f32_16x16x32_bf16 v[244:247], v[44:47], v[84:87], v[244:247]\n"
      "v_pk_max_i16 v199, v199, 0\n"
      "v_cvt_pk_bf16_f32 v200, v200, v201\n"
      "v_mfma_f32_16x16x32_bf16 v[244:247], v[60:63], v[116:119], v[244:247]\n"
      "v_cvt_pk_bf16_f32 v201, v202, v203\n"
      "v_cvt_pk_bf16_f32 v202, v232, v233\n"
      "v_mfma_f32_16x16x32_bf16 v[212:215], v[56:59], v[116:119], v[212:215]\n"
      "v_cvt_pk_bf16_f32 v203, v234, v235\n"
      "v_pk_max_i16 v200, v200, 0\n"
      "v_mfma_f32_16x16x32_bf16 v[212:215], v[40:43], v[84:87], v[212:215]\n"
      "v_pk_max_i16 v201, v201, 0\n"
      "v_pk_max_i16 v202, v202, 0\n"
      "v_mfma_f32_16x16x32_bf16 v[216:219], v[40:43], v[88:91], v[216:219]\n"
      "v_pk_max_i16 v203, v203, 0\n"
      "v_cvt_pk_bf16_f32 v204, v204, v205\n"
      "v_mfma_f32_16x16x32_bf16 v[216:219], v[56:59], v[120:123], v[216:219]\n"
      "v_cvt_pk_bf16_f32 v205, v206, v207\n"
      "v_cvt_pk_bf16_f32 v206, v236, v237\n"
      "v_mfma_f32_16x16x32_bf16 v[248:251], v[60:63], v[120:123], v[248:251]\n"
      "v_cvt_pk_bf16_f32 v207, v238, v239\n"
      "v_pk_max_i16 v204, v204, 0\n"
      "v_mfma_f32_16x16x32_bf16 v[248:251], v[44:47], v[88:91], v[248:251]\n"
      "v_pk_max_i16 v205, v205, 0\n"
      "v_pk_max_i16 v206, v206, 0\n"
      "v_mfma_f32_16x16x32_bf16 v[252:255], v[44:47], v[92:95], v[252:255]\n"
      "v_pk_max_i16 v207, v207, 0\n"
      "v_cvt_pk_bf16_f32 v208, v208, v209\n"
      "v_mfma_f32_16x16x32_bf16 v[252:255], v[60:63], v[124:127], v[252:255]\n"
      "v_cvt_pk_bf16_f32 v209, v210, v211\n"
      "v_cvt_pk_bf16_f32 v210, v240, v241\n"
      "v_mfma_f32_16x16x32_bf16 v[220:223], v[56:59], v[124:127], v[220:223]\n"
      "v_cvt_pk_bf16_f32 v211, v242, v243\n"
      "v_pk_max_i16 v208, v208, 0\n"
      "v_mfma_f32_16x16x32_bf16 v[220:223], v[40:43], v[92:95], v[220:223]\n"
      "v_pk_max_i16 v209, v209, 0\n"
      "v_pk_max_i16 v210, v210, 0\n"
      "s_waitcnt vmcnt(0)\n"
      "s_nop 7\n"
      "v_pk_max_i16 v211, v211, 0\n"
      "v_cvt_pk_bf16_f32 v212, v212, v213\n"
      "v_cvt_pk_bf16_f32 v213, v214, v215\n"
      "v_cvt_pk_bf16_f32 v214, v244, v245\n"
      "v_cvt_pk_bf16_f32 v215, v246, v247\n"
      "v_pk_max_i16 v212, v212, 0\n"
      "v_pk_max_i16 v213, v213, 0\n"
      "v_pk_max_i16 v214, v214, 0\n"
      "v_pk_max_i16 v215, v215, 0\n"
      "v_cvt_pk_bf16_f32 v216, v216, v217\n"
      "v_cvt_pk_bf16_f32 v217, v218, v219\n"
      "v_cvt_pk_bf16_f32 v218, v248, v249\n"
      "v_cvt_pk_bf16_f32 v219, v250, v251\n"
      "v_pk_max_i16 v216, v216, 0\n"
      "v_pk_max_i16 v217, v217, 0\n"
      "v_pk_max_i16 v218, v218, 0\n"
      "v_pk_max_i16 v219, v219, 0\n"
      "v_cvt_pk_bf16_f32 v220, v220, v221\n"
      "v_cvt_pk_bf16_f32 v221, v222, v223\n"
      "v_cvt_pk_bf16_f32 v222, v252, v253\n"
      "v_cvt_pk_bf16_f32 v223, v254, v255\n"
      "v_pk_max_i16 v220, v220, 0\n"
      "v_pk_max_i16 v221, v221, 0\n"
      "v_pk_max_i16 v222, v222, 0\n"
      "v_pk_max_i16 v223, v223, 0\n"
      "s_nop 1\n"
      "v_mfma_f32_16x16x32_bf16 v[160:163], v[24:27], v[128:131], 0\n"
      "v_mfma_f32_16x16x32_bf16 v[164:167], v[24:27], v[132:135], 0\n"
      "v_mfma_f32_16x16x32_bf16 v[168:171], v[24:27], v[136:139], 0\n"
      "v_mfma_f32_16x16x32_bf16 v[172:175], v[24:27], v[140:143], 0\n"
      "v_mfma_f32_16x16x32_bf16 v[176:179], v[24:27], v[144:147], 0\n"
      "v_mfma_f32_16x16x32_bf16 v[180:183], v[24:27], v[148:151], 0\n"
      "v_mfma_f32_16x16x32_bf16 v[184:187], v[24:27], v[152:155], 0\n"
      "v_mfma_f32_16x16x32_bf16 v[188:191], v[24:27], v[156:159], 0\n"
      "v_mfma_f32_16x16x32_bf16 v[160:163], v[28:31], v[192:195], v[160:163]\n"
      "v_mfma_f32_16x16x32_bf16 v[164:167], v[28:31], v[196:199], v[164:167]\n"
      "v_mfma_f32_16x16x32_bf16 v[168:171], v[28:31], v[200:203], v[168:171]\n"
      "v_mfma_f32_16x16x32_bf16 v[172:175], v[28:31], v[204:207], v[172:175]\n"
      "v_mfma_f32_16x16x32_bf16 v[176:179], v[28:31], v[208:211], v[176:179]\n"
      "v_mfma_f32_16x16x32_bf16 v[180:183], v[28:31], v[212:215], v[180:183]\n"
      "v_mfma_f32_16x16x32_bf16 v[184:187], v[28:31], v[216:219], v[184:187]\n"
      "v_mfma_f32_16x16x32_bf16 v[188:191], v[28:31], v[220:223], v[188:191]\n"
      "s_nop 7\n"
      "v_mov_b32 %[p0], v160\n"
      "v_mov_b32 %[p1], v164\n"
      "v_mov_b32 %[p2], v168\n"
      "v_mov_b32 %[p3], v172\n"
      "v_mov_b32 %[p4], v176\n"
      "v_mov_b32 %[p5], v180\n"
      "v_mov_b32 %[p6], v184\n"
      "v_mov_b32 %[p7], v188\n"
      : [p0] "=v"(p0), [p1] "=v"(p1), [p2] "=v"(p2), [p3] "=v"(p3), [p4] "=v"(p4), [p5] "=v"(p5), [p6] "=v"(p6), [p7] "=v"(p7),
        [sW] "+s"(sW), [sC] "+s"(sC)
      : [xa0] "v"(xa0), [xa1] "v"(xa1),
        [wa0] "v"(wa0), [wa1] "v"(wa1), [wa2] "v"(wa2), [wa3] "v"(wa3), [wv0] "v"(wv0), [wv1] "v"(wv1), [wv2] "v"(wv2), [wv3] "v"(wv3),
        [vb] "v"(vb), [srdW] "s"(srdW), [sWb] "s"(sWb), [pb1] "s"(b1), [pw2] "s"(W2), [d0] "s"(h ? (u32)(-3 * 32768) : 32768u), [d1] "s"(h ? (u32)(5 * 32768) : 32768u)
      : "memory", "scc",
        "v24", "v25", "v26", "v27", "v28", "v29", "v30", "v31", "v32", "v33", "v34", "v35", "v36", "v37", "v38", "v39", "v40", "v41", "v42", "v43", "v44", "v45", "v46", "v47", "v48", "v49", "v50", "v51", "v52", "v53", "v54", "v55", "v56", "v57", "v58", "v59", "v60", "v61", "v62", "v63", "v64", "v65", "v66", "v67", "v68", "v69", "v70", "v71", "v72", "v73", "v74", "v75", "v76", "v77", "v78", "v79", "v80", "v81", "v82", "v83", "v84", "v85", "v86", "v87", "v88", "v89", "v90", "v91", "v92", "v93", "v94", "v95", "v96", "v97", "v98", "v99", "v100", "v101", "v102", "v103", "v104", "v105", "v106", "v107", "v108", "v109", "v110", "v111", "v112", "v113", "v114", "v115", "v116", "v117", "v118", "v119", "v120", "v121", "v122", "v123", "v124", "v125", "v126", "v127", "v128", "v129", "v130", "v131", "v132", "v133", "v134", "v135", "v136", "v137", "v138", "v139", "v140", "v141", "v142", "v143", "v144", "v145", "v146", "v147", "v148", "v149", "v150", "v151", "v152", "v153", "v154", "v155", "v156", "v157", "v158", "v159", "v160", "v161", "v162", "v163", "v164", "v165", "v166", "v167", "v168", "v169", "v170", "v171", "v172", "v173", "v174", "v175", "v176", "v177", "v178", "v179", "v180", "v181", "v182", "v183", "v184", "v185", "v186", "v187", "v188", "v189", "v190", "v191", "v192", "v193", "v194", "v195", "v196", "v197", "v198", "v199", "v200", "v201", "v202", "v203", "v204", "v205", "v206", "v207", "v208", "v209", "v210", "v211", "v212", "v213", "v214", "v215", "v216", "v217", "v218", "v219", "v220", "v221", "v222", "v223", "v224", "v225", "v226", "v227", "v228", "v229", "v230", "v231", "v232", "v233", "v234", "v235", "v236", "v237", "v238", "v239", "v240", "v241", "v242", "v243", "v244", "v245", "v246", "v247", "v248", "v249", "v250", "v251", "v252", "v253", "v254", "v255");
    const float q0 = g == 0u ? p0 : g == 1u ? p1 : g == 2u ? p2 : p3, q1 = g == 0u ? p4 : g == 1u ? p5 : g == 2u ? p6 : p7;
    red[w * 128u + lane] = q0; red[w * 128u + 64u + lane] = q1;
    asm volatile("s_waitcnt lgkmcnt(0)" ::: "memory");
    BAR();
  } else if (w < 4u + NL) {
    const u32 lw = w - 4u;
    const u32x4 srdT = make_srd((const char*)table - 4096, (u32)V * 256u + 4096u);
    const u32x4 srdI = make_srd(idx, BB * FF * 4);
    const u32 r8 = lane >> 3, c8 = lane & 7u;
    const u32 ce = (c8 ^ (r8 >> 1)) << 4, co = (c8 ^ (4u + (r8 >> 1))) << 4;
    const u32 gc0 = ce + 4096u, gc1 = co + 3072u, gc2 = ce + 2048u, gc3 = co + 1024u;
    const u32 vdi = (row0 + lw * (GPL * 8u) + r8) * (FF * 4u);
    const u32 sLb = lw * (GPL * 1024u);
    if (h == 0u) {
    asm volatile(
      "s_nop 4\n"
      "s_setprio 2\n"
      "buffer_load_dwordx4 v[32:35], %[vdi], %[srdI], 0 offen offset:0\n"
      "buffer_load_dwordx4 v[36:39], %[vdi], %[srdI], 0 offen offset:832\n"
      "buffer_load_dwordx4 v[40:43], %[vdi], %[srdI], 0 offen offset:1664\n"
      "buffer_load_dwordx4 v[44:47], %[vdi], %[srdI], 0 offen offset:2496\n"
      "buffer_load_dwordx4 v[48:51], %[vdi], %[srdI], 0 offen offset:3328\n"
      "buffer_load_dwordx4 v[52:55], %[vdi], %[srdI], %[s4k] offen offset:832\n"
      "buffer_load_dwordx4 v[56:59], %[vdi], %[srdI], %[s4k] offen offset:1664\n"
      "buffer_load_dwordx4 v[60:63], %[vdi], %[srdI], %[s4k] offen offset:2496\n"
      "s_add_u32 m0, %[sLb], 0\n"
      "s_waitcnt vmcnt(7)\n"
      "v_add_u32 v31, %[sV], v32\n"
      "v_min_u32 v32, v32, v31\n"
      "v_lshl_add_u32 v32, v32, 8, %[gc0]\n"
      "buffer_load_dwordx4 v32, %[srdT], 0 offen offset:0 lds\n"
      "s_waitcnt vmcnt(7)\n"
      "v_add_u32 v31, %[sV], v36\n"
      "v_min_u32 v36, v36, v31\n"
      "v_lshl_add_u32 v36, v36, 8, %[gc1]\n"
      "buffer_load_dwordx4 v36, %[srdT], 0 offen offset:1024 lds\n"
      "s_waitcnt vmcnt(7)\n"
      "v_add_u32 v31, %[sV], v40\n"
      "v_min_u32 v40, v40, v31\n"
      "v_lshl_add_u32 v40, v40, 8, %[gc2]\n"
      "buffer_load_dwordx4 v40, %[srdT], 0 offen offset:2048 lds\n"
      "s_waitcnt vmcnt(7)\n"
      "v_add_u32 v31, %[sV], v44\n"
      "v_min_u32 v44, v44, v31\n"
      "v_lshl_add_u32 v44, v44, 8, %[gc3]\n"
      "buffer_load_dwordx4 v44, %[srdT], 0 offen offset:3072 lds\n"
      "s_add_u32 m0, %[sLb], 4096\n"
      "s_waitcnt vmcnt(7)\n"
      "v_add_u32 v31, %[sV], v48\n"
      "v_min_u32 v48, v48, v31\n"
      "v_lshl_add_u32 v48, v48, 8, %[gc0]\n"
      "buffer_load_dwordx4 v48, %[srdT], 0 offen offset:0 lds\n"
      "s_waitcnt vmcnt(7)\n"
      "v_add_u32 v31, %[sV], v52\n"
      "v_min_u32 v52, v52, v31\n"
      "v_lshl_add_u32 v52, v52, 8, %[gc1]\n"
      "buffer_load_dwordx4 v52, %[srdT], 0 offen offset:1024 lds\n"
      "s_waitcnt vmcnt(7)\n"
      "v_add_u32 v31, %[sV], v56\n"
      "v_min_u32 v56, v56, v31\n"
      "v_lshl_add_u32 v56, v56, 8, %[gc2]\n"
      "buffer_load_dwordx4 v56, %[srdT], 0 offen offset:2048 lds\n"
      "s_waitcnt vmcnt(7)\n"
      "v_add_u32 v31, %[sV], v60\n"
      "v_min_u32 v60, v60, v31\n"
      "v_lshl_add_u32 v60, v60, 8, %[gc3]\n"
      "buffer_load_dwordx4 v60, %[srdT], 0 offen offset:3072 lds\n"
      "s_add_u32 m0, %[sLb], 16384\n"
      "s_nop 0\n"
      "buffer_load_dwordx4 v32, %[srdT], %[s128] offen offset:0 lds\n"
      "buffer_load_dwordx4 v36, %[srdT], %[s128] offen offset:1024 lds\n"
      "buffer_load_dwordx4 v40, %[srdT], %[s128] offen offset:2048 lds\n"
      "buffer_load_dwordx4 v44, %[srdT], %[s128] offen offset:3072 lds\n"
      "s_add_u32 m0, %[sLb], 20480\n"
      "s_nop 0\n"
      "buffer_load_dwordx4 v48, %[srdT], %[s128] offen offset:0 lds\n"
      "buffer_load_dwordx4 v52, %[srdT], %[s128] offen offset:1024 lds\n"
      "buffer_load_dwordx4 v56, %[srdT], %[s128] offen offset:2048 lds\n"
      "buffer_load_dwordx4 v60, %[srdT], %[s128] offen offset:3072 lds\n"
      "s_waitcnt vmcnt(8)\n"
      "s_barrier\n"
      "s_add_u32 m0, %[sLb], 32768\n"
      "v_add_u32 v31, %[sV], v33\n"
      "v_min_u32 v33, v33, v31\n"
      "v_lshl_add_u32 v33, v33, 8, %[gc0]\n"
      "buffer_load_dwordx4 v33, %[srdT], 0 offen offset:0 lds\n"
      "v_add_u32 v31, %[sV], v37\n"
      "v_min_u32 v37, v37, v31\n"
      "v_lshl_add_u32 v37, v37, 8, %[gc1]\n"
      "buffer_load_dwordx4 v37, %[srdT], 0 offen offset:1024 lds\n"
      "v_add_u32 v31, %[sV], v41\n"
      "v_min_u32 v41, v41, v31\n"
      "v_lshl_add_u32 v41, v41, 8, %[gc2]\n"
      "buffer_load_dwordx4 v41, %[srdT], 0 offen offset:2048 lds\n"
      "v_add_u32 v31, %[sV], v45\n"
      "v_min_u32 v45, v45, v31\n"
      "v_lshl_add_u32 v45, v45, 8, %[gc3]\n"
      "buffer_load_dwordx4 v45, %[srdT], 0 offen offset:3072 lds\n"
      "s_add_u32 m0, %[sLb], 36864\n"
      "v_add_u32 v31, %[sV], v49\n"
      "v_min_u32 v49, v49, v31\n"
      "v_lshl_add_u32 v49, v49, 8, %[gc0]\n"
      "buffer_load_dwordx4 v49, %[srdT], 0 offen offset:0 lds\n"
      "v_add_u32 v31, %[sV], v53\n"
      "v_min_u32 v53, v53, v31\n"
      "v_lshl_add_u32 v53, v53, 8, %[gc1]\n"
      "buffer_load_dwordx4 v53, %[srdT], 0 offen offset:1024 lds\n"
      "v_add_u32 v31, %[sV], v57\n"
      "v_min_u32 v57, v57, v31\n"
      "v_lshl_add_u32 v57, v57, 8, %[gc2]\n"
      "buffer_load_dwordx4 v57, %[srdT], 0 offen offset:2048 lds\n"
      "v_add_u32 v31, %[sV], v61\n"
      "v_min_u32 v61, v61, v31\n"
      "v_lshl_add_u32 v61, v61, 8, %[gc3]\n"
      "buffer_load_dwordx4 v61, %[srdT], 0 offen offset:3072 lds\n"
      "s_add_u32 m0, %[sLb], 49152\n"
      "s_nop 0\n"
      "buffer_load_dwordx4 v33, %[srdT], %[s128] offen offset:0 lds\n"
      "buffer_load_dwordx4 v37, %[srdT], %[s128] offen offset:1024 lds\n"
      "buffer_load_dwordx4 v41, %[srdT], %[s128] offen offset:2048 lds\n"
      "buffer_load_dwordx4 v45, %[srdT], %[s128] offen offset:3072 lds\n"
      "s_add_u32 m0, %[sLb], 53248\n"
      "s_nop 0\n"
      "buffer_load_dwordx4 v49, %[srdT], %[s128] offen offset:0 lds\n"
      "buffer_load_dwordx4 v53, %[srdT], %[s128] offen offset:1024 lds\n"
      "buffer_load_dwordx4 v57, %[srdT], %[s128] offen offset:2048 lds\n"
      "buffer_load_dwordx4 v61, %[srdT], %[s128] offen offset:3072 lds\n"
      "buffer_load_dwordx4 v[64:67], %[vdi], %[srdI], 0 offen offset:16\n"
      "buffer_load_dwordx4 v[68:71], %[vdi], %[srdI], 0 offen offset:848\n"
      "buffer_load_dwordx4 v[72:75], %[vdi], %[srdI], 0 offen offset:1680\n"
      "buffer_load_dwordx4 v[76:79], %[vdi], %[srdI], 0 offen offset:2512\n"
      "buffer_load_dwordx4 v[80:83], %[vdi], %[srdI], 0 offen offset:3344\n"
      "buffer_load_dwordx4 v[84:87], %[vdi], %[srdI], %[s4k] offen offset:848\n"
      "buffer_load_dwordx4 v[88:91], %[vdi], %[srdI], %[s4k] offen offset:1680\n"
      "buffer_load_dwordx4 v[92:95], %[vdi], %[srdI], %[s4k] offen offset:2512\n"
      "s_waitcnt vmcnt(24)\n"
      "s_barrier\n"
      "s_add_u32 m0, %[sLb], 0\n"
      "v_add_u32 v31, %[sV], v34\n"
      "v_min_u32 v34, v34, v31\n"
      "v_lshl_add_u32 v34, v34, 8, %[gc0]\n"
      "buffer_load_dwordx4 v34, %[srdT], 0 offen offset:0 lds\n"
      "v_add_u32 v31, %[sV], v38\n"
      "v_min_u32 v38, v38, v31\n"
      "v_lshl_add_u32 v38, v38, 8, %[gc1]\n"
      "buffer_load_dwordx4 v38, %[srdT], 0 offen offset:1024 lds\n"
      "v_add_u32 v31, %[sV], v42\n"
      "v_min_u32 v42, v42, v31\n"
      "v_lshl_add_u32 v42, v42, 8, %[gc2]\n"
      "buffer_load_dwordx4 v42, %[srdT], 0 offen offset:2048 lds\n"
      "v_add_u32 v31, %[sV], v46\n"
      "v_min_u32 v46, v46, v31\n"
      "v_lshl_add_u32 v46, v46, 8, %[gc3]\n"
      "buffer_load_dwordx4 v46, %[srdT], 0 offen offset:3072 lds\n"
      "s_add_u32 m0, %[sLb], 4096\n"
      "v_add_u32 v31, %[sV], v50\n"
      "v_min_u32 v50, v50, v31\n"
      "v_lshl_add_u32 v50, v50, 8, %[gc0]\n"
      "buffer_load_dwordx4 v50, %[srdT], 0 offen offset:0 lds\n"
      "v_add_u32 v31, %[sV], v54\n"
      "v_min_u32 v54, v54, v31\n"
      "v_lshl_add_u32 v54, v54, 8, %[gc1]\n"
      "buffer_load_dwordx4 v54, %[srdT], 0 offen offset:1024 lds\n"
      "v_add_u32 v31, %[sV], v58\n"
      "v_min_u32 v58, v58, v31\n"
      "v_lshl_add_u32 v58, v58, 8, %[gc2]\n"
      "buffer_load_dwordx4 v58, %[srdT], 0 offen offset:2048 lds\n"
      "v_add_u32 v31, %[sV], v62\n"
      "v_min_u32 v62, v62, v31\n"
      "v_lshl_add_u32 v62, v62, 8, %[gc3]\n"
      "buffer_load_dwordx4 v62, %[srdT], 0 offen offset:3072 lds\n"
      "s_waitcnt vmcnt(24)\n"
      "s_barrier\n"
      "s_add_u32 m0, %[sLb], 16384\n"
      "s_nop 0\n"
      "buffer_load_dwordx4 v34, %[srdT], %[s128] offen offset:0 lds\n"
      "buffer_load_dwordx4 v38, %[srdT], %[s128] offen offset:1024 lds\n"
      "buffer_load_dwordx4 v42, %[srdT], %[s128] offen offset:2048 lds\n"
      "buffer_load_dwordx4 v46, %[srdT], %[s128] offen offset:3072 lds\n"
      "s_add_u32 m0, %[sLb], 20480\n"
      "s_nop 0\n"
      "buffer_load_dwordx4 v50, %[srdT], %[s128] offen offset:0 lds\n"
      "buffer_load_dwordx4 v54, %[srdT], %[s128] offen offset:1024 lds\n"
      "buffer_load_dwordx4 v58, %[srdT], %[s128] offen offset:2048 lds\n"
      "buffer_load_dwordx4 v62, %[srdT], %[s128] offen offset:3072 lds\n"
      "s_waitcnt vmcnt(24)\n"
      "s_barrier\n"
      "s_add_u32 m0, %[sLb], 32768\n"
      "v_add_u32 v31, %[sV], v35\n"
      "v_min_u32 v35, v35, v31\n"
      "v_lshl_add_u32 v35, v35, 8, %[gc0]\n"
      "buffer_load_dwordx4 v35, %[srdT], 0 offen offset:0 lds\n"
      "v_add_u32 v31, %[sV], v39\n"
      "v_min_u32 v39, v39, v31\n"
      "v_lshl_add_u32 v39, v39, 8, %[gc1]\n"
      "buffer_load_dwordx4 v39, %[srdT], 0 offen offset:1024 lds\n"
      "v_add_u32 v31, %[sV], v43\n"
      "v_min_u32 v43, v43, v31\n"
      "v_lshl_add_u32 v43, v43, 8, %[gc2]\n"
      "buffer_load_dwordx4 v43, %[srdT], 0 offen offset:2048 lds\n"
      "v_add_u32 v31, %[sV], v47\n"
      "v_min_u32 v47, v47, v31\n"
      "v_lshl_add_u32 v47, v47, 8, %[gc3]\n"
      "buffer_load_dwordx4 v47, %[srdT], 0 offen offset:3072 lds\n"
      "s_add_u32 m0, %[sLb], 36864\n"
      "v_add_u32 v31, %[sV], v51\n"
      "v_min_u32 v51, v51, v31\n"
      "v_lshl_add_u32 v51, v51, 8, %[gc0]\n"
      "buffer_load_dwordx4 v51, %[srdT], 0 offen offset:0 lds\n"
      "v_add_u32 v31, %[sV], v55\n"
      "v_min_u32 v55, v55, v31\n"
      "v_lshl_add_u32 v55, v55, 8, %[gc1]\n"
      "buffer_load_dwordx4 v55, %[srdT], 0 offen offset:1024 lds\n"
      "v_add_u32 v31, %[sV], v59\n"
      "v_min_u32 v59, v59, v31\n"
      "v_lshl_add_u32 v59, v59, 8, %[gc2]\n"
      "buffer_load_dwordx4 v59, %[srdT], 0 offen offset:2048 lds\n"
      "v_add_u32 v31, %[sV], v63\n"
      "v_min_u32 v63, v63, v31\n"
      "v_lshl_add_u32 v63, v63, 8, %[gc3]\n"
      "buffer_load_dwordx4 v63, %[srdT], 0 offen offset:3072 lds\n"
      "s_waitcnt vmcnt(16)\n"
      "s_barrier\n"
      "s_add_u32 m0, %[sLb], 49152\n"
      "s_nop 0\n"
      "buffer_load_dwordx4 v35, %[srdT], %[s128] offen offset:0 lds\n"
      "buffer_load_dwordx4 v39, %[srdT], %[s128] offen offset:1024 lds\n"
      "buffer_load_dwordx4 v43, %[srdT], %[s128] offen offset:2048 lds\n"
      "buffer_load_dwordx4 v47, %[srdT], %[s128] offen offset:3072 lds\n"
      "s_add_u32 m0, %[sLb], 53248\n"
      "s_nop 0\n"
      "buffer_load_dwordx4 v51, %[srdT], %[s128] offen offset:0 lds\n"
      "buffer_load_dwordx4 v55, %[srdT], %[s128] offen offset:1024 lds\n"
      "buffer_load_dwordx4 v59, %[srdT], %[s128] offen offset:2048 lds\n"
      "buffer_load_dwordx4 v63, %[srdT], %[s128] offen offset:3072 lds\n"
      "buffer_load_dwordx4 v[96:99], %[vdi], %[srdI], 0 offen offset:32\n"
      "buffer_load_dwordx4 v[100:103], %[vdi], %[srdI], 0 offen offset:864\n"
      "buffer_load_dwordx4 v[104:107], %[vdi], %[srdI], 0 offen offset:1696\n"
      "buffer_load_dwordx4 v[108:111], %[vdi], %[srdI], 0 offen offset:2528\n"
      "buffer_load_dwordx4 v[112:115], %[vdi], %[srdI], 0 offen offset:3360\n"
      "buffer_load_dwordx4 v[116:119], %[vdi], %[srdI], %[s4k] offen offset:864\n"
      "buffer_load_dwordx4 v[120:123], %[vdi], %[srdI], %[s4k] offen offset:1696\n"
      "buffer_load_dwordx4 v[124:127], %[vdi], %[srdI], %[s4k] offen offset:2528\n"
      "s_waitcnt vmcnt(24)\n"
      "s_barrier\n"
      "s_add_u32 m0, %[sLb], 0\n"
      "v_add_u32 v31, %[sV], v64\n"
      "v_min_u32 v64, v64, v31\n"
      "v_lshl_add_u32 v64, v64, 8, %[gc0]\n"
      "buffer_load_dwordx4 v64, %[srdT], 0 offen offset:0 lds\n"
      "v_add_u32 v31, %[sV], v68\n"
      "v_min_u32 v68, v68, v31\n"
      "v_lshl_add_u32 v68, v68, 8, %[gc1]\n"
      "buffer_load_dwordx4 v68, %[srdT], 0 offen offset:1024 lds\n"
      "v_add_u32 v31, %[sV], v72\n"
      "v_min_u32 v72, v72, v31\n"
      "v_lshl_add_u32 v72, v72, 8, %[gc2]\n"
      "buffer_load_dwordx4 v72, %[srdT], 0 offen offset:2048 lds\n"
      "v_add_u32 v31, %[sV], v76\n"
      "v_min_u32 v76, v76, v31\n"
      "v_lshl_add_u32 v76, v76, 8, %[gc3]\n"
      "buffer_load_dwordx4 v76, %[srdT], 0 offen offset:3072 lds\n"
      "s_add_u32 m0, %[sLb], 4096\n"
      "v_add_u32 v31, %[sV], v80\n"
      "v_min_u32 v80, v80, v31\n"
      "v_lshl_add_u32 v80, v80, 8, %[gc0]\n"
      "buffer_load_dwordx4 v80, %[srdT], 0 offen offset:0 lds\n"
      "v_add_u32 v31, %[sV], v84\n"
      "v_min_u32 v84, v84, v31\n"
      "v_lshl_add_u32 v84, v84, 8, %[gc1]\n"
      "buffer_load_dwordx4 v84, %[srdT], 0 offen offset:1024 lds\n"
      "v_add_u32 v31, %[sV], v88\n"
      "v_min_u32 v88, v88, v31\n"
      "v_lshl_add_u32 v88, v88, 8, %[gc2]\n"
      "buffer_load_dwordx4 v88, %[srdT], 0 offen offset:2048 lds\n"
      "v_add_u32 v31, %[sV], v92\n"
      "v_min_u32 v92, v92, v31\n"
      "v_lshl_add_u32 v92, v92, 8, %[gc3]\n"
      "buffer_load_dwordx4 v92, %[srdT], 0 offen offset:3072 lds\n"
      "s_waitcnt vmcnt(24)\n"
      "s_barrier\n"
      "s_add_u32 m0, %[sLb], 16384\n"
      "s_nop 0\n"
      "buffer_load_dwordx4 v64, %[srdT], %[s128] offen offset:0 lds\n"
      "buffer_load_dwordx4 v68, %[srdT], %[s128] offen offset:1024 lds\n"
      "buffer_load_dwordx4 v72, %[srdT], %[s128] offen offset:2048 lds\n"
      "buffer_load_dwordx4 v76, %[srdT], %[s128] offen offset:3072 lds\n"
      "s_add_u32 m0, %[sLb], 20480\n"
      "s_nop 0\n"
      "buffer_load_dwordx4 v80, %[srdT], %[s128] offen offset:0 lds\n"
      "buffer_load_dwordx4 v84, %[srdT], %[s128] offen offset:1024 lds\n"
      "buffer_load_dwordx4 v88, %[srdT], %[s128] offen offset:2048 lds\n"
      "buffer_load_dwordx4 v92, %[srdT], %[s128] offen offset:3072 lds\n"
      "s_waitcnt vmcnt(24)\n"
      "s_barrier\n"
      "s_add_u32 m0, %[sLb], 32768\n"
      "v_add_u32 v31, %[sV], v65\n"
      "v_min_u32 v65, v65, v31\n"
      "v_lshl_add_u32 v65, v65, 8, %[gc0]\n"
      "buffer_load_dwordx4 v65, %[srdT], 0 offen offset:0 lds\n"
      "v_add_u32 v31, %[sV], v69\n"
      "v_min_u32 v69, v69, v31\n"
      "v_lshl_add_u32 v69, v69, 8, %[gc1]\n"
      "buffer_load_dwordx4 v69, %[srdT], 0 offen offset:1024 lds\n"
      "v_add_u32 v31, %[sV], v73\n"
      "v_min_u32 v73, v73, v31\n"
      "v_lshl_add_u32 v73, v73, 8, %[gc2]\n"
      "buffer_load_dwordx4 v73, %[srdT], 0 offen offset:2048 lds\n"
      "v_add_u32 v31, %[sV], v77\n"
      "v_min_u32 v77, v77, v31\n"
      "v_lshl_add_u32 v77, v77, 8, %[gc3]\n"
      "buffer_load_dwordx4 v77, %[srdT], 0 offen offset:3072 lds\n"
      "s_add_u32 m0, %[sLb], 36864\n"
      "v_add_u32 v31, %[sV], v81\n"
      "v_min_u32 v81, v81, v31\n"
      "v_lshl_add_u32 v81, v81, 8, %[gc0]\n"
      "buffer_load_dwordx4 v81, %[srdT], 0 offen offset:0 lds\n"
      "v_add_u32 v31, %[sV], v85\n"
      "v_min_u32 v85, v85, v31\n"
      "v_lshl_add_u32 v85, v85, 8, %[gc1]\n"
      "buffer_load_dwordx4 v85, %[srdT], 0 offen offset:1024 lds\n"
      "v_add_u32 v31, %[sV], v89\n"
      "v_min_u32 v89, v89, v31\n"
      "v_lshl_add_u32 v89, v89, 8, %[gc2]\n"
      "buffer_load_dwordx4 v89, %[srdT], 0 offen offset:2048 lds\n"
      "v_add_u32 v31, %[sV], v93\n"
      "v_min_u32 v93, v93, v31\n"
      "v_lshl_add_u32 v93, v93, 8, %[gc3]\n"
      "buffer_load_dwordx4 v93, %[srdT], 0 offen offset:3072 lds\n"
      "s_waitcnt vmcnt(16)\n"
      "s_barrier\n"
      "s_add_u32 m0, %[sLb], 49152\n"
      "s_nop 0\n"
      "buffer_load_dwordx4 v65, %[srdT], %[s128] offen offset:0 lds\n"
      "buffer_load_dwordx4 v69, %[srdT], %[s128] offen offset:1024 lds\n"
      "buffer_load_dwordx4 v73, %[srdT], %[s128] offen offset:2048 lds\n"
      "buffer_load_dwordx4 v77, %[srdT], %[s128] offen offset:3072 lds\n"
      "s_add_u32 m0, %[sLb], 53248\n"
      "s_nop 0\n"
      "buffer_load_dwordx4 v81, %[srdT], %[s128] offen offset:0 lds\n"
      "buffer_load_dwordx4 v85, %[srdT], %[s128] offen offset:1024 lds\n"
      "buffer_load_dwordx4 v89, %[srdT], %[s128] offen offset:2048 lds\n"
      "buffer_load_dwordx4 v93, %[srdT], %[s128] offen offset:3072 lds\n"
      "buffer_load_dwordx4 v[32:35], %[vdi], %[srdI], 0 offen offset:48\n"
      "s_waitcnt vmcnt(17)\n"
      "s_barrier\n"
      "s_add_u32 m0, %[sLb], 0\n"
      "v_add_u32 v31, %[sV], v66\n"
      "v_min_u32 v66, v66, v31\n"
      "v_lshl_add_u32 v66, v66, 8, %[gc0]\n"
      "buffer_load_dwordx4 v66, %[srdT], 0 offen offset:0 lds\n"
      "v_add_u32 v31, %[sV], v70\n"
      "v_min_u32 v70, v70, v31\n"
      "v_lshl_add_u32 v70, v70, 8, %[gc1]\n"
      "buffer_load_dwordx4 v70, %[srdT], 0 offen offset:1024 lds\n"
      "v_add_u32 v31, %[sV], v74\n"
      "v_min_u32 v74, v74, v31\n"
      "v_lshl_add_u32 v74, v74, 8, %[gc2]\n"
      "buffer_load_dwordx4 v74, %[srdT], 0 offen offset:2048 lds\n"
      "v_add_u32 v31, %[sV], v78\n"
      "v_min_u32 v78, v78, v31\n"
      "v_lshl_add_u32 v78, v78, 8, %[gc3]\n"
      "buffer_load_dwordx4 v78, %[srdT], 0 offen offset:3072 lds\n"
      "s_add_u32 m0, %[sLb], 4096\n"
      "v_add_u32 v31, %[sV], v82\n"
      "v_min_u32 v82, v82, v31\n"
      "v_lshl_add_u32 v82, v82, 8, %[gc0]\n"
      "buffer_load_dwordx4 v82, %[srdT], 0 offen offset:0 lds\n"
      "v_add_u32 v31, %[sV], v86\n"
      "v_min_u32 v86, v86, v31\n"
      "v_lshl_add_u32 v86, v86, 8, %[gc1]\n"
      "buffer_load_dwordx4 v86, %[srdT], 0 offen offset:1024 lds\n"
      "v_add_u32 v31, %[sV], v90\n"
      "v_min_u32 v90, v90, v31\n"
      "v_lshl_add_u32 v90, v90, 8, %[gc2]\n"
      "buffer_load_dwordx4 v90, %[srdT], 0 offen offset:2048 lds\n"
      "v_add_u32 v31, %[sV], v94\n"
      "v_min_u32 v94, v94, v31\n"
      "v_lshl_add_u32 v94, v94, 8, %[gc3]\n"
      "buffer_load_dwordx4 v94, %[srdT], 0 offen offset:3072 lds\n"
      "buffer_load_dwordx4 v[36:39], %[vdi], %[srdI], 0 offen offset:880\n"
      "s_waitcnt vmcnt(18)\n"
      "s_barrier\n"
      "s_add_u32 m0, %[sLb], 16384\n"
      "s_nop 0\n"
      "buffer_load_dwordx4 v66, %[srdT], %[s128] offen offset:0 lds\n"
      "buffer_load_dwordx4 v70, %[srdT], %[s128] offen offset:1024 lds\n"
      "buffer_load_dwordx4 v74, %[srdT], %[s128] offen offset:2048 lds\n"
      "buffer_load_dwordx4 v78, %[srdT], %[s128] offen offset:3072 lds\n"
      "s_add_u32 m0, %[sLb], 20480\n"
      "s_nop 0\n"
      "buffer_load_dwordx4 v82, %[srdT], %[s128] offen offset:0 lds\n"
      "buffer_load_dwordx4 v86, %[srdT], %[s128] offen offset:1024 lds\n"
      "buffer_load_dwordx4 v90, %[srdT], %[s128] offen offset:2048 lds\n"
      "buffer_load_dwordx4 v94, %[srdT], %[s128] offen offset:3072 lds\n"
      "buffer_load_dwordx4 v[40:43], %[vdi], %[srdI], 0 offen offset:1712\n"
      "s_waitcnt vmcnt(19)\n"
      "s_barrier\n"
      "s_add_u32 m0, %[sLb], 32768\n"
      "v_add_u32 v31, %[sV], v67\n"
      "v_min_u32 v67, v67, v31\n"
      "v_lshl_add_u32 v67, v67, 8, %[gc0]\n"
      "buffer_load_dwordx4 v67, %[srdT], 0 offen offset:0 lds\n"
      "v_add_u32 v31, %[sV], v71\n"
      "v_min_u32 v71, v71, v31\n"
      "v_lshl_add_u32 v71, v71, 8, %[gc1]\n"
      "buffer_load_dwordx4 v71, %[srdT], 0 offen offset:1024 lds\n"
      "v_add_u32 v31, %[sV], v75\n"
      "v_min_u32 v75, v75, v31\n"
      "v_lshl_add_u32 v75, v75, 8, %[gc2]\n"
      "buffer_load_dwordx4 v75, %[srdT], 0 offen offset:2048 lds\n"
      "v_add_u32 v31, %[sV], v79\n"
      "v_min_u32 v79, v79, v31\n"
      "v_lshl_add_u32 v79, v79, 8, %[gc3]\n"
      "buffer_load_dwordx4 v79, %[srdT], 0 offen offset:3072 lds\n"
      "s_add_u32 m0, %[sLb], 36864\n"
      "v_add_u32 v31, %[sV], v83\n"
      "v_min_u32 v83, v83, v31\n"
      "v_lshl_add_u32 v83, v83, 8, %[gc0]\n"
      "buffer_load_dwordx4 v83, %[srdT], 0 offen offset:0 lds\n"
      "v_add_u32 v31, %[sV], v87\n"
      "v_min_u32 v87, v87, v31\n"
      "v_lshl_add_u32 v87, v87, 8, %[gc1]\n"
      "buffer_load_dwordx4 v87, %[srdT], 0 offen offset:1024 lds\n"
      "v_add_u32 v31, %[sV], v91\n"
      "v_min_u32 v91, v91, v31\n"
      "v_lshl_add_u32 v91, v91, 8, %[gc2]\n"
      "buffer_load_dwordx4 v91, %[srdT], 0 offen offset:2048 lds\n"
      "v_add_u32 v31, %[sV], v95\n"
      "v_min_u32 v95, v95, v31\n"
      "v_lshl_add_u32 v95, v95, 8, %[gc3]\n"
      "buffer_load_dwordx4 v95, %[srdT], 0 offen offset:3072 lds\n"
      "buffer_load_dwordx4 v[44:47], %[vdi], %[srdI], 0 offen offset:2544\n"
      "s_waitcnt vmcnt(19)\n"
      "s_barrier\n"
      "s_add_u32 m0, %[sLb], 49152\n"
      "s_nop 0\n"
      "buffer_load_dwordx4 v67, %[srdT], %[s128] offen offset:0 lds\n"
      "buffer_load_dwordx4 v71, %[srdT], %[s128] offen offset:1024 lds\n"
      "buffer_load_dwordx4 v75, %[srdT], %[s128] offen offset:2048 lds\n"
      "buffer_load_dwordx4 v79, %[srdT], %[s128] offen offset:3072 lds\n"
      "s_add_u32 m0, %[sLb], 53248\n"
      "s_nop 0\n"
      "buffer_load_dwordx4 v83, %[srdT], %[s128] offen offset:0 lds\n"
      "buffer_load_dwordx4 v87, %[srdT], %[s128] offen offset:1024 lds\n"
      "buffer_load_dwordx4 v91, %[srdT], %[s128] offen offset:2048 lds\n"
      "buffer_load_dwordx4 v95, %[srdT], %[s128] offen offset:3072 lds\n"
      "buffer_load_dwordx4 v[48:51], %[vdi], %[srdI], 0 offen offset:3376\n"
      "s_waitcnt vmcnt(19)\n"
      "s_barrier\n"
      "s_add_u32 m0, %[sLb], 0\n"
      "v_add_u32 v31, %[sV], v96\n"
      "v_min_u32 v96, v96, v31\n"
      "v_lshl_add_u32 v96, v96, 8, %[gc0]\n"
      "buffer_load_dwordx4 v96, %[srdT], 0 offen offset:0 lds\n"
      "v_add_u32 v31, %[sV], v100\n"
      "v_min_u32 v100, v100, v31\n"
      "v_lshl_add_u32 v100, v100, 8, %[gc1]\n"
      "buffer_load_dwordx4 v100, %[srdT], 0 offen offset:1024 lds\n"
      "v_add_u32 v31, %[sV], v104\n"
      "v_min_u32 v104, v104, v31\n"
      "v_lshl_add_u32 v104, v104, 8, %[gc2]\n"
      "buffer_load_dwordx4 v104, %[srdT], 0 offen offset:2048 lds\n"
      "v_add_u32 v31, %[sV], v108\n"
      "v_min_u32 v108, v108, v31\n"
      "v_lshl_add_u32 v108, v108, 8, %[gc3]\n"
      "buffer_load_dwordx4 v108, %[srdT], 0 offen offset:3072 lds\n"
      "s_add_u32 m0, %[sLb], 4096\n"
      "v_add_u32 v31, %[sV], v112\n"
      "v_min_u32 v112, v112, v31\n"
      "v_lshl_add_u32 v112, v112, 8, %[gc0]\n"
      "buffer_load_dwordx4 v112, %[srdT], 0 offen offset:0 lds\n"
      "v_add_u32 v31, %[sV], v116\n"
      "v_min_u32 v116, v116, v31\n"
      "v_lshl_add_u32 v116, v116, 8, %[gc1]\n"
      "buffer_load_dwordx4 v116, %[srdT], 0 offen offset:1024 lds\n"
      "v_add_u32 v31, %[sV], v120\n"
      "v_min_u32 v120, v120, v31\n"
      "v_lshl_add_u32 v120, v120, 8, %[gc2]\n"
      "buffer_load_dwordx4 v120, %[srdT], 0 offen offset:2048 lds\n"
      "v_add_u32 v31, %[sV], v124\n"
      "v_min_u32 v124, v124, v31\n"
      "v_lshl_add_u32 v124, v124, 8, %[gc3]\n"
      "buffer_load_dwordx4 v124, %[srdT], 0 offen offset:3072 lds\n"
      "buffer_load_dwordx4 v[52:55], %[vdi], %[srdI], %[s4k] offen offset:880\n"
      "s_waitcnt vmcnt(19)\n"
      "s_barrier\n"
      "s_add_u32 m0, %[sLb], 16384\n"
      "s_nop 0\n"
      "buffer_load_dwordx4 v96, %[srdT], %[s128] offen offset:0 lds\n"
      "buffer_load_dwordx4 v100, %[srdT], %[s128] offen offset:1024 lds\n"
      "buffer_load_dwordx4 v104, %[srdT], %[s128] offen offset:2048 lds\n"
      "buffer_load_dwordx4 v108, %[srdT], %[s128] offen offset:3072 lds\n"
      "s_add_u32 m0, %[sLb], 20480\n"
      "s_nop 0\n"
      "buffer_load_dwordx4 v112, %[srdT], %[s128] offen offset:0 lds\n"
      "buffer_load_dwordx4 v116, %[srdT], %[s128] offen offset:1024 lds\n"
      "buffer_load_dwordx4 v120, %[srdT], %[s128] offen offset:2048 lds\n"
      "buffer_load_dwordx4 v124, %[srdT], %[s128] offen offset:3072 lds\n"
      "buffer_load_dwordx4 v[56:59], %[vdi], %[srdI], %[s4k] offen offset:1712\n"
      "s_waitcnt vmcnt(19)\n"
      "s_barrier\n"
      "s_add_u32 m0, %[sLb], 32768\n"
      "v_add_u32 v31, %[sV], v97\n"
      "v_min_u32 v97, v97, v31\n"
      "v_lshl_add_u32 v97, v97, 8, %[gc0]\n"
      "buffer_load_dwordx4 v97, %[srdT], 0 offen offset:0 lds\n"
      "v_add_u32 v31, %[sV], v101\n"
      "v_min_u32 v101, v101, v31\n"
      "v_lshl_add_u32 v101, v101, 8, %[gc1]\n"
      "buffer_load_dwordx4 v101, %[srdT], 0 offen offset:1024 lds\n"
      "v_add_u32 v31, %[sV], v105\n"
      "v_min_u32 v105, v105, v31\n"
      "v_lshl_add_u32 v105, v105, 8, %[gc2]\n"
      "buffer_load_dwordx4 v105, %[srdT], 0 offen offset:2048 lds\n"
      "v_add_u32 v31, %[sV], v109\n"
      "v_min_u32 v109, v109, v31\n"
      "v_lshl_add_u32 v109, v109, 8, %[gc3]\n"
      "buffer_load_dwordx4 v109, %[srdT], 0 offen offset:3072 lds\n"
      "s_add_u32 m0, %[sLb], 36864\n"
      "v_add_u32 v31, %[sV], v113\n"
      "v_min_u32 v113, v113, v31\n"
      "v_lshl_add_u32 v113, v113, 8, %[gc0]\n"
      "buffer_load_dwordx4 v113, %[srdT], 0 offen offset:0 lds\n"
      "v_add_u32 v31, %[sV], v117\n"
      "v_min_u32 v117, v117, v31\n"
      "v_lshl_add_u32 v117, v117, 8, %[gc1]\n"
      "buffer_load_dwordx4 v117, %[srdT], 0 offen offset:1024 lds\n"
      "v_add_u32 v31, %[sV], v121\n"
      "v_min_u32 v121, v121, v31\n"
      "v_lshl_add_u32 v121, v121, 8, %[gc2]\n"
      "buffer_load_dwordx4 v121, %[srdT], 0 offen offset:2048 lds\n"
      "v_add_u32 v31, %[sV], v125\n"
      "v_min_u32 v125, v125, v31\n"
      "v_lshl_add_u32 v125, v125, 8, %[gc3]\n"
      "buffer_load_dwordx4 v125, %[srdT], 0 offen offset:3072 lds\n"
      "buffer_load_dwordx4 v[60:63], %[vdi], %[srdI], %[s4k] offen offset:2544\n"
      "s_waitcnt vmcnt(19)\n"
      "s_barrier\n"
      "s_add_u32 m0, %[sLb], 49152\n"
      "s_nop 0\n"
      "buffer_load_dwordx4 v97, %[srdT], %[s128] offen offset:0 lds\n"
      "buffer_load_dwordx4 v101, %[srdT], %[s128] offen offset:1024 lds\n"
      "buffer_load_dwordx4 v105, %[srdT], %[s128] offen offset:2048 lds\n"
      "buffer_load_dwordx4 v109, %[srdT], %[s128] offen offset:3072 lds\n"
      "s_add_u32 m0, %[sLb], 53248\n"
      "s_nop 0\n"
      "buffer_load_dwordx4 v113, %[srdT], %[s128] offen offset:0 lds\n"
      "buffer_load_dwordx4 v117, %[srdT], %[s128] offen offset:1024 lds\n"
      "buffer_load_dwordx4 v121, %[srdT], %[s128] offen offset:2048 lds\n"
      "buffer_load_dwordx4 v125, %[srdT], %[s128] offen offset:3072 lds\n"
      "buffer_load_dwordx4 v[64:67], %[vdi], %[srdI], 0 offen offset:64\n"
      "s_waitcnt vmcnt(19)\n"
      "s_barrier\n"
      "s_add_u32 m0, %[sLb], 0\n"
      "v_add_u32 v31, %[sV], v98\n"
      "v_min_u32 v98, v98, v31\n"
      "v_lshl_add_u32 v98, v98, 8, %[gc0]\n"
      "buffer_load_dwordx4 v98, %[srdT], 0 offen offset:0 lds\n"
      "v_add_u32 v31, %[sV], v102\n"
      "v_min_u32 v102, v102, v31\n"
      "v_lshl_add_u32 v102, v102, 8, %[gc1]\n"
      "buffer_load_dwordx4 v102, %[srdT], 0 offen offset:1024 lds\n"
      "v_add_u32 v31, %[sV], v106\n"
      "v_min_u32 v106, v106, v31\n"
      "v_lshl_add_u32 v106, v106, 8, %[gc2]\n"
      "buffer_load_dwordx4 v106, %[srdT], 0 offen offset:2048 lds\n"
      "v_add_u32 v31, %[sV], v110\n"
      "v_min_u32 v110, v110, v31\n"
      "v_lshl_add_u32 v110, v110, 8, %[gc3]\n"
      "buffer_load_dwordx4 v110, %[srdT], 0 offen offset:3072 lds\n"
      "s_add_u32 m0, %[sLb], 4096\n"
      "v_add_u32 v31, %[sV], v114\n"
      "v_min_u32 v114, v114, v31\n"
      "v_lshl_add_u32 v114, v114, 8, %[gc0]\n"
      "buffer_load_dwordx4 v114, %[srdT], 0 offen offset:0 lds\n"
      "v_add_u32 v31, %[sV], v118\n"
      "v_min_u32 v118, v118, v31\n"
      "v_lshl_add_u32 v118, v118, 8, %[gc1]\n"
      "buffer_load_dwordx4 v118, %[srdT], 0 offen offset:1024 lds\n"
      "v_add_u32 v31, %[sV], v122\n"
      "v_min_u32 v122, v122, v31\n"
      "v_lshl_add_u32 v122, v122, 8, %[gc2]\n"
      "buffer_load_dwordx4 v122, %[srdT], 0 offen offset:2048 lds\n"
      "v_add_u32 v31, %[sV], v126\n"
      "v_min_u32 v126, v126, v31\n"
      "v_lshl_add_u32 v126, v126, 8, %[gc3]\n"
      "buffer_load_dwordx4 v126, %[srdT], 0 offen offset:3072 lds\n"
      "buffer_load_dwordx4 v[68:71], %[vdi], %[srdI], 0 offen offset:896\n"
      "s_waitcnt vmcnt(19)\n"
      "s_barrier\n"
      "s_add_u32 m0, %[sLb], 16384\n"
      "s_nop 0\n"
      "buffer_load_dwordx4 v98, %[srdT], %[s128] offen offset:0 lds\n"
      "buffer_load_dwordx4 v102, %[srdT], %[s128] offen offset:1024 lds\n"
      "buffer_load_dwordx4 v106, %[srdT], %[s128] offen offset:2048 lds\n"
      "buffer_load_dwordx4 v110, %[srdT], %[s128] offen offset:3072 lds\n"
      "s_add_u32 m0, %[sLb], 20480\n"
      "s_nop 0\n"
      "buffer_load_dwordx4 v114, %[srdT], %[s128] offen offset:0 lds\n"
      "buffer_load_dwordx4 v118, %[srdT], %[s128] offen offset:1024 lds\n"
      "buffer_load_dwordx4 v122, %[srdT], %[s128] offen offset:2048 lds\n"
      "buffer_load_dwordx4 v126, %[srdT], %[s128] offen offset:3072 lds\n"
      "buffer_load_dwordx4 v[72:75], %[vdi], %[srdI], 0 offen offset:1728\n"
      "s_waitcnt vmcnt(19)\n"
      "s_barrier\n"
      "s_add_u32 m0, %[sLb], 32768\n"
      "v_add_u32 v31, %[sV], v99\n"
      "v_min_u32 v99, v99, v31\n"
      "v_lshl_add_u32 v99, v99, 8, %[gc0]\n"
      "buffer_load_dwordx4 v99, %[srdT], 0 offen offset:0 lds\n"
      "v_add_u32 v31, %[sV], v103\n"
      "v_min_u32 v103, v103, v31\n"
      "v_lshl_add_u32 v103, v103, 8, %[gc1]\n"
      "buffer_load_dwordx4 v103, %[srdT], 0 offen offset:1024 lds\n"
      "v_add_u32 v31, %[sV], v107\n"
      "v_min_u32 v107, v107, v31\n"
      "v_lshl_add_u32 v107, v107, 8, %[gc2]\n"
      "buffer_load_dwordx4 v107, %[srdT], 0 offen offset:2048 lds\n"
      "v_add_u32 v31, %[sV], v111\n"
      "v_min_u32 v111, v111, v31\n"
      "v_lshl_add_u32 v111, v111, 8, %[gc3]\n"
      "buffer_load_dwordx4 v111, %[srdT], 0 offen offset:3072 lds\n"
      "s_add_u32 m0, %[sLb], 36864\n"
      "v_add_u32 v31, %[sV], v115\n"
      "v_min_u32 v115, v115, v31\n"
      "v_lshl_add_u32 v115, v115, 8, %[gc0]\n"
      "buffer_load_dwordx4 v115, %[srdT], 0 offen offset:0 lds\n"
      "v_add_u32 v31, %[sV], v119\n"
      "v_min_u32 v119, v119, v31\n"
      "v_lshl_add_u32 v119, v119, 8, %[gc1]\n"
      "buffer_load_dwordx4 v119, %[srdT], 0 offen offset:1024 lds\n"
      "v_add_u32 v31, %[sV], v123\n"
      "v_min_u32 v123, v123, v31\n"
      "v_lshl_add_u32 v123, v123, 8, %[gc2]\n"
      "buffer_load_dwordx4 v123, %[srdT], 0 offen offset:2048 lds\n"
      "v_add_u32 v31, %[sV], v127\n"
      "v_min_u32 v127, v127, v31\n"
      "v_lshl_add_u32 v127, v127, 8, %[gc3]\n"
      "buffer_load_dwordx4 v127, %[srdT], 0 offen offset:3072 lds\n"
      "buffer_load_dwordx4 v[76:79], %[vdi], %[srdI], 0 offen offset:2560\n"
      "s_waitcnt vmcnt(19)\n"
      "s_barrier\n"
      "s_add_u32 m0, %[sLb], 49152\n"
      "s_nop 0\n"
      "buffer_load_dwordx4 v99, %[srdT], %[s128] offen offset:0 lds\n"
      "buffer_load_dwordx4 v103, %[srdT], %[s128] offen offset:1024 lds\n"
      "buffer_load_dwordx4 v107, %[srdT], %[s128] offen offset:2048 lds\n"
      "buffer_load_dwordx4 v111, %[srdT], %[s128] offen offset:3072 lds\n"
      "s_add_u32 m0, %[sLb], 53248\n"
      "s_nop 0\n"
      "buffer_load_dwordx4 v115, %[srdT], %[s128] offen offset:0 lds\n"
      "buffer_load_dwordx4 v119, %[srdT], %[s128] offen offset:1024 lds\n"
      "buffer_load_dwordx4 v123, %[srdT], %[s128] offen offset:2048 lds\n"
      "buffer_load_dwordx4 v127, %[srdT], %[s128] offen offset:3072 lds\n"
      "buffer_load_dwordx4 v[80:83], %[vdi], %[srdI], 0 offen offset:3392\n"
      "s_waitcnt vmcnt(19)\n"
      "s_barrier\n"
      "s_add_u32 m0, %[sLb], 0\n"
      "v_add_u32 v31, %[sV], v32\n"
      "v_min_u32 v32, v32, v31\n"
      "v_lshl_add_u32 v32, v32, 8, %[gc0]\n"
      "buffer_load_dwordx4 v32, %[srdT], 0 offen offset:0 lds\n"
      "v_add_u32 v31, %[sV], v36\n"
      "v_min_u32 v36, v36, v31\n"
      "v_lshl_add_u32 v36, v36, 8, %[gc1]\n"
      "buffer_load_dwordx4 v36, %[srdT], 0 offen offset:1024 lds\n"
      "v_add_u32 v31, %[sV], v40\n"
      "v_min_u32 v40, v40, v31\n"
      "v_lshl_add_u32 v40, v40, 8, %[gc2]\n"
      "buffer_load_dwordx4 v40, %[srdT], 0 offen offset:2048 lds\n"
      "v_add_u32 v31, %[sV], v44\n"
      "v_min_u32 v44, v44, v31\n"
      "v_lshl_add_u32 v44, v44, 8, %[gc3]\n"
      "buffer_load_dwordx4 v44, %[srdT], 0 offen offset:3072 lds\n"
      "s_add_u32 m0, %[sLb], 4096\n"
      "v_add_u32 v31, %[sV], v48\n"
      "v_min_u32 v48, v48, v31\n"
      "v_lshl_add_u32 v48, v48, 8, %[gc0]\n"
      "buffer_load_dwordx4 v48, %[srdT], 0 offen offset:0 lds\n"
      "v_add_u32 v31, %[sV], v52\n"
      "v_min_u32 v52, v52, v31\n"
      "v_lshl_add_u32 v52, v52, 8, %[gc1]\n"
      "buffer_load_dwordx4 v52, %[srdT], 0 offen offset:1024 lds\n"
      "v_add_u32 v31, %[sV], v56\n"
      "v_min_u32 v56, v56, v31\n"
      "v_lshl_add_u32 v56, v56, 8, %[gc2]\n"
      "buffer_load_dwordx4 v56, %[srdT], 0 offen offset:2048 lds\n"
      "v_add_u32 v31, %[sV], v60\n"
      "v_min_u32 v60, v60, v31\n"
      "v_lshl_add_u32 v60, v60, 8, %[gc3]\n"
      "buffer_load_dwordx4 v60, %[srdT], 0 offen offset:3072 lds\n"
      "buffer_load_dwordx4 v[84:87], %[vdi], %[srdI], %[s4k] offen offset:896\n"
      "s_waitcnt vmcnt(19)\n"
      "s_barrier\n"
      "s_add_u32 m0, %[sLb], 16384\n"
      "s_nop 0\n"
      "buffer_load_dwordx4 v32, %[srdT], %[s128] offen offset:0 lds\n"
      "buffer_load_dwordx4 v36, %[srdT], %[s128] offen offset:1024 lds\n"
      "buffer_load_dwordx4 v40, %[srdT], %[s128] offen offset:2048 lds\n"
      "buffer_load_dwordx4 v44, %[srdT], %[s128] offen offset:3072 lds\n"
      "s_add_u32 m0, %[sLb], 20480\n"
      "s_nop 0\n"
      "buffer_load_dwordx4 v48, %[srdT], %[s128] offen offset:0 lds\n"
      "buffer_load_dwordx4 v52, %[srdT], %[s128] offen offset:1024 lds\n"
      "buffer_load_dwordx4 v56, %[srdT], %[s128] offen offset:2048 lds\n"
      "buffer_load_dwordx4 v60, %[srdT], %[s128] offen offset:3072 lds\n"
      "buffer_load_dwordx4 v[88:91], %[vdi], %[srdI], %[s4k] offen offset:1728\n"
      "s_waitcnt vmcnt(19)\n"
      "s_barrier\n"
      "s_add_u32 m0, %[sLb], 32768\n"
      "v_add_u32 v31, %[sV], v33\n"
      "v_min_u32 v33, v33, v31\n"
      "v_lshl_add_u32 v33, v33, 8, %[gc0]\n"
      "buffer_load_dwordx4 v33, %[srdT], 0 offen offset:0 lds\n"
      "v_add_u32 v31, %[sV], v37\n"
      "v_min_u32 v37, v37, v31\n"
      "v_lshl_add_u32 v37, v37, 8, %[gc1]\n"
      "buffer_load_dwordx4 v37, %[srdT], 0 offen offset:1024 lds\n"
      "v_add_u32 v31, %[sV], v41\n"
      "v_min_u32 v41, v41, v31\n"
      "v_lshl_add_u32 v41, v41, 8, %[gc2]\n"
      "buffer_load_dwordx4 v41, %[srdT], 0 offen offset:2048 lds\n"
      "v_add_u32 v31, %[sV], v45\n"
      "v_min_u32 v45, v45, v31\n"
      "v_lshl_add_u32 v45, v45, 8, %[gc3]\n"
      "buffer_load_dwordx4 v45, %[srdT], 0 offen offset:3072 lds\n"
      "s_add_u32 m0, %[sLb], 36864\n"
      "v_add_u32 v31, %[sV], v49\n"
      "v_min_u32 v49, v49, v31\n"
      "v_lshl_add_u32 v49, v49, 8, %[gc0]\n"
      "buffer_load_dwordx4 v49, %[srdT], 0 offen offset:0 lds\n"
      "v_add_u32 v31, %[sV], v53\n"
      "v_min_u32 v53, v53, v31\n"
      "v_lshl_add_u32 v53, v53, 8, %[gc1]\n"
      "buffer_load_dwordx4 v53, %[srdT], 0 offen offset:1024 lds\n"
      "v_add_u32 v31, %[sV], v57\n"
      "v_min_u32 v57, v57, v31\n"
      "v_lshl_add_u32 v57, v57, 8, %[gc2]\n"
      "buffer_load_dwordx4 v57, %[srdT], 0 offen offset:2048 lds\n"
      "v_add_u32 v31, %[sV], v61\n"
      "v_min_u32 v61, v61, v31\n"
      "v_lshl_add_u32 v61, v61, 8, %[gc3]\n"
      "buffer_load_dwordx4 v61, %[srdT], 0 offen offset:3072 lds\n"
      "buffer_load_dwordx4 v[92:95], %[vdi], %[srdI], %[s4k] offen offset:2560\n"
      "s_waitcnt vmcnt(19)\n"
      "s_barrier\n"
      "s_add_u32 m0, %[sLb], 49152\n"
      "s_nop 0\n"
      "buffer_load_dwordx4 v33, %[srdT], %[s128] offen offset:0 lds\n"
      "buffer_load_dwordx4 v37, %[srdT], %[s128] offen offset:1024 lds\n"
      "buffer_load_dwordx4 v41, %[srdT], %[s128] offen offset:2048 lds\n"
      "buffer_load_dwordx4 v45, %[srdT], %[s128] offen offset:3072 lds\n"
      "s_add_u32 m0, %[sLb], 53248\n"
      "s_nop 0\n"
      "buffer_load_dwordx4 v49, %[srdT], %[s128] offen offset:0 lds\n"
      "buffer_load_dwordx4 v53, %[srdT], %[s128] offen offset:1024 lds\n"
      "buffer_load_dwordx4 v57, %[srdT], %[s128] offen offset:2048 lds\n"
      "buffer_load_dwordx4 v61, %[srdT], %[s128] offen offset:3072 lds\n"
      "buffer_load_dwordx4 v[96:99], %[vdi], %[srdI], 0 offen offset:80\n"
      "s_waitcnt vmcnt(19)\n"
      "s_barrier\n"
      "s_add_u32 m0, %[sLb], 0\n"
      "v_add_u32 v31, %[sV], v34\n"
      "v_min_u32 v34, v34, v31\n"
      "v_lshl_add_u32 v34, v34, 8, %[gc0]\n"
      "buffer_load_dwordx4 v34, %[srdT], 0 offen offset:0 lds\n"
      "v_add_u32 v31, %[sV], v38\n"
      "v_min_u32 v38, v38, v31\n"
      "v_lshl_add_u32 v38, v38, 8, %[gc1]\n"
      "buffer_load_dwordx4 v38, %[srdT], 0 offen offset:1024 lds\n"
      "v_add_u32 v31, %[sV], v42\n"
      "v_min_u32 v42, v42, v31\n"
      "v_lshl_add_u32 v42, v42, 8, %[gc2]\n"
      "buffer_load_dwordx4 v42, %[srdT], 0 offen offset:2048 lds\n"
      "v_add_u32 v31, %[sV], v46\n"
      "v_min_u32 v46, v46, v31\n"
      "v_lshl_add_u32 v46, v46, 8, %[gc3]\n"
      "buffer_load_dwordx4 v46, %[srdT], 0 offen offset:3072 lds\n"
      "s_add_u32 m0, %[sLb], 4096\n"
      "v_add_u32 v31, %[sV], v50\n"
      "v_min_u32 v50, v50, v31\n"
      "v_lshl_add_u32 v50, v50, 8, %[gc0]\n"
      "buffer_load_dwordx4 v50, %[srdT], 0 offen offset:0 lds\n"
      "v_add_u32 v31, %[sV], v54\n"
      "v_min_u32 v54, v54, v31\n"
      "v_lshl_add_u32 v54, v54, 8, %[gc1]\n"
      "buffer_load_dwordx4 v54, %[srdT], 0 offen offset:1024 lds\n"
      "v_add_u32 v31, %[sV], v58\n"
      "v_min_u32 v58, v58, v31\n"
      "v_lshl_add_u32 v58, v58, 8, %[gc2]\n"
      "buffer_load_dwordx4 v58, %[srdT], 0 offen offset:2048 lds\n"
      "v_add_u32 v31, %[sV], v62\n"
      "v_min_u32 v62, v62, v31\n"
      "v_lshl_add_u32 v62, v62, 8, %[gc3]\n"
      "buffer_load_dwordx4 v62, %[srdT], 0 offen offset:3072 lds\n"
      "buffer_load_dwordx4 v[100:103], %[vdi], %[srdI], 0 offen offset:912\n"
      "s_waitcnt vmcnt(19)\n"
      "s_barrier\n"
      "s_add_u32 m0, %[sLb], 16384\n"
      "s_nop 0\n"
      "buffer_load_dwordx4 v34, %[srdT], %[s128] offen offset:0 lds\n"
      "buffer_load_dwordx4 v38, %[srdT], %[s128] offen offset:1024 lds\n"
      "buffer_load_dwordx4 v42, %[srdT], %[s128] offen offset:2048 lds\n"
      "buffer_load_dwordx4 v46, %[srdT], %[s128] offen offset:3072 lds\n"
      "s_add_u32 m0, %[sLb], 20480\n"
      "s_nop 0\n"
      "buffer_load_dwordx4 v50, %[srdT], %[s128] offen offset:0 lds\n"
      "buffer_load_dwordx4 v54, %[srdT], %[s128] offen offset:1024 lds\n"
      "buffer_load_dwordx4 v58, %[srdT], %[s128] offen offset:2048 lds\n"
      "buffer_load_dwordx4 v62, %[srdT], %[s128] offen offset:3072 lds\n"
      "buffer_load_dwordx4 v[104:107], %[vdi], %[srdI], 0 offen offset:1744\n"
      "s_waitcnt vmcnt(19)\n"
      "s_barrier\n"
      "s_add_u32 m0, %[sLb], 32768\n"
      "v_add_u32 v31, %[sV], v35\n"
      "v_min_u32 v35, v35, v31\n"
      "v_lshl_add_u32 v35, v35, 8, %[gc0]\n"
      "buffer_load_dwordx4 v35, %[srdT], 0 offen offset:0 lds\n"
      "v_add_u32 v31, %[sV], v39\n"
      "v_min_u32 v39, v39, v31\n"
      "v_lshl_add_u32 v39, v39, 8, %[gc1]\n"
      "buffer_load_dwordx4 v39, %[srdT], 0 offen offset:1024 lds\n"
      "v_add_u32 v31, %[sV], v43\n"
      "v_min_u32 v43, v43, v31\n"
      "v_lshl_add_u32 v43, v43, 8, %[gc2]\n"
      "buffer_load_dwordx4 v43, %[srdT], 0 offen offset:2048 lds\n"
      "v_add_u32 v31, %[sV], v47\n"
      "v_min_u32 v47, v47, v31\n"
      "v_lshl_add_u32 v47, v47, 8, %[gc3]\n"
      "buffer_load_dwordx4 v47, %[srdT], 0 offen offset:3072 lds\n"
      "s_add_u32 m0, %[sLb], 36864\n"
      "v_add_u32 v31, %[sV], v51\n"
      "v_min_u32 v51, v51, v31\n"
      "v_lshl_add_u32 v51, v51, 8, %[gc0]\n"
      "buffer_load_dwordx4 v51, %[srdT], 0 offen offset:0 lds\n"
      "v_add_u32 v31, %[sV], v55\n"
      "v_min_u32 v55, v55, v31\n"
      "v_lshl_add_u32 v55, v55, 8, %[gc1]\n"
      "buffer_load_dwordx4 v55, %[srdT], 0 offen offset:1024 lds\n"
      "v_add_u32 v31, %[sV], v59\n"
      "v_min_u32 v59, v59, v31\n"
      "v_lshl_add_u32 v59, v59, 8, %[gc2]\n"
      "buffer_load_dwordx4 v59, %[srdT], 0 offen offset:2048 lds\n"
      "v_add_u32 v31, %[sV], v63\n"
      "v_min_u32 v63, v63, v31\n"
      "v_lshl_add_u32 v63, v63, 8, %[gc3]\n"
      "buffer_load_dwordx4 v63, %[srdT], 0 offen offset:3072 lds\n"
      "buffer_load_dwordx4 v[108:111], %[vdi], %[srdI], 0 offen offset:2576\n"
      "s_waitcnt vmcnt(19)\n"
      "s_barrier\n"
      "s_add_u32 m0, %[sLb], 49152\n"
      "s_nop 0\n"
      "buffer_load_dwordx4 v35, %[srdT], %[s128] offen offset:0 lds\n"
      "buffer_load_dwordx4 v39, %[srdT], %[s128] offen offset:1024 lds\n"
      "buffer_load_dwordx4 v43, %[srdT], %[s128] offen offset:2048 lds\n"
      "buffer_load_dwordx4 v47, %[srdT], %[s128] offen offset:3072 lds\n"
      "s_add_u32 m0, %[sLb], 53248\n"
      "s_nop 0\n"
      "buffer_load_dwordx4 v51, %[srdT], %[s128] offen offset:0 lds\n"
      "buffer_load_dwordx4 v55, %[srdT], %[s128] offen offset:1024 lds\n"
      "buffer_load_dwordx4 v59, %[srdT], %[s128] offen offset:2048 lds\n"
      "buffer_load_dwordx4 v63, %[srdT], %[s128] offen offset:3072 lds\n"
      "buffer_load_dwordx4 v[112:115], %[vdi], %[srdI], 0 offen offset:3408\n"
      "s_waitcnt vmcnt(19)\n"
      "s_barrier\n"
      "s_add_u32 m0, %[sLb], 0\n"
      "v_add_u32 v31, %[sV], v64\n"
      "v_min_u32 v64, v64, v31\n"
      "v_lshl_add_u32 v64, v64, 8, %[gc0]\n"
      "buffer_load_dwordx4 v64, %[srdT], 0 offen offset:0 lds\n"
      "v_add_u32 v31, %[sV], v68\n"
      "v_min_u32 v68, v68, v31\n"
      "v_lshl_add_u32 v68, v68, 8, %[gc1]\n"
      "buffer_load_dwordx4 v68, %[srdT], 0 offen offset:1024 lds\n"
      "v_add_u32 v31, %[sV], v72\n"
      "v_min_u32 v72, v72, v31\n"
      "v_lshl_add_u32 v72, v72, 8, %[gc2]\n"
      "buffer_load_dwordx4 v72, %[srdT], 0 offen offset:2048 lds\n"
      "v_add_u32 v31, %[sV], v76\n"
      "v_min_u32 v76, v76, v31\n"
      "v_lshl_add_u32 v76, v76, 8, %[gc3]\n"
      "buffer_load_dwordx4 v76, %[srdT], 0 offen offset:3072 lds\n"
      "s_add_u32 m0, %[sLb], 4096\n"
      "v_add_u32 v31, %[sV], v80\n"
      "v_min_u32 v80, v80, v31\n"
      "v_lshl_add_u32 v80, v80, 8, %[gc0]\n"
      "buffer_load_dwordx4 v80, %[srdT], 0 offen offset:0 lds\n"
      "v_add_u32 v31, %[sV], v84\n"
      "v_min_u32 v84, v84, v31\n"
      "v_lshl_add_u32 v84, v84, 8, %[gc1]\n"
      "buffer_load_dwordx4 v84, %[srdT], 0 offen offset:1024 lds\n"
      "v_add_u32 v31, %[sV], v88\n"
      "v_min_u32 v88, v88, v31\n"
      "v_lshl_add_u32 v88, v88, 8, %[gc2]\n"
      "buffer_load_dwordx4 v88, %[srdT], 0 offen offset:2048 lds\n"
      "v_add_u32 v31, %[sV], v92\n"
      "v_min_u32 v92, v92, v31\n"
      "v_lshl_add_u32 v92, v92, 8, %[gc3]\n"
      "buffer_load_dwordx4 v92, %[srdT], 0 offen offset:3072 lds\n"
      "buffer_load_dwordx4 v[116:119], %[vdi], %[srdI], %[s4k] offen offset:912\n"
      "s_waitcnt vmcnt(19)\n"
      "s_barrier\n"
      "s_add_u32 m0, %[sLb], 16384\n"
      "s_nop 0\n"
      "buffer_load_dwordx4 v64, %[srdT], %[s128] offen offset:0 lds\n"
      "buffer_load_dwordx4 v68, %[srdT], %[s128] offen offset:1024 lds\n"
      "buffer_load_dwordx4 v72, %[srdT], %[s128] offen offset:2048 lds\n"
      "buffer_load_dwordx4 v76, %[srdT], %[s128] offen offset:3072 lds\n"
      "s_add_u32 m0, %[sLb], 20480\n"
      "s_nop 0\n"
      "buffer_load_dwordx4 v80, %[srdT], %[s128] offen offset:0 lds\n"
      "buffer_load_dwordx4 v84, %[srdT], %[s128] offen offset:1024 lds\n"
      "buffer_load_dwordx4 v88, %[srdT], %[s128] offen offset:2048 lds\n"
      "buffer_load_dwordx4 v92, %[srdT], %[s128] offen offset:3072 lds\n"
      "buffer_load_dwordx4 v[120:123], %[vdi], %[srdI], %[s4k] offen offset:1744\n"
      "s_waitcnt vmcnt(19)\n"
      "s_barrier\n"
      "s_add_u32 m0, %[sLb], 32768\n"
      "v_add_u32 v31, %[sV], v65\n"
      "v_min_u32 v65, v65, v31\n"
      "v_lshl_add_u32 v65, v65, 8, %[gc0]\n"
      "buffer_load_dwordx4 v65, %[srdT], 0 offen offset:0 lds\n"
      "v_add_u32 v31, %[sV], v69\n"
      "v_min_u32 v69, v69, v31\n"
      "v_lshl_add_u32 v69, v69, 8, %[gc1]\n"
      "buffer_load_dwordx4 v69, %[srdT], 0 offen offset:1024 lds\n"
      "v_add_u32 v31, %[sV], v73\n"
      "v_min_u32 v73, v73, v31\n"
      "v_lshl_add_u32 v73, v73, 8, %[gc2]\n"
      "buffer_load_dwordx4 v73, %[srdT], 0 offen offset:2048 lds\n"
      "v_add_u32 v31, %[sV], v77\n"
      "v_min_u32 v77, v77, v31\n"
      "v_lshl_add_u32 v77, v77, 8, %[gc3]\n"
      "buffer_load_dwordx4 v77, %[srdT], 0 offen offset:3072 lds\n"
      "s_add_u32 m0, %[sLb], 36864\n"
      "v_add_u32 v31, %[sV], v81\n"
      "v_min_u32 v81, v81, v31\n"
      "v_lshl_add_u32 v81, v81, 8, %[gc0]\n"
      "buffer_load_dwordx4 v81, %[srdT], 0 offen offset:0 lds\n"
      "v_add_u32 v31, %[sV], v85\n"
      "v_min_u32 v85, v85, v31\n"
      "v_lshl_add_u32 v85, v85, 8, %[gc1]\n"
      "buffer_load_dwordx4 v85, %[srdT], 0 offen offset:1024 lds\n"
      "v_add_u32 v31, %[sV], v89\n"
      "v_min_u32 v89, v89, v31\n"
      "v_lshl_add_u32 v89, v89, 8, %[gc2]\n"
      "buffer_load_dwordx4 v89, %[srdT], 0 offen offset:2048 lds\n"
      "v_add_u32 v31, %[sV], v93\n"
      "v_min_u32 v93, v93, v31\n"
      "v_lshl_add_u32 v93, v93, 8, %[gc3]\n"
      "buffer_load_dwordx4 v93, %[srdT], 0 offen offset:3072 lds\n"
      "buffer_load_dwordx4 v[124:127], %[vdi], %[srdI], %[s4k] offen offset:2576\n"
      "s_waitcnt vmcnt(19)\n"
      "s_barrier\n"
      "s_add_u32 m0, %[sLb], 49152\n"
      "s_nop 0\n"
      "buffer_load_dwordx4 v65, %[srdT], %[s128] offen offset:0 lds\n"
      "buffer_load_dwordx4 v69, %[srdT], %[s128] offen offset:1024 lds\n"
      "buffer_load_dwordx4 v73, %[srdT], %[s128] offen offset:2048 lds\n"
      "buffer_load_dwordx4 v77, %[srdT], %[s128] offen offset:3072 lds\n"
      "s_add_u32 m0, %[sLb], 53248\n"
      "s_nop 0\n"
      "buffer_load_dwordx4 v81, %[srdT], %[s128] offen offset:0 lds\n"
      "buffer_load_dwordx4 v85, %[srdT], %[s128] offen offset:1024 lds\n"
      "buffer_load_dwordx4 v89, %[srdT], %[s128] offen offset:2048 lds\n"
      "buffer_load_dwordx4 v93, %[srdT], %[s128] offen offset:3072 lds\n"
      "buffer_load_dwordx4 v[32:35], %[vdi], %[srdI], 0 offen offset:96\n"
      "s_waitcnt vmcnt(19)\n"
      "s_barrier\n"
      "s_add_u32 m0, %[sLb], 0\n"
      "v_add_u32 v31, %[sV], v66\n"
      "v_min_u32 v66, v66, v31\n"
      "v_lshl_add_u32 v66, v66, 8, %[gc0]\n"
      "buffer_load_dwordx4 v66, %[srdT], 0 offen offset:0 lds\n"
      "v_add_u32 v31, %[sV], v70\n"
      "v_min_u32 v70, v70, v31\n"
      "v_lshl_add_u32 v70, v70, 8, %[gc1]\n"
      "buffer_load_dwordx4 v70, %[srdT], 0 offen offset:1024 lds\n"
      "v_add_u32 v31, %[sV], v74\n"
      "v_min_u32 v74, v74, v31\n"
      "v_lshl_add_u32 v74, v74, 8, %[gc2]\n"
      "buffer_load_dwordx4 v74, %[srdT], 0 offen offset:2048 lds\n"
      "v_add_u32 v31, %[sV], v78\n"
      "v_min_u32 v78, v78, v31\n"
      "v_lshl_add_u32 v78, v78, 8, %[gc3]\n"
      "buffer_load_dwordx4 v78, %[srdT], 0 offen offset:3072 lds\n"
      "s_add_u32 m0, %[sLb], 4096\n"
      "v_add_u32 v31, %[sV], v82\n"
      "v_min_u32 v82, v82, v31\n"
      "v_lshl_add_u32 v82, v82, 8, %[gc0]\n"
      "buffer_load_dwordx4 v82, %[srdT], 0 offen offset:0 lds\n"
      "v_add_u32 v31, %[sV], v86\n"
      "v_min_u32 v86, v86, v31\n"
      "v_lshl_add_u32 v86, v86, 8, %[gc1]\n"
      "buffer_load_dwordx4 v86, %[srdT], 0 offen offset:1024 lds\n"
      "v_add_u32 v31, %[sV], v90\n"
      "v_min_u32 v90, v90, v31\n"
      "v_lshl_add_u32 v90, v90, 8, %[gc2]\n"
      "buffer_load_dwordx4 v90, %[srdT], 0 offen offset:2048 lds\n"
      "v_add_u32 v31, %[sV], v94\n"
      "v_min_u32 v94, v94, v31\n"
      "v_lshl_add_u32 v94, v94, 8, %[gc3]\n"
      "buffer_load_dwordx4 v94, %[srdT], 0 offen offset:3072 lds\n"
      "buffer_load_dwordx4 v[36:39], %[vdi], %[srdI], 0 offen offset:928\n"
      "s_waitcnt vmcnt(19)\n"
      "s_barrier\n"
      "s_add_u32 m0, %[sLb], 16384\n"
      "s_nop 0\n"
      "buffer_load_dwordx4 v66, %[srdT], %[s128] offen offset:0 lds\n"
      "buffer_load_dwordx4 v70, %[srdT], %[s128] offen offset:1024 lds\n"
      "buffer_load_dwordx4 v74, %[srdT], %[s128] offen offset:2048 lds\n"
      "buffer_load_dwordx4 v78, %[srdT], %[s128] offen offset:3072 lds\n"
      "s_add_u32 m0, %[sLb], 20480\n"
      "s_nop 0\n"
      "buffer_load_dwordx4 v82, %[srdT], %[s128] offen offset:0 lds\n"
      "buffer_load_dwordx4 v86, %[srdT], %[s128] offen offset:1024 lds\n"
      "buffer_load_dwordx4 v90, %[srdT], %[s128] offen offset:2048 lds\n"
      "buffer_load_dwordx4 v94, %[srdT], %[s128] offen offset:3072 lds\n"
      "buffer_load_dwordx4 v[40:43], %[vdi], %[srdI], 0 offen offset:1760\n"
      "s_waitcnt vmcnt(19)\n"
      "s_barrier\n"
      "s_add_u32 m0, %[sLb], 32768\n"
      "v_add_u32 v31, %[sV], v67\n"
      "v_min_u32 v67, v67, v31\n"
      "v_lshl_add_u32 v67, v67, 8, %[gc0]\n"
      "buffer_load_dwordx4 v67, %[srdT], 0 offen offset:0 lds\n"
      "v_add_u32 v31, %[sV], v71\n"
      "v_min_u32 v71, v71, v31\n"
      "v_lshl_add_u32 v71, v71, 8, %[gc1]\n"
      "buffer_load_dwordx4 v71, %[srdT], 0 offen offset:1024 lds\n"
      "v_add_u32 v31, %[sV], v75\n"
      "v_min_u32 v75, v75, v31\n"
      "v_lshl_add_u32 v75, v75, 8, %[gc2]\n"
      "buffer_load_dwordx4 v75, %[srdT], 0 offen offset:2048 lds\n"
      "v_add_u32 v31, %[sV], v79\n"
      "v_min_u32 v79, v79, v31\n"
      "v_lshl_add_u32 v79, v79, 8, %[gc3]\n"
      "buffer_load_dwordx4 v79, %[srdT], 0 offen offset:3072 lds\n"
      "s_add_u32 m0, %[sLb], 36864\n"
      "v_add_u32 v31, %[sV], v83\n"
      "v_min_u32 v83, v83, v31\n"
      "v_lshl_add_u32 v83, v83, 8, %[gc0]\n"
      "buffer_load_dwordx4 v83, %[srdT], 0 offen offset:0 lds\n"
      "v_add_u32 v31, %[sV], v87\n"
      "v_min_u32 v87, v87, v31\n"
      "v_lshl_add_u32 v87, v87, 8, %[gc1]\n"
      "buffer_load_dwordx4 v87, %[srdT], 0 offen offset:1024 lds\n"
      "v_add_u32 v31, %[sV], v91\n"
      "v_min_u32 v91, v91, v31\n"
      "v_lshl_add_u32 v91, v91, 8, %[gc2]\n"
      "buffer_load_dwordx4 v91, %[srdT], 0 offen offset:2048 lds\n"
      "v_add_u32 v31, %[sV], v95\n"
      "v_min_u32 v95, v95, v31\n"
      "v_lshl_add_u32 v95, v95, 8, %[gc3]\n"
      "buffer_load_dwordx4 v95, %[srdT], 0 offen offset:3072 lds\n"
      "buffer_load_dwordx4 v[44:47], %[vdi], %[srdI], 0 offen offset:2592\n"
      "s_waitcnt vmcnt(19)\n"
      "s_barrier\n"
      "s_add_u32 m0, %[sLb], 49152\n"
      "s_nop 0\n"
      "buffer_load_dwordx4 v67, %[srdT], %[s128] offen offset:0 lds\n"
      "buffer_load_dwordx4 v71, %[srdT], %[s128] offen offset:1024 lds\n"
      "buffer_load_dwordx4 v75, %[srdT], %[s128] offen offset:2048 lds\n"
      "buffer_load_dwordx4 v79, %[srdT], %[s128] offen offset:3072 lds\n"
      "s_add_u32 m0, %[sLb], 53248\n"
      "s_nop 0\n"
      "buffer_load_dwordx4 v83, %[srdT], %[s128] offen offset:0 lds\n"
      "buffer_load_dwordx4 v87, %[srdT], %[s128] offen offset:1024 lds\n"
      "buffer_load_dwordx4 v91, %[srdT], %[s128] offen offset:2048 lds\n"
      "buffer_load_dwordx4 v95, %[srdT], %[s128] offen offset:3072 lds\n"
      "buffer_load_dwordx4 v[48:51], %[vdi], %[srdI], 0 offen offset:3424\n"
      "s_waitcnt vmcnt(19)\n"
      "s_barrier\n"
      "s_add_u32 m0, %[sLb], 0\n"
      "v_add_u32 v31, %[sV], v96\n"
      "v_min_u32 v96, v96, v31\n"
      "v_lshl_add_u32 v96, v96, 8, %[gc0]\n"
      "buffer_load_dwordx4 v96, %[srdT], 0 offen offset:0 lds\n"
      "v_add_u32 v31, %[sV], v100\n"
      "v_min_u32 v100, v100, v31\n"
      "v_lshl_add_u32 v100, v100, 8, %[gc1]\n"
      "buffer_load_dwordx4 v100, %[srdT], 0 offen offset:1024 lds\n"
      "v_add_u32 v31, %[sV], v104\n"
      "v_min_u32 v104, v104, v31\n"
      "v_lshl_add_u32 v104, v104, 8, %[gc2]\n"
      "buffer_load_dwordx4 v104, %[srdT], 0 offen offset:2048 lds\n"
      "v_add_u32 v31, %[sV], v108\n"
      "v_min_u32 v108, v108, v31\n"
      "v_lshl_add_u32 v108, v108, 8, %[gc3]\n"
      "buffer_load_dwordx4 v108, %[srdT], 0 offen offset:3072 lds\n"
      "s_add_u32 m0, %[sLb], 4096\n"
      "v_add_u32 v31, %[sV], v112\n"
      "v_min_u32 v112, v112, v31\n"
      "v_lshl_add_u32 v112, v112, 8, %[gc0]\n"
      "buffer_load_dwordx4 v112, %[srdT], 0 offen offset:0 lds\n"
      "v_add_u32 v31, %[sV], v116\n"
      "v_min_u32 v116, v116, v31\n"
      "v_lshl_add_u32 v116, v116, 8, %[gc1]\n"
      "buffer_load_dwordx4 v116, %[srdT], 0 offen offset:1024 lds\n"
      "v_add_u32 v31, %[sV], v120\n"
      "v_min_u32 v120, v120, v31\n"
      "v_lshl_add_u32 v120, v120, 8, %[gc2]\n"
      "buffer_load_dwordx4 v120, %[srdT], 0 offen offset:2048 lds\n"
      "v_add_u32 v31, %[sV], v124\n"
      "v_min_u32 v124, v124, v31\n"
      "v_lshl_add_u32 v124, v124, 8, %[gc3]\n"
      "buffer_load_dwordx4 v124, %[srdT], 0 offen offset:3072 lds\n"
      "buffer_load_dwordx4 v[52:55], %[vdi], %[srdI], %[s4k] offen offset:928\n"
      "s_waitcnt vmcnt(19)\n"
      "s_barrier\n"
      "s_add_u32 m0, %[sLb], 16384\n"
      "s_nop 0\n"
      "buffer_load_dwordx4 v96, %[srdT], %[s128] offen offset:0 lds\n"
      "buffer_load_dwordx4 v100, %[srdT], %[s128] offen offset:1024 lds\n"
      "buffer_load_dwordx4 v104, %[srdT], %[s128] offen offset:2048 lds\n"
      "buffer_load_dwordx4 v108, %[srdT], %[s128] offen offset:3072 lds\n"
      "s_add_u32 m0, %[sLb], 20480\n"
      "s_nop 0\n"
      "buffer_load_dwordx4 v112, %[srdT], %[s128] offen offset:0 lds\n"
      "buffer_load_dwordx4 v116, %[srdT], %[s128] offen offset:1024 lds\n"
      "buffer_load_dwordx4 v120, %[srdT], %[s128] offen offset:2048 lds\n"
      "buffer_load_dwordx4 v124, %[srdT], %[s128] offen offset:3072 lds\n"
      "buffer_load_dwordx4 v[56:59], %[vdi], %[srdI], %[s4k] offen offset:1760\n"
      "s_waitcnt vmcnt(19)\n"
      "s_barrier\n"
      "s_add_u32 m0, %[sLb], 32768\n"
      "v_add_u32 v31, %[sV], v97\n"
      "v_min_u32 v97, v97, v31\n"
      "v_lshl_add_u32 v97, v97, 8, %[gc0]\n"
      "buffer_load_dwordx4 v97, %[srdT], 0 offen offset:0 lds\n"
      "v_add_u32 v31, %[sV], v101\n"
      "v_min_u32 v101, v101, v31\n"
      "v_lshl_add_u32 v101, v101, 8, %[gc1]\n"
      "buffer_load_dwordx4 v101, %[srdT], 0 offen offset:1024 lds\n"
      "v_add_u32 v31, %[sV], v105\n"
      "v_min_u32 v105, v105, v31\n"
      "v_lshl_add_u32 v105, v105, 8, %[gc2]\n"
      "buffer_load_dwordx4 v105, %[srdT], 0 offen offset:2048 lds\n"
      "v_add_u32 v31, %[sV], v109\n"
      "v_min_u32 v109, v109, v31\n"
      "v_lshl_add_u32 v109, v109, 8, %[gc3]\n"
      "buffer_load_dwordx4 v109, %[srdT], 0 offen offset:3072 lds\n"
      "s_add_u32 m0, %[sLb], 36864\n"
      "v_add_u32 v31, %[sV], v113\n"
      "v_min_u32 v113, v113, v31\n"
      "v_lshl_add_u32 v113, v113, 8, %[gc0]\n"
      "buffer_load_dwordx4 v113, %[srdT], 0 offen offset:0 lds\n"
      "v_add_u32 v31, %[sV], v117\n"
      "v_min_u32 v117, v117, v31\n"
      "v_lshl_add_u32 v117, v117, 8, %[gc1]\n"
      "buffer_load_dwordx4 v117, %[srdT], 0 offen offset:1024 lds\n"
      "v_add_u32 v31, %[sV], v121\n"
      "v_min_u32 v121, v121, v31\n"
      "v_lshl_add_u32 v121, v121, 8, %[gc2]\n"
      "buffer_load_dwordx4 v121, %[srdT], 0 offen offset:2048 lds\n"
      "v_add_u32 v31, %[sV], v125\n"
      "v_min_u32 v125, v125, v31\n"
      "v_lshl_add_u32 v125, v125, 8, %[gc3]\n"
      "buffer_load_dwordx4 v125, %[srdT], 0 offen offset:3072 lds\n"
      "buffer_load_dwordx4 v[60:63], %[vdi], %[srdI], %[s4k] offen offset:2592\n"
      "s_waitcnt vmcnt(19)\n"
      "s_barrier\n"
      "s_add_u32 m0, %[sLb], 49152\n"
      "s_nop 0\n"
      "buffer_load_dwordx4 v97, %[srdT], %[s128] offen offset:0 lds\n"
      "buffer_load_dwordx4 v101, %[srdT], %[s128] offen offset:1024 lds\n"
      "buffer_load_dwordx4 v105, %[srdT], %[s128] offen offset:2048 lds\n"
      "buffer_load_dwordx4 v109, %[srdT], %[s128] offen offset:3072 lds\n"
      "s_add_u32 m0, %[sLb], 53248\n"
      "s_nop 0\n"
      "buffer_load_dwordx4 v113, %[srdT], %[s128] offen offset:0 lds\n"
      "buffer_load_dwordx4 v117, %[srdT], %[s128] offen offset:1024 lds\n"
      "buffer_load_dwordx4 v121, %[srdT], %[s128] offen offset:2048 lds\n"
      "buffer_load_dwordx4 v125, %[srdT], %[s128] offen offset:3072 lds\n"
      "s_waitcnt vmcnt(18)\n"
      "s_barrier\n"
      "s_add_u32 m0, %[sLb], 0\n"
      "v_add_u32 v31, %[sV], v98\n"
      "v_min_u32 v98, v98, v31\n"
      "v_lshl_add_u32 v98, v98, 8, %[gc0]\n"
      "buffer_load_dwordx4 v98, %[srdT], 0 offen offset:0 lds\n"
      "v_add_u32 v31, %[sV], v102\n"
      "v_min_u32 v102, v102, v31\n"
      "v_lshl_add_u32 v102, v102, 8, %[gc1]\n"
      "buffer_load_dwordx4 v102, %[srdT], 0 offen offset:1024 lds\n"
      "v_add_u32 v31, %[sV], v106\n"
      "v_min_u32 v106, v106, v31\n"
      "v_lshl_add_u32 v106, v106, 8, %[gc2]\n"
      "buffer_load_dwordx4 v106, %[srdT], 0 offen offset:2048 lds\n"
      "v_add_u32 v31, %[sV], v110\n"
      "v_min_u32 v110, v110, v31\n"
      "v_lshl_add_u32 v110, v110, 8, %[gc3]\n"
      "buffer_load_dwordx4 v110, %[srdT], 0 offen offset:3072 lds\n"
      "s_add_u32 m0, %[sLb], 4096\n"
      "v_add_u32 v31, %[sV], v114\n"
      "v_min_u32 v114, v114, v31\n"
      "v_lshl_add_u32 v114, v114, 8, %[gc0]\n"
      "buffer_load_dwordx4 v114, %[srdT], 0 offen offset:0 lds\n"
      "v_add_u32 v31, %[sV], v118\n"
      "v_min_u32 v118, v118, v31\n"
      "v_lshl_add_u32 v118, v118, 8, %[gc1]\n"
      "buffer_load_dwordx4 v118, %[srdT], 0 offen offset:1024 lds\n"
      "v_add_u32 v31, %[sV], v122\n"
      "v_min_u32 v122, v122, v31\n"
      "v_lshl_add_u32 v122, v122, 8, %[gc2]\n"
      "buffer_load_dwordx4 v122, %[srdT], 0 offen offset:2048 lds\n"
      "v_add_u32 v31, %[sV], v126\n"
      "v_min_u32 v126, v126, v31\n"
      "v_lshl_add_u32 v126, v126, 8, %[gc3]\n"
      "buffer_load_dwordx4 v126, %[srdT], 0 offen offset:3072 lds\n"
      "s_waitcnt vmcnt(17)\n"
      "s_barrier\n"
      "s_add_u32 m0, %[sLb], 16384\n"
      "s_nop 0\n"
      "buffer_load_dwordx4 v98, %[srdT], %[s128] offen offset:0 lds\n"
      "buffer_load_dwordx4 v102, %[srdT], %[s128] offen offset:1024 lds\n"
      "buffer_load_dwordx4 v106, %[srdT], %[s128] offen offset:2048 lds\n"
      "buffer_load_dwordx4 v110, %[srdT], %[s128] offen offset:3072 lds\n"
      "s_add_u32 m0, %[sLb], 20480\n"
      "s_nop 0\n"
      "buffer_load_dwordx4 v114, %[srdT], %[s128] offen offset:0 lds\n"
      "buffer_load_dwordx4 v118, %[srdT], %[s128] offen offset:1024 lds\n"
      "buffer_load_dwordx4 v122, %[srdT], %[s128] offen offset:2048 lds\n"
      "buffer_load_dwordx4 v126, %[srdT], %[s128] offen offset:3072 lds\n"
      "s_waitcnt vmcnt(16)\n"
      "s_barrier\n"
      "s_add_u32 m0, %[sLb], 32768\n"
      "v_add_u32 v31, %[sV], v99\n"
      "v_min_u32 v99, v99, v31\n"
      "v_lshl_add_u32 v99, v99, 8, %[gc0]\n"
      "buffer_load_dwordx4 v99, %[srdT], 0 offen offset:0 lds\n"
      "v_add_u32 v31, %[sV], v103\n"
      "v_min_u32 v103, v103, v31\n"
      "v_lshl_add_u32 v103, v103, 8, %[gc1]\n"
      "buffer_load_dwordx4 v103, %[srdT], 0 offen offset:1024 lds\n"
      "v_add_u32 v31, %[sV], v107\n"
      "v_min_u32 v107, v107, v31\n"
      "v_lshl_add_u32 v107, v107, 8, %[gc2]\n"
      "buffer_load_dwordx4 v107, %[srdT], 0 offen offset:2048 lds\n"
      "v_add_u32 v31, %[sV], v111\n"
      "v_min_u32 v111, v111, v31\n"
      "v_lshl_add_u32 v111, v111, 8, %[gc3]\n"
      "buffer_load_dwordx4 v111, %[srdT], 0 offen offset:3072 lds\n"
      "s_add_u32 m0, %[sLb], 36864\n"
      "v_add_u32 v31, %[sV], v115\n"
      "v_min_u32 v115, v115, v31\n"
      "v_lshl_add_u32 v115, v115, 8, %[gc0]\n"
      "buffer_load_dwordx4 v115, %[srdT], 0 offen offset:0 lds\n"
      "v_add_u32 v31, %[sV], v119\n"
      "v_min_u32 v119, v119, v31\n"
      "v_lshl_add_u32 v119, v119, 8, %[gc1]\n"
      "buffer_load_dwordx4 v119, %[srdT], 0 offen offset:1024 lds\n"
      "v_add_u32 v31, %[sV], v123\n"
      "v_min_u32 v123, v123, v31\n"
      "v_lshl_add_u32 v123, v123, 8, %[gc2]\n"
      "buffer_load_dwordx4 v123, %[srdT], 0 offen offset:2048 lds\n"
      "v_add_u32 v31, %[sV], v127\n"
      "v_min_u32 v127, v127, v31\n"
      "v_lshl_add_u32 v127, v127, 8, %[gc3]\n"
      "buffer_load_dwordx4 v127, %[srdT], 0 offen offset:3072 lds\n"
      "s_waitcnt vmcnt(16)\n"
      "s_barrier\n"
      "s_add_u32 m0, %[sLb], 49152\n"
      "s_nop 0\n"
      "buffer_load_dwordx4 v99, %[srdT], %[s128] offen offset:0 lds\n"
      "buffer_load_dwordx4 v103, %[srdT], %[s128] offen offset:1024 lds\n"
      "buffer_load_dwordx4 v107, %[srdT], %[s128] offen offset:2048 lds\n"
      "buffer_load_dwordx4 v111, %[srdT], %[s128] offen offset:3072 lds\n"
      "s_add_u32 m0, %[sLb], 53248\n"
      "s_nop 0\n"
      "buffer_load_dwordx4 v115, %[srdT], %[s128] offen offset:0 lds\n"
      "buffer_load_dwordx4 v119, %[srdT], %[s128] offen offset:1024 lds\n"
      "buffer_load_dwordx4 v123, %[srdT], %[s128] offen offset:2048 lds\n"
      "buffer_load_dwordx4 v127, %[srdT], %[s128] offen offset:3072 lds\n"
      "s_waitcnt vmcnt(16)\n"
      "s_barrier\n"
      "s_add_u32 m0, %[sLb], 0\n"
      "v_add_u32 v31, %[sV], v32\n"
      "v_min_u32 v32, v32, v31\n"
      "v_lshl_add_u32 v32, v32, 8, %[gc0]\n"
      "buffer_load_dwordx4 v32, %[srdT], 0 offen offset:0 lds\n"
      "v_add_u32 v31, %[sV], v36\n"
      "v_min_u32 v36, v36, v31\n"
      "v_lshl_add_u32 v36, v36, 8, %[gc1]\n"
      "buffer_load_dwordx4 v36, %[srdT], 0 offen offset:1024 lds\n"
      "v_add_u32 v31, %[sV], v40\n"
      "v_min_u32 v40, v40, v31\n"
      "v_lshl_add_u32 v40, v40, 8, %[gc2]\n"
      "buffer_load_dwordx4 v40, %[srdT], 0 offen offset:2048 lds\n"
      "v_add_u32 v31, %[sV], v44\n"
      "v_min_u32 v44, v44, v31\n"
      "v_lshl_add_u32 v44, v44, 8, %[gc3]\n"
      "buffer_load_dwordx4 v44, %[srdT], 0 offen offset:3072 lds\n"
      "s_add_u32 m0, %[sLb], 4096\n"
      "v_add_u32 v31, %[sV], v48\n"
      "v_min_u32 v48, v48, v31\n"
      "v_lshl_add_u32 v48, v48, 8, %[gc0]\n"
      "buffer_load_dwordx4 v48, %[srdT], 0 offen offset:0 lds\n"
      "v_add_u32 v31, %[sV], v52\n"
      "v_min_u32 v52, v52, v31\n"
      "v_lshl_add_u32 v52, v52, 8, %[gc1]\n"
      "buffer_load_dwordx4 v52, %[srdT], 0 offen offset:1024 lds\n"
      "v_add_u32 v31, %[sV], v56\n"
      "v_min_u32 v56, v56, v31\n"
      "v_lshl_add_u32 v56, v56, 8, %[gc2]\n"
      "buffer_load_dwordx4 v56, %[srdT], 0 offen offset:2048 lds\n"
      "v_add_u32 v31, %[sV], v60\n"
      "v_min_u32 v60, v60, v31\n"
      "v_lshl_add_u32 v60, v60, 8, %[gc3]\n"
      "buffer_load_dwordx4 v60, %[srdT], 0 offen offset:3072 lds\n"
      "s_waitcnt vmcnt(16)\n"
      "s_barrier\n"
      "s_add_u32 m0, %[sLb], 16384\n"
      "s_nop 0\n"
      "buffer_load_dwordx4 v32, %[srdT], %[s128] offen offset:0 lds\n"
      "buffer_load_dwordx4 v36, %[srdT], %[s128] offen offset:1024 lds\n"
      "buffer_load_dwordx4 v40, %[srdT], %[s128] offen offset:2048 lds\n"
      "buffer_load_dwordx4 v44, %[srdT], %[s128] offen offset:3072 lds\n"
      "s_add_u32 m0, %[sLb], 20480\n"
      "s_nop 0\n"
      "buffer_load_dwordx4 v48, %[srdT], %[s128] offen offset:0 lds\n"
      "buffer_load_dwordx4 v52, %[srdT], %[s128] offen offset:1024 lds\n"
      "buffer_load_dwordx4 v56, %[srdT], %[s128] offen offset:2048 lds\n"
      "buffer_load_dwordx4 v60, %[srdT], %[s128] offen offset:3072 lds\n"
      "s_waitcnt vmcnt(16)\n"
      "s_barrier\n"
      "s_add_u32 m0, %[sLb], 32768\n"
      "v_add_u32 v31, %[sV], v33\n"
      "v_min_u32 v33, v33, v31\n"
      "v_lshl_add_u32 v33, v33, 8, %[gc0]\n"
      "buffer_load_dwordx4 v33, %[srdT], 0 offen offset:0 lds\n"
      "v_add_u32 v31, %[sV], v37\n"
      "v_min_u32 v37, v37, v31\n"
      "v_lshl_add_u32 v37, v37, 8, %[gc1]\n"
      "buffer_load_dwordx4 v37, %[srdT], 0 offen offset:1024 lds\n"
      "v_add_u32 v31, %[sV], v41\n"
      "v_min_u32 v41, v41, v31\n"
      "v_lshl_add_u32 v41, v41, 8, %[gc2]\n"
      "buffer_load_dwordx4 v41, %[srdT], 0 offen offset:2048 lds\n"
      "v_add_u32 v31, %[sV], v45\n"
      "v_min_u32 v45, v45, v31\n"
      "v_lshl_add_u32 v45, v45, 8, %[gc3]\n"
      "buffer_load_dwordx4 v45, %[srdT], 0 offen offset:3072 lds\n"
      "s_add_u32 m0, %[sLb], 36864\n"
      "v_add_u32 v31, %[sV], v49\n"
      "v_min_u32 v49, v49, v31\n"
      "v_lshl_add_u32 v49, v49, 8, %[gc0]\n"
      "buffer_load_dwordx4 v49, %[srdT], 0 offen offset:0 lds\n"
      "v_add_u32 v31, %[sV], v53\n"
      "v_min_u32 v53, v53, v31\n"
      "v_lshl_add_u32 v53, v53, 8, %[gc1]\n"
      "buffer_load_dwordx4 v53, %[srdT], 0 offen offset:1024 lds\n"
      "v_add_u32 v31, %[sV], v57\n"
      "v_min_u32 v57, v57, v31\n"
      "v_lshl_add_u32 v57, v57, 8, %[gc2]\n"
      "buffer_load_dwordx4 v57, %[srdT], 0 offen offset:2048 lds\n"
      "v_add_u32 v31, %[sV], v61\n"
      "v_min_u32 v61, v61, v31\n"
      "v_lshl_add_u32 v61, v61, 8, %[gc3]\n"
      "buffer_load_dwordx4 v61, %[srdT], 0 offen offset:3072 lds\n"
      "s_waitcnt vmcnt(16)\n"
      "s_barrier\n"
      "s_add_u32 m0, %[sLb], 49152\n"
      "s_nop 0\n"
      "buffer_load_dwordx4 v33, %[srdT], %[s128] offen offset:0 lds\n"
      "buffer_load_dwordx4 v37, %[srdT], %[s128] offen offset:1024 lds\n"
      "buffer_load_dwordx4 v41, %[srdT], %[s128] offen offset:2048 lds\n"
      "buffer_load_dwordx4 v45, %[srdT], %[s128] offen offset:3072 lds\n"
      "s_add_u32 m0, %[sLb], 53248\n"
      "s_nop 0\n"
      "buffer_load_dwordx4 v49, %[srdT], %[s128] offen offset:0 lds\n"
      "buffer_load_dwordx4 v53, %[srdT], %[s128] offen offset:1024 lds\n"
      "buffer_load_dwordx4 v57, %[srdT], %[s128] offen offset:2048 lds\n"
      "buffer_load_dwordx4 v61, %[srdT], %[s128] offen offset:3072 lds\n"
      "s_waitcnt vmcnt(16)\n"
      "s_barrier\n"
      "s_waitcnt vmcnt(8)\n"
      "s_barrier\n"
      "s_waitcnt vmcnt(0)\n"
      "s_barrier\n"
      :
      : [gc0] "v"(gc0), [gc1] "v"(gc1), [gc2] "v"(gc2), [gc3] "v"(gc3), [vdi] "v"(vdi), [srdT] "s"(srdT), [srdI] "s"(srdI),
        [sLb] "s"(sLb), [sV] "s"(V), [s4k] "s"(3328u), [s128] "s"(128u)
      : "memory", "scc", "v31", "v32", "v33", "v34", "v35", "v36", "v37", "v38", "v39", "v40", "v41", "v42", "v43", "v44", "v45", "v46", "v47", "v48", "v49", "v50", "v51", "v52", "v53", "v54", "v55", "v56", "v57", "v58", "v59", "v60", "v61", "v62", "v63", "v64", "v65", "v66", "v67", "v68", "v69", "v70", "v71", "v72", "v73", "v74", "v75", "v76", "v77", "v78", "v79", "v80", "v81", "v82", "v83", "v84", "v85", "v86", "v87", "v88", "v89", "v90", "v91", "v92", "v93", "v94", "v95", "v96", "v97", "v98", "v99", "v100", "v101", "v102", "v103", "v104", "v105", "v106", "v107", "v108", "v109", "v110", "v111", "v112", "v113", "v114", "v115", "v116", "v117", "v118", "v119", "v120", "v121", "v122", "v123", "v124", "v125", "v126", "v127");
    } else {
    asm volatile(
      "s_nop 4\n"
      "s_setprio 2\n"
      "buffer_load_dwordx4 v[32:35], %[vdi], %[srdI], 0 offen offset:0\n"
      "buffer_load_dwordx4 v[36:39], %[vdi], %[srdI], 0 offen offset:832\n"
      "buffer_load_dwordx4 v[40:43], %[vdi], %[srdI], 0 offen offset:1664\n"
      "buffer_load_dwordx4 v[44:47], %[vdi], %[srdI], 0 offen offset:2496\n"
      "buffer_load_dwordx4 v[48:51], %[vdi], %[srdI], 0 offen offset:3328\n"
      "buffer_load_dwordx4 v[52:55], %[vdi], %[srdI], %[s4k] offen offset:832\n"
      "buffer_load_dwordx4 v[56:59], %[vdi], %[srdI], %[s4k] offen offset:1664\n"
      "buffer_load_dwordx4 v[60:63], %[vdi], %[srdI], %[s4k] offen offset:2496\n"
      "s_add_u32 m0, %[sLb], 0\n"
      "s_waitcnt vmcnt(7)\n"
      "v_add_u32 v31, %[sV], v32\n"
      "v_min_u32 v32, v32, v31\n"
      "v_lshl_add_u32 v32, v32, 8, %[gc0]\n"
      "buffer_load_dwordx4 v32, %[srdT], %[s128] offen offset:0 lds\n"
      "s_waitcnt vmcnt(7)\n"
      "v_add_u32 v31, %[sV], v36\n"
      "v_min_u32 v36, v36, v31\n"
      "v_lshl_add_u32 v36, v36, 8, %[gc1]\n"
      "buffer_load_dwordx4 v36, %[srdT], %[s128] offen offset:1024 lds\n"
      "s_waitcnt vmcnt(7)\n"
      "v_add_u32 v31, %[sV], v40\n"
      "v_min_u32 v40, v40, v31\n"
      "v_lshl_add_u32 v40, v40, 8, %[gc2]\n"
      "buffer_load_dwordx4 v40, %[srdT], %[s128] offen offset:2048 lds\n"
      "s_waitcnt vmcnt(7)\n"
      "v_add_u32 v31, %[sV], v44\n"
      "v_min_u32 v44, v44, v31\n"
      "v_lshl_add_u32 v44, v44, 8, %[gc3]\n"
      "buffer_load_dwordx4 v44, %[srdT], %[s128] offen offset:3072 lds\n"
      "s_add_u32 m0, %[sLb], 4096\n"
      "s_waitcnt vmcnt(7)\n"
      "v_add_u32 v31, %[sV], v48\n"
      "v_min_u32 v48, v48, v31\n"
      "v_lshl_add_u32 v48, v48, 8, %[gc0]\n"
      "buffer_load_dwordx4 v48, %[srdT], %[s128] offen offset:0 lds\n"
      "s_waitcnt vmcnt(7)\n"
      "v_add_u32 v31, %[sV], v52\n"
      "v_min_u32 v52, v52, v31\n"
      "v_lshl_add_u32 v52, v52, 8, %[gc1]\n"
      "buffer_load_dwordx4 v52, %[srdT], %[s128] offen offset:1024 lds\n"
      "s_waitcnt vmcnt(7)\n"
      "v_add_u32 v31, %[sV], v56\n"
      "v_min_u32 v56, v56, v31\n"
      "v_lshl_add_u32 v56, v56, 8, %[gc2]\n"
      "buffer_load_dwordx4 v56, %[srdT], %[s128] offen offset:2048 lds\n"
      "s_waitcnt vmcnt(7)\n"
      "v_add_u32 v31, %[sV], v60\n"
      "v_min_u32 v60, v60, v31\n"
      "v_lshl_add_u32 v60, v60, 8, %[gc3]\n"
      "buffer_load_dwordx4 v60, %[srdT], %[s128] offen offset:3072 lds\n"
      "s_add_u32 m0, %[sLb], 16384\n"
      "s_nop 0\n"
      "buffer_load_dwordx4 v32, %[srdT], 0 offen offset:0 lds\n"
      "buffer_load_dwordx4 v36, %[srdT], 0 offen offset:1024 lds\n"
      "buffer_load_dwordx4 v40, %[srdT], 0 offen offset:2048 lds\n"
      "buffer_load_dwordx4 v44, %[srdT], 0 offen offset:3072 lds\n"
      "s_add_u32 m0, %[sLb], 20480\n"
      "s_nop 0\n"
      "buffer_load_dwordx4 v48, %[srdT], 0 offen offset:0 lds\n"
      "buffer_load_dwordx4 v52, %[srdT], 0 offen offset:1024 lds\n"
      "buffer_load_dwordx4 v56, %[srdT], 0 offen offset:2048 lds\n"
      "buffer_load_dwordx4 v60, %[srdT], 0 offen offset:3072 lds\n"
      "s_waitcnt vmcnt(8)\n"
      "s_barrier\n"
      "s_add_u32 m0, %[sLb], 32768\n"
      "v_add_u32 v31, %[sV], v33\n"
      "v_min_u32 v33, v33, v31\n"
      "v_lshl_add_u32 v33, v33, 8, %[gc0]\n"
      "buffer_load_dwordx4 v33, %[srdT], %[s128] offen offset:0 lds\n"
      "v_add_u32 v31, %[sV], v37\n"
      "v_min_u32 v37, v37, v31\n"
      "v_lshl_add_u32 v37, v37, 8, %[gc1]\n"
      "buffer_load_dwordx4 v37, %[srdT], %[s128] offen offset:1024 lds\n"
      "v_add_u32 v31, %[sV], v41\n"
      "v_min_u32 v41, v41, v31\n"
      "v_lshl_add_u32 v41, v41, 8, %[gc2]\n"
      "buffer_load_dwordx4 v41, %[srdT], %[s128] offen offset:2048 lds\n"
      "v_add_u32 v31, %[sV], v45\n"
      "v_min_u32 v45, v45, v31\n"
      "v_lshl_add_u32 v45, v45, 8, %[gc3]\n"
      "buffer_load_dwordx4 v45, %[srdT], %[s128] offen offset:3072 lds\n"
      "s_add_u32 m0, %[sLb], 36864\n"
      "v_add_u32 v31, %[sV], v49\n"
      "v_min_u32 v49, v49, v31\n"
      "v_lshl_add_u32 v49, v49, 8, %[gc0]\n"
      "buffer_load_dwordx4 v49, %[srdT], %[s128] offen offset:0 lds\n"
      "v_add_u32 v31, %[sV], v53\n"
      "v_min_u32 v53, v53, v31\n"
      "v_lshl_add_u32 v53, v53, 8, %[gc1]\n"
      "buffer_load_dwordx4 v53, %[srdT], %[s128] offen offset:1024 lds\n"
      "v_add_u32 v31, %[sV], v57\n"
      "v_min_u32 v57, v57, v31\n"
      "v_lshl_add_u32 v57, v57, 8, %[gc2]\n"
      "buffer_load_dwordx4 v57, %[srdT], %[s128] offen offset:2048 lds\n"
      "v_add_u32 v31, %[sV], v61\n"
      "v_min_u32 v61, v61, v31\n"
      "v_lshl_add_u32 v61, v61, 8, %[gc3]\n"
      "buffer_load_dwordx4 v61, %[srdT], %[s128] offen offset:3072 lds\n"
      "s_add_u32 m0, %[sLb], 49152\n"
      "s_nop 0\n"
      "buffer_load_dwordx4 v33, %[srdT], 0 offen offset:0 lds\n"
      "buffer_load_dwordx4 v37, %[srdT], 0 offen offset:1024 lds\n"
      "buffer_load_dwordx4 v41, %[srdT], 0 offen offset:2048 lds\n"
      "buffer_load_dwordx4 v45, %[srdT], 0 offen offset:3072 lds\n"
      "s_add_u32 m0, %[sLb], 53248\n"
      "s_nop 0\n"
      "buffer_load_dwordx4 v49, %[srdT], 0 offen offset:0 lds\n"
      "buffer_load_dwordx4 v53, %[srdT], 0 offen offset:1024 lds\n"
      "buffer_load_dwordx4 v57, %[srdT], 0 offen offset:2048 lds\n"
      "buffer_load_dwordx4 v61, %[srdT], 0 offen offset:3072 lds\n"
      "buffer_load_dwordx4 v[64:67], %[vdi], %[srdI], 0 offen offset:16\n"
      "buffer_load_dwordx4 v[68:71], %[vdi], %[srdI], 0 offen offset:848\n"
      "buffer_load_dwordx4 v[72:75], %[vdi], %[srdI], 0 offen offset:1680\n"
      "buffer_load_dwordx4 v[76:79], %[vdi], %[srdI], 0 offen offset:2512\n"
      "buffer_load_dwordx4 v[80:83], %[vdi], %[srdI], 0 offen offset:3344\n"
      "buffer_load_dwordx4 v[84:87], %[vdi], %[srdI], %[s4k] offen offset:848\n"
      "buffer_load_dwordx4 v[88:91], %[vdi], %[srdI], %[s4k] offen offset:1680\n"
      "buffer_load_dwordx4 v[92:95], %[vdi], %[srdI], %[s4k] offen offset:2512\n"
      "s_waitcnt vmcnt(24)\n"
      "s_barrier\n"
      "s_add_u32 m0, %[sLb], 0\n"
      "v_add_u32 v31, %[sV], v34\n"
      "v_min_u32 v34, v34, v31\n"
      "v_lshl_add_u32 v34, v34, 8, %[gc0]\n"
      "buffer_load_dwordx4 v34, %[srdT], %[s128] offen offset:0 lds\n"
      "v_add_u32 v31, %[sV], v38\n"
      "v_min_u32 v38, v38, v31\n"
      "v_lshl_add_u32 v38, v38, 8, %[gc1]\n"
      "buffer_load_dwordx4 v38, %[srdT], %[s128] offen offset:1024 lds\n"
      "v_add_u32 v31, %[sV], v42\n"
      "v_min_u32 v42, v42, v31\n"
      "v_lshl_add_u32 v42, v42, 8, %[gc2]\n"
      "buffer_load_dwordx4 v42, %[srdT], %[s128] offen offset:2048 lds\n"
      "v_add_u32 v31, %[sV], v46\n"
      "v_min_u32 v46, v46, v31\n"
      "v_lshl_add_u32 v46, v46, 8, %[gc3]\n"
      "buffer_load_dwordx4 v46, %[srdT], %[s128] offen offset:3072 lds\n"
      "s_add_u32 m0, %[sLb], 4096\n"
      "v_add_u32 v31, %[sV], v50\n"
      "v_min_u32 v50, v50, v31\n"
      "v_lshl_add_u32 v50, v50, 8, %[gc0]\n"
      "buffer_load_dwordx4 v50, %[srdT], %[s128] offen offset:0 lds\n"
      "v_add_u32 v31, %[sV], v54\n"
      "v_min_u32 v54, v54, v31\n"
      "v_lshl_add_u32 v54, v54, 8, %[gc1]\n"
      "buffer_load_dwordx4 v54, %[srdT], %[s128] offen offset:1024 lds\n"
      "v_add_u32 v31, %[sV], v58\n"
      "v_min_u32 v58, v58, v31\n"
      "v_lshl_add_u32 v58, v58, 8, %[gc2]\n"
      "buffer_load_dwordx4 v58, %[srdT], %[s128] offen offset:2048 lds\n"
      "v_add_u32 v31, %[sV], v62\n"
      "v_min_u32 v62, v62, v31\n"
      "v_lshl_add_u32 v62, v62, 8, %[gc3]\n"
      "buffer_load_dwordx4 v62, %[srdT], %[s128] offen offset:3072 lds\n"
      "s_waitcnt vmcnt(24)\n"
      "s_barrier\n"
      "s_add_u32 m0, %[sLb], 16384\n"
      "s_nop 0\n"
      "buffer_load_dwordx4 v34, %[srdT], 0 offen offset:0 lds\n"
      "buffer_load_dwordx4 v38, %[srdT], 0 offen offset:1024 lds\n"
      "buffer_load_dwordx4 v42, %[srdT], 0 offen offset:2048 lds\n"
      "buffer_load_dwordx4 v46, %[srdT], 0 offen offset:3072 lds\n"
      "s_add_u32 m0, %[sLb], 20480\n"
      "s_nop 0\n"
      "buffer_load_dwordx4 v50, %[srdT], 0 offen offset:0 lds\n"
      "buffer_load_dwordx4 v54, %[srdT], 0 offen offset:1024 lds\n"
      "buffer_load_dwordx4 v58, %[srdT], 0 offen offset:2048 lds\n"
      "buffer_load_dwordx4 v62, %[srdT], 0 offen offset:3072 lds\n"
      "s_waitcnt vmcnt(24)\n"
      "s_barrier\n"
      "s_add_u32 m0, %[sLb], 32768\n"
      "v_add_u32 v31, %[sV], v35\n"
      "v_min_u32 v35, v35, v31\n"
      "v_lshl_add_u32 v35, v35, 8, %[gc0]\n"
      "buffer_load_dwordx4 v35, %[srdT], %[s128] offen offset:0 lds\n"
      "v_add_u32 v31, %[sV], v39\n"
      "v_min_u32 v39, v39, v31\n"
      "v_lshl_add_u32 v39, v39, 8, %[gc1]\n"
      "buffer_load_dwordx4 v39, %[srdT], %[s128] offen offset:1024 lds\n"
      "v_add_u32 v31, %[sV], v43\n"
      "v_min_u32 v43, v43, v31\n"
      "v_lshl_add_u32 v43, v43, 8, %[gc2]\n"
      "buffer_load_dwordx4 v43, %[srdT], %[s128] offen offset:2048 lds\n"
      "v_add_u32 v31, %[sV], v47\n"
      "v_min_u32 v47, v47, v31\n"
      "v_lshl_add_u32 v47, v47, 8, %[gc3]\n"
      "buffer_load_dwordx4 v47, %[srdT], %[s128] offen offset:3072 lds\n"
      "s_add_u32 m0, %[sLb], 36864\n"
      "v_add_u32 v31, %[sV], v51\n"
      "v_min_u32 v51, v51, v31\n"
      "v_lshl_add_u32 v51, v51, 8, %[gc0]\n"
      "buffer_load_dwordx4 v51, %[srdT], %[s128] offen offset:0 lds\n"
      "v_add_u32 v31, %[sV], v55\n"
      "v_min_u32 v55, v55, v31\n"
      "v_lshl_add_u32 v55, v55, 8, %[gc1]\n"
      "buffer_load_dwordx4 v55, %[srdT], %[s128] offen offset:1024 lds\n"
      "v_add_u32 v31, %[sV], v59\n"
      "v_min_u32 v59, v59, v31\n"
      "v_lshl_add_u32 v59, v59, 8, %[gc2]\n"
      "buffer_load_dwordx4 v59, %[srdT], %[s128] offen offset:2048 lds\n"
      "v_add_u32 v31, %[sV], v63\n"
      "v_min_u32 v63, v63, v31\n"
      "v_lshl_add_u32 v63, v63, 8, %[gc3]\n"
      "buffer_load_dwordx4 v63, %[srdT], %[s128] offen offset:3072 lds\n"
      "s_waitcnt vmcnt(16)\n"
      "s_barrier\n"
      "s_add_u32 m0, %[sLb], 49152\n"
      "s_nop 0\n"
      "buffer_load_dwordx4 v35, %[srdT], 0 offen offset:0 lds\n"
      "buffer_load_dwordx4 v39, %[srdT], 0 offen offset:1024 lds\n"
      "buffer_load_dwordx4 v43, %[srdT], 0 offen offset:2048 lds\n"
      "buffer_load_dwordx4 v47, %[srdT], 0 offen offset:3072 lds\n"
      "s_add_u32 m0, %[sLb], 53248\n"
      "s_nop 0\n"
      "buffer_load_dwordx4 v51, %[srdT], 0 offen offset:0 lds\n"
      "buffer_load_dwordx4 v55, %[srdT], 0 offen offset:1024 lds\n"
      "buffer_load_dwordx4 v59, %[srdT], 0 offen offset:2048 lds\n"
      "buffer_load_dwordx4 v63, %[srdT], 0 offen offset:3072 lds\n"
      "buffer_load_dwordx4 v[96:99], %[vdi], %[srdI], 0 offen offset:32\n"
      "buffer_load_dwordx4 v[100:103], %[vdi], %[srdI], 0 offen offset:864\n"
      "buffer_load_dwordx4 v[104:107], %[vdi], %[srdI], 0 offen offset:1696\n"
      "buffer_load_dwordx4 v[108:111], %[vdi], %[srdI], 0 offen offset:2528\n"
      "buffer_load_dwordx4 v[112:115], %[vdi], %[srdI], 0 offen offset:3360\n"
      "buffer_load_dwordx4 v[116:119], %[vdi], %[srdI], %[s4k] offen offset:864\n"
      "buffer_load_dwordx4 v[120:123], %[vdi], %[srdI], %[s4k] offen offset:1696\n"
      "buffer_load_dwordx4 v[124:127], %[vdi], %[srdI], %[s4k] offen offset:2528\n"
      "s_waitcnt vmcnt(24)\n"
      "s_barrier\n"
      "s_add_u32 m0, %[sLb], 0\n"
      "v_add_u32 v31, %[sV], v64\n"
      "v_min_u32 v64, v64, v31\n"
      "v_lshl_add_u32 v64, v64, 8, %[gc0]\n"
      "buffer_load_dwordx4 v64, %[srdT], %[s128] offen offset:0 lds\n"
      "v_add_u32 v31, %[sV], v68\n"
      "v_min_u32 v68, v68, v31\n"
      "v_lshl_add_u32 v68, v68, 8, %[gc1]\n"
      "buffer_load_dwordx4 v68, %[srdT], %[s128] offen offset:1024 lds\n"
      "v_add_u32 v31, %[sV], v72\n"
      "v_min_u32 v72, v72, v31\n"
      "v_lshl_add_u32 v72, v72, 8, %[gc2]\n"
      "buffer_load_dwordx4 v72, %[srdT], %[s128] offen offset:2048 lds\n"
      "v_add_u32 v31, %[sV], v76\n"
      "v_min_u32 v76, v76, v31\n"
      "v_lshl_add_u32 v76, v76, 8, %[gc3]\n"
      "buffer_load_dwordx4 v76, %[srdT], %[s128] offen offset:3072 lds\n"
      "s_add_u32 m0, %[sLb], 4096\n"
      "v_add_u32 v31, %[sV], v80\n"
      "v_min_u32 v80, v80, v31\n"
      "v_lshl_add_u32 v80, v80, 8, %[gc0]\n"
      "buffer_load_dwordx4 v80, %[srdT], %[s128] offen offset:0 lds\n"
      "v_add_u32 v31, %[sV], v84\n"
      "v_min_u32 v84, v84, v31\n"
      "v_lshl_add_u32 v84, v84, 8, %[gc1]\n"
      "buffer_load_dwordx4 v84, %[srdT], %[s128] offen offset:1024 lds\n"
      "v_add_u32 v31, %[sV], v88\n"
      "v_min_u32 v88, v88, v31\n"
      "v_lshl_add_u32 v88, v88, 8, %[gc2]\n"
      "buffer_load_dwordx4 v88, %[srdT], %[s128] offen offset:2048 lds\n"
      "v_add_u32 v31, %[sV], v92\n"
      "v_min_u32 v92, v92, v31\n"
      "v_lshl_add_u32 v92, v92, 8, %[gc3]\n"
      "buffer_load_dwordx4 v92, %[srdT], %[s128] offen offset:3072 lds\n"
      "s_waitcnt vmcnt(24)\n"
      "s_barrier\n"
      "s_add_u32 m0, %[sLb], 16384\n"
      "s_nop 0\n"
      "buffer_load_dwordx4 v64, %[srdT], 0 offen offset:0 lds\n"
      "buffer_load_dwordx4 v68, %[srdT], 0 offen offset:1024 lds\n"
      "buffer_load_dwordx4 v72, %[srdT], 0 offen offset:2048 lds\n"
      "buffer_load_dwordx4 v76, %[srdT], 0 offen offset:3072 lds\n"
      "s_add_u32 m0, %[sLb], 20480\n"
      "s_nop 0\n"
      "buffer_load_dwordx4 v80, %[srdT], 0 offen offset:0 lds\n"
      "buffer_load_dwordx4 v84, %[srdT], 0 offen offset:1024 lds\n"
      "buffer_load_dwordx4 v88, %[srdT], 0 offen offset:2048 lds\n"
      "buffer_load_dwordx4 v92, %[srdT], 0 offen offset:3072 lds\n"
      "s_waitcnt vmcnt(24)\n"
      "s_barrier\n"
      "s_add_u32 m0, %[sLb], 32768\n"
      "v_add_u32 v31, %[sV], v65\n"
      "v_min_u32 v65, v65, v31\n"
      "v_lshl_add_u32 v65, v65, 8, %[gc0]\n"
      "buffer_load_dwordx4 v65, %[srdT], %[s128] offen offset:0 lds\n"
      "v_add_u32 v31, %[sV], v69\n"
      "v_min_u32 v69, v69, v31\n"
      "v_lshl_add_u32 v69, v69, 8, %[gc1]\n"
      "buffer_load_dwordx4 v69, %[srdT], %[s128] offen offset:1024 lds\n"
      "v_add_u32 v31, %[sV], v73\n"
      "v_min_u32 v73, v73, v31\n"
      "v_lshl_add_u32 v73, v73, 8, %[gc2]\n"
      "buffer_load_dwordx4 v73, %[srdT], %[s128] offen offset:2048 lds\n"
      "v_add_u32 v31, %[sV], v77\n"
      "v_min_u32 v77, v77, v31\n"
      "v_lshl_add_u32 v77, v77, 8, %[gc3]\n"
      "buffer_load_dwordx4 v77, %[srdT], %[s128] offen offset:3072 lds\n"
      "s_add_u32 m0, %[sLb], 36864\n"
      "v_add_u32 v31, %[sV], v81\n"
      "v_min_u32 v81, v81, v31\n"
      "v_lshl_add_u32 v81, v81, 8, %[gc0]\n"
      "buffer_load_dwordx4 v81, %[srdT], %[s128] offen offset:0 lds\n"
      "v_add_u32 v31, %[sV], v85\n"
      "v_min_u32 v85, v85, v31\n"
      "v_lshl_add_u32 v85, v85, 8, %[gc1]\n"
      "buffer_load_dwordx4 v85, %[srdT], %[s128] offen offset:1024 lds\n"
      "v_add_u32 v31, %[sV], v89\n"
      "v_min_u32 v89, v89, v31\n"
      "v_lshl_add_u32 v89, v89, 8, %[gc2]\n"
      "buffer_load_dwordx4 v89, %[srdT], %[s128] offen offset:2048 lds\n"
      "v_add_u32 v31, %[sV], v93\n"
      "v_min_u32 v93, v93, v31\n"
      "v_lshl_add_u32 v93, v93, 8, %[gc3]\n"
      "buffer_load_dwordx4 v93, %[srdT], %[s128] offen offset:3072 lds\n"
      "s_waitcnt vmcnt(16)\n"
      "s_barrier\n"
      "s_add_u32 m0, %[sLb], 49152\n"
      "s_nop 0\n"
      "buffer_load_dwordx4 v65, %[srdT], 0 offen offset:0 lds\n"
      "buffer_load_dwordx4 v69, %[srdT], 0 offen offset:1024 lds\n"
      "buffer_load_dwordx4 v73, %[srdT], 0 offen offset:2048 lds\n"
      "buffer_load_dwordx4 v77, %[srdT], 0 offen offset:3072 lds\n"
      "s_add_u32 m0, %[sLb], 53248\n"
      "s_nop 0\n"
      "buffer_load_dwordx4 v81, %[srdT], 0 offen offset:0 lds\n"
      "buffer_load_dwordx4 v85, %[srdT], 0 offen offset:1024 lds\n"
      "buffer_load_dwordx4 v89, %[srdT], 0 offen offset:2048 lds\n"
      "buffer_load_dwordx4 v93, %[srdT], 0 offen offset:3072 lds\n"
      "buffer_load_dwordx4 v[32:35], %[vdi], %[srdI], 0 offen offset:48\n"
      "s_waitcnt vmcnt(17)\n"
      "s_barrier\n"
      "s_add_u32 m0, %[sLb], 0\n"
      "v_add_u32 v31, %[sV], v66\n"
      "v_min_u32 v66, v66, v31\n"
      "v_lshl_add_u32 v66, v66, 8, %[gc0]\n"
      "buffer_load_dwordx4 v66, %[srdT], %[s128] offen offset:0 lds\n"
      "v_add_u32 v31, %[sV], v70\n"
      "v_min_u32 v70, v70, v31\n"
      "v_lshl_add_u32 v70, v70, 8, %[gc1]\n"
      "buffer_load_dwordx4 v70, %[srdT], %[s128] offen offset:1024 lds\n"
      "v_add_u32 v31, %[sV], v74\n"
      "v_min_u32 v74, v74, v31\n"
      "v_lshl_add_u32 v74, v74, 8, %[gc2]\n"
      "buffer_load_dwordx4 v74, %[srdT], %[s128] offen offset:2048 lds\n"
      "v_add_u32 v31, %[sV], v78\n"
      "v_min_u32 v78, v78, v31\n"
      "v_lshl_add_u32 v78, v78, 8, %[gc3]\n"
      "buffer_load_dwordx4 v78, %[srdT], %[s128] offen offset:3072 lds\n"
      "s_add_u32 m0, %[sLb], 4096\n"
      "v_add_u32 v31, %[sV], v82\n"
      "v_min_u32 v82, v82, v31\n"
      "v_lshl_add_u32 v82, v82, 8, %[gc0]\n"
      "buffer_load_dwordx4 v82, %[srdT], %[s128] offen offset:0 lds\n"
      "v_add_u32 v31, %[sV], v86\n"
      "v_min_u32 v86, v86, v31\n"
      "v_lshl_add_u32 v86, v86, 8, %[gc1]\n"
      "buffer_load_dwordx4 v86, %[srdT], %[s128] offen offset:1024 lds\n"
      "v_add_u32 v31, %[sV], v90\n"
      "v_min_u32 v90, v90, v31\n"
      "v_lshl_add_u32 v90, v90, 8, %[gc2]\n"
      "buffer_load_dwordx4 v90, %[srdT], %[s128] offen offset:2048 lds\n"
      "v_add_u32 v31, %[sV], v94\n"
      "v_min_u32 v94, v94, v31\n"
      "v_lshl_add_u32 v94, v94, 8, %[gc3]\n"
      "buffer_load_dwordx4 v94, %[srdT], %[s128] offen offset:3072 lds\n"
      "buffer_load_dwordx4 v[36:39], %[vdi], %[srdI], 0 offen offset:880\n"
      "s_waitcnt vmcnt(18)\n"
      "s_barrier\n"
      "s_add_u32 m0, %[sLb], 16384\n"
      "s_nop 0\n"
      "buffer_load_dwordx4 v66, %[srdT], 0 offen offset:0 lds\n"
      "buffer_load_dwordx4 v70, %[srdT], 0 offen offset:1024 lds\n"
      "buffer_load_dwordx4 v74, %[srdT], 0 offen offset:2048 lds\n"
      "buffer_load_dwordx4 v78, %[srdT], 0 offen offset:3072 lds\n"
      "s_add_u32 m0, %[sLb], 20480\n"
      "s_nop 0\n"
      "buffer_load_dwordx4 v82, %[srdT], 0 offen offset:0 lds\n"
      "buffer_load_dwordx4 v86, %[srdT], 0 offen offset:1024 lds\n"
      "buffer_load_dwordx4 v90, %[srdT], 0 offen offset:2048 lds\n"
      "buffer_load_dwordx4 v94, %[srdT], 0 offen offset:3072 lds\n"
      "buffer_load_dwordx4 v[40:43], %[vdi], %[srdI], 0 offen offset:1712\n"
      "s_waitcnt vmcnt(19)\n"
      "s_barrier\n"
      "s_add_u32 m0, %[sLb], 32768\n"
      "v_add_u32 v31, %[sV], v67\n"
      "v_min_u32 v67, v67, v31\n"
      "v_lshl_add_u32 v67, v67, 8, %[gc0]\n"
      "buffer_load_dwordx4 v67, %[srdT], %[s128] offen offset:0 lds\n"
      "v_add_u32 v31, %[sV], v71\n"
      "v_min_u32 v71, v71, v31\n"
      "v_lshl_add_u32 v71, v71, 8, %[gc1]\n"
      "buffer_load_dwordx4 v71, %[srdT], %[s128] offen offset:1024 lds\n"
      "v_add_u32 v31, %[sV], v75\n"
      "v_min_u32 v75, v75, v31\n"
      "v_lshl_add_u32 v75, v75, 8, %[gc2]\n"
      "buffer_load_dwordx4 v75, %[srdT], %[s128] offen offset:2048 lds\n"
      "v_add_u32 v31, %[sV], v79\n"
      "v_min_u32 v79, v79, v31\n"
      "v_lshl_add_u32 v79, v79, 8, %[gc3]\n"
      "buffer_load_dwordx4 v79, %[srdT], %[s128] offen offset:3072 lds\n"
      "s_add_u32 m0, %[sLb], 36864\n"
      "v_add_u32 v31, %[sV], v83\n"
      "v_min_u32 v83, v83, v31\n"
      "v_lshl_add_u32 v83, v83, 8, %[gc0]\n"
      "buffer_load_dwordx4 v83, %[srdT], %[s128] offen offset:0 lds\n"
      "v_add_u32 v31, %[sV], v87\n"
      "v_min_u32 v87, v87, v31\n"
      "v_lshl_add_u32 v87, v87, 8, %[gc1]\n"
      "buffer_load_dwordx4 v87, %[srdT], %[s128] offen offset:1024 lds\n"
      "v_add_u32 v31, %[sV], v91\n"
      "v_min_u32 v91, v91, v31\n"
      "v_lshl_add_u32 v91, v91, 8, %[gc2]\n"
      "buffer_load_dwordx4 v91, %[srdT], %[s128] offen offset:2048 lds\n"
      "v_add_u32 v31, %[sV], v95\n"
      "v_min_u32 v95, v95, v31\n"
      "v_lshl_add_u32 v95, v95, 8, %[gc3]\n"
      "buffer_load_dwordx4 v95, %[srdT], %[s128] offen offset:3072 lds\n"
      "buffer_load_dwordx4 v[44:47], %[vdi], %[srdI], 0 offen offset:2544\n"
      "s_waitcnt vmcnt(19)\n"
      "s_barrier\n"
      "s_add_u32 m0, %[sLb], 49152\n"
      "s_nop 0\n"
      "buffer_load_dwordx4 v67, %[srdT], 0 offen offset:0 lds\n"
      "buffer_load_dwordx4 v71, %[srdT], 0 offen offset:1024 lds\n"
      "buffer_load_dwordx4 v75, %[srdT], 0 offen offset:2048 lds\n"
      "buffer_load_dwordx4 v79, %[srdT], 0 offen offset:3072 lds\n"
      "s_add_u32 m0, %[sLb], 53248\n"
      "s_nop 0\n"
      "buffer_load_dwordx4 v83, %[srdT], 0 offen offset:0 lds\n"
      "buffer_load_dwordx4 v87, %[srdT], 0 offen offset:1024 lds\n"
      "buffer_load_dwordx4 v91, %[srdT], 0 offen offset:2048 lds\n"
      "buffer_load_dwordx4 v95, %[srdT], 0 offen offset:3072 lds\n"
      "buffer_load_dwordx4 v[48:51], %[vdi], %[srdI], 0 offen offset:3376\n"
      "s_waitcnt vmcnt(19)\n"
      "s_barrier\n"
      "s_add_u32 m0, %[sLb], 0\n"
      "v_add_u32 v31, %[sV], v96\n"
      "v_min_u32 v96, v96, v31\n"
      "v_lshl_add_u32 v96, v96, 8, %[gc0]\n"
      "buffer_load_dwordx4 v96, %[srdT], %[s128] offen offset:0 lds\n"
      "v_add_u32 v31, %[sV], v100\n"
      "v_min_u32 v100, v100, v31\n"
      "v_lshl_add_u32 v100, v100, 8, %[gc1]\n"
      "buffer_load_dwordx4 v100, %[srdT], %[s128] offen offset:1024 lds\n"
      "v_add_u32 v31, %[sV], v104\n"
      "v_min_u32 v104, v104, v31\n"
      "v_lshl_add_u32 v104, v104, 8, %[gc2]\n"
      "buffer_load_dwordx4 v104, %[srdT], %[s128] offen offset:2048 lds\n"
      "v_add_u32 v31, %[sV], v108\n"
      "v_min_u32 v108, v108, v31\n"
      "v_lshl_add_u32 v108, v108, 8, %[gc3]\n"
      "buffer_load_dwordx4 v108, %[srdT], %[s128] offen offset:3072 lds\n"
      "s_add_u32 m0, %[sLb], 4096\n"
      "v_add_u32 v31, %[sV], v112\n"
      "v_min_u32 v112, v112, v31\n"
      "v_lshl_add_u32 v112, v112, 8, %[gc0]\n"
      "buffer_load_dwordx4 v112, %[srdT], %[s128] offen offset:0 lds\n"
      "v_add_u32 v31, %[sV], v116\n"
      "v_min_u32 v116, v116, v31\n"
      "v_lshl_add_u32 v116, v116, 8, %[gc1]\n"
      "buffer_load_dwordx4 v116, %[srdT], %[s128] offen offset:1024 lds\n"
      "v_add_u32 v31, %[sV], v120\n"
      "v_min_u32 v120, v120, v31\n"
      "v_lshl_add_u32 v120, v120, 8, %[gc2]\n"
      "buffer_load_dwordx4 v120, %[srdT], %[s128] offen offset:2048 lds\n"
      "v_add_u32 v31, %[sV], v124\n"
      "v_min_u32 v124, v124, v31\n"
      "v_lshl_add_u32 v124, v124, 8, %[gc3]\n"
      "buffer_load_dwordx4 v124, %[srdT], %[s128] offen offset:3072 lds\n"
      "buffer_load_dwordx4 v[52:55], %[vdi], %[srdI], %[s4k] offen offset:880\n"
      "s_waitcnt vmcnt(19)\n"
      "s_barrier\n"
      "s_add_u32 m0, %[sLb], 16384\n"
      "s_nop 0\n"
      "buffer_load_dwordx4 v96, %[srdT], 0 offen offset:0 lds\n"
      "buffer_load_dwordx4 v100, %[srdT], 0 offen offset:1024 lds\n"
      "buffer_load_dwordx4 v104, %[srdT], 0 offen offset:2048 lds\n"
      "buffer_load_dwordx4 v108, %[srdT], 0 offen offset:3072 lds\n"
      "s_add_u32 m0, %[sLb], 20480\n"
      "s_nop 0\n"
      "buffer_load_dwordx4 v112, %[srdT], 0 offen offset:0 lds\n"
      "buffer_load_dwordx4 v116, %[srdT], 0 offen offset:1024 lds\n"
      "buffer_load_dwordx4 v120, %[srdT], 0 offen offset:2048 lds\n"
      "buffer_load_dwordx4 v124, %[srdT], 0 offen offset:3072 lds\n"
      "buffer_load_dwordx4 v[56:59], %[vdi], %[srdI], %[s4k] offen offset:1712\n"
      "s_waitcnt vmcnt(19)\n"
      "s_barrier\n"
      "s_add_u32 m0, %[sLb], 32768\n"
      "v_add_u32 v31, %[sV], v97\n"
      "v_min_u32 v97, v97, v31\n"
      "v_lshl_add_u32 v97, v97, 8, %[gc0]\n"
      "buffer_load_dwordx4 v97, %[srdT], %[s128] offen offset:0 lds\n"
      "v_add_u32 v31, %[sV], v101\n"
      "v_min_u32 v101, v101, v31\n"
      "v_lshl_add_u32 v101, v101, 8, %[gc1]\n"
      "buffer_load_dwordx4 v101, %[srdT], %[s128] offen offset:1024 lds\n"
      "v_add_u32 v31, %[sV], v105\n"
      "v_min_u32 v105, v105, v31\n"
      "v_lshl_add_u32 v105, v105, 8, %[gc2]\n"
      "buffer_load_dwordx4 v105, %[srdT], %[s128] offen offset:2048 lds\n"
      "v_add_u32 v31, %[sV], v109\n"
      "v_min_u32 v109, v109, v31\n"
      "v_lshl_add_u32 v109, v109, 8, %[gc3]\n"
      "buffer_load_dwordx4 v109, %[srdT], %[s128] offen offset:3072 lds\n"
      "s_add_u32 m0, %[sLb], 36864\n"
      "v_add_u32 v31, %[sV], v113\n"
      "v_min_u32 v113, v113, v31\n"
      "v_lshl_add_u32 v113, v113, 8, %[gc0]\n"
      "buffer_load_dwordx4 v113, %[srdT], %[s128] offen offset:0 lds\n"
      "v_add_u32 v31, %[sV], v117\n"
      "v_min_u32 v117, v117, v31\n"
      "v_lshl_add_u32 v117, v117, 8, %[gc1]\n"
      "buffer_load_dwordx4 v117, %[srdT], %[s128] offen offset:1024 lds\n"
      "v_add_u32 v31, %[sV], v121\n"
      "v_min_u32 v121, v121, v31\n"
      "v_lshl_add_u32 v121, v121, 8, %[gc2]\n"
      "buffer_load_dwordx4 v121, %[srdT], %[s128] offen offset:2048 lds\n"
      "v_add_u32 v31, %[sV], v125\n"
      "v_min_u32 v125, v125, v31\n"
      "v_lshl_add_u32 v125, v125, 8, %[gc3]\n"
      "buffer_load_dwordx4 v125, %[srdT], %[s128] offen offset:3072 lds\n"
      "buffer_load_dwordx4 v[60:63], %[vdi], %[srdI], %[s4k] offen offset:2544\n"
      "s_waitcnt vmcnt(19)\n"
      "s_barrier\n"
      "s_add_u32 m0, %[sLb], 49152\n"
      "s_nop 0\n"
      "buffer_load_dwordx4 v97, %[srdT], 0 offen offset:0 lds\n"
      "buffer_load_dwordx4 v101, %[srdT], 0 offen offset:1024 lds\n"
      "buffer_load_dwordx4 v105, %[srdT], 0 offen offset:2048 lds\n"
      "buffer_load_dwordx4 v109, %[srdT], 0 offen offset:3072 lds\n"
      "s_add_u32 m0, %[sLb], 53248\n"
      "s_nop 0\n"
      "buffer_load_dwordx4 v113, %[srdT], 0 offen offset:0 lds\n"
      "buffer_load_dwordx4 v117, %[srdT], 0 offen offset:1024 lds\n"
      "buffer_load_dwordx4 v121, %[srdT], 0 offen offset:2048 lds\n"
      "buffer_load_dwordx4 v125, %[srdT], 0 offen offset:3072 lds\n"
      "buffer_load_dwordx4 v[64:67], %[vdi], %[srdI], 0 offen offset:64\n"
      "s_waitcnt vmcnt(19)\n"
      "s_barrier\n"
      "s_add_u32 m0, %[sLb], 0\n"
      "v_add_u32 v31, %[sV], v98\n"
      "v_min_u32 v98, v98, v31\n"
      "v_lshl_add_u32 v98, v98, 8, %[gc0]\n"
      "buffer_load_dwordx4 v98, %[srdT], %[s128] offen offset:0 lds\n"
      "v_add_u32 v31, %[sV], v102\n"
      "v_min_u32 v102, v102, v31\n"
      "v_lshl_add_u32 v102, v102, 8, %[gc1]\n"
      "buffer_load_dwordx4 v102, %[srdT], %[s128] offen offset:1024 lds\n"
      "v_add_u32 v31, %[sV], v106\n"
      "v_min_u32 v106, v106, v31\n"
      "v_lshl_add_u32 v106, v106, 8, %[gc2]\n"
      "buffer_load_dwordx4 v106, %[srdT], %[s128] offen offset:2048 lds\n"
      "v_add_u32 v31, %[sV], v110\n"
      "v_min_u32 v110, v110, v31\n"
      "v_lshl_add_u32 v110, v110, 8, %[gc3]\n"
      "buffer_load_dwordx4 v110, %[srdT], %[s128] offen offset:3072 lds\n"
      "s_add_u32 m0, %[sLb], 4096\n"
      "v_add_u32 v31, %[sV], v114\n"
      "v_min_u32 v114, v114, v31\n"
      "v_lshl_add_u32 v114, v114, 8, %[gc0]\n"
      "buffer_load_dwordx4 v114, %[srdT], %[s128] offen offset:0 lds\n"
      "v_add_u32 v31, %[sV], v118\n"
      "v_min_u32 v118, v118, v31\n"
      "v_lshl_add_u32 v118, v118, 8, %[gc1]\n"
      "buffer_load_dwordx4 v118, %[srdT], %[s128] offen offset:1024 lds\n"
      "v_add_u32 v31, %[sV], v122\n"
      "v_min_u32 v122, v122, v31\n"
      "v_lshl_add_u32 v122, v122, 8, %[gc2]\n"
      "buffer_load_dwordx4 v122, %[srdT], %[s128] offen offset:2048 lds\n"
      "v_add_u32 v31, %[sV], v126\n"
      "v_min_u32 v126, v126, v31\n"
      "v_lshl_add_u32 v126, v126, 8, %[gc3]\n"
      "buffer_load_dwordx4 v126, %[srdT], %[s128] offen offset:3072 lds\n"
      "buffer_load_dwordx4 v[68:71], %[vdi], %[srdI], 0 offen offset:896\n"
      "s_waitcnt vmcnt(19)\n"
      "s_barrier\n"
      "s_add_u32 m0, %[sLb], 16384\n"
      "s_nop 0\n"
      "buffer_load_dwordx4 v98, %[srdT], 0 offen offset:0 lds\n"
      "buffer_load_dwordx4 v102, %[srdT], 0 offen offset:1024 lds\n"
      "buffer_load_dwordx4 v106, %[srdT], 0 offen offset:2048 lds\n"
      "buffer_load_dwordx4 v110, %[srdT], 0 offen offset:3072 lds\n"
      "s_add_u32 m0, %[sLb], 20480\n"
      "s_nop 0\n"
      "buffer_load_dwordx4 v114, %[srdT], 0 offen offset:0 lds\n"
      "buffer_load_dwordx4 v118, %[srdT], 0 offen offset:1024 lds\n"
      "buffer_load_dwordx4 v122, %[srdT], 0 offen offset:2048 lds\n"
      "buffer_load_dwordx4 v126, %[srdT], 0 offen offset:3072 lds\n"
      "buffer_load_dwordx4 v[72:75], %[vdi], %[srdI], 0 offen offset:1728\n"
      "s_waitcnt vmcnt(19)\n"
      "s_barrier\n"
      "s_add_u32 m0, %[sLb], 32768\n"
      "v_add_u32 v31, %[sV], v99\n"
      "v_min_u32 v99, v99, v31\n"
      "v_lshl_add_u32 v99, v99, 8, %[gc0]\n"
      "buffer_load_dwordx4 v99, %[srdT], %[s128] offen offset:0 lds\n"
      "v_add_u32 v31, %[sV], v103\n"
      "v_min_u32 v103, v103, v31\n"
      "v_lshl_add_u32 v103, v103, 8, %[gc1]\n"
      "buffer_load_dwordx4 v103, %[srdT], %[s128] offen offset:1024 lds\n"
      "v_add_u32 v31, %[sV], v107\n"
      "v_min_u32 v107, v107, v31\n"
      "v_lshl_add_u32 v107, v107, 8, %[gc2]\n"
      "buffer_load_dwordx4 v107, %[srdT], %[s128] offen offset:2048 lds\n"
      "v_add_u32 v31, %[sV], v111\n"
      "v_min_u32 v111, v111, v31\n"
      "v_lshl_add_u32 v111, v111, 8, %[gc3]\n"
      "buffer_load_dwordx4 v111, %[srdT], %[s128] offen offset:3072 lds\n"
      "s_add_u32 m0, %[sLb], 36864\n"
      "v_add_u32 v31, %[sV], v115\n"
      "v_min_u32 v115, v115, v31\n"
      "v_lshl_add_u32 v115, v115, 8, %[gc0]\n"
      "buffer_load_dwordx4 v115, %[srdT], %[s128] offen offset:0 lds\n"
      "v_add_u32 v31, %[sV], v119\n"
      "v_min_u32 v119, v119, v31\n"
      "v_lshl_add_u32 v119, v119, 8, %[gc1]\n"
      "buffer_load_dwordx4 v119, %[srdT], %[s128] offen offset:1024 lds\n"
      "v_add_u32 v31, %[sV], v123\n"
      "v_min_u32 v123, v123, v31\n"
      "v_lshl_add_u32 v123, v123, 8, %[gc2]\n"
      "buffer_load_dwordx4 v123, %[srdT], %[s128] offen offset:2048 lds\n"
      "v_add_u32 v31, %[sV], v127\n"
      "v_min_u32 v127, v127, v31\n"
      "v_lshl_add_u32 v127, v127, 8, %[gc3]\n"
      "buffer_load_dwordx4 v127, %[srdT], %[s128] offen offset:3072 lds\n"
      "buffer_load_dwordx4 v[76:79], %[vdi], %[srdI], 0 offen offset:2560\n"
      "s_waitcnt vmcnt(19)\n"
      "s_barrier\n"
      "s_add_u32 m0, %[sLb], 49152\n"
      "s_nop 0\n"
      "buffer_load_dwordx4 v99, %[srdT], 0 offen offset:0 lds\n"
      "buffer_load_dwordx4 v103, %[srdT], 0 offen offset:1024 lds\n"
      "buffer_load_dwordx4 v107, %[srdT], 0 offen offset:2048 lds\n"
      "buffer_load_dwordx4 v111, %[srdT], 0 offen offset:3072 lds\n"
      "s_add_u32 m0, %[sLb], 53248\n"
      "s_nop 0\n"
      "buffer_load_dwordx4 v115, %[srdT], 0 offen offset:0 lds\n"
      "buffer_load_dwordx4 v119, %[srdT], 0 offen offset:1024 lds\n"
      "buffer_load_dwordx4 v123, %[srdT], 0 offen offset:2048 lds\n"
      "buffer_load_dwordx4 v127, %[srdT], 0 offen offset:3072 lds\n"
      "buffer_load_dwordx4 v[80:83], %[vdi], %[srdI], 0 offen offset:3392\n"
      "s_waitcnt vmcnt(19)\n"
      "s_barrier\n"
      "s_add_u32 m0, %[sLb], 0\n"
      "v_add_u32 v31, %[sV], v32\n"
      "v_min_u32 v32, v32, v31\n"
      "v_lshl_add_u32 v32, v32, 8, %[gc0]\n"
      "buffer_load_dwordx4 v32, %[srdT], %[s128] offen offset:0 lds\n"
      "v_add_u32 v31, %[sV], v36\n"
      "v_min_u32 v36, v36, v31\n"
      "v_lshl_add_u32 v36, v36, 8, %[gc1]\n"
      "buffer_load_dwordx4 v36, %[srdT], %[s128] offen offset:1024 lds\n"
      "v_add_u32 v31, %[sV], v40\n"
      "v_min_u32 v40, v40, v31\n"
      "v_lshl_add_u32 v40, v40, 8, %[gc2]\n"
      "buffer_load_dwordx4 v40, %[srdT], %[s128] offen offset:2048 lds\n"
      "v_add_u32 v31, %[sV], v44\n"
      "v_min_u32 v44, v44, v31\n"
      "v_lshl_add_u32 v44, v44, 8, %[gc3]\n"
      "buffer_load_dwordx4 v44, %[srdT], %[s128] offen offset:3072 lds\n"
      "s_add_u32 m0, %[sLb], 4096\n"
      "v_add_u32 v31, %[sV], v48\n"
      "v_min_u32 v48, v48, v31\n"
      "v_lshl_add_u32 v48, v48, 8, %[gc0]\n"
      "buffer_load_dwordx4 v48, %[srdT], %[s128] offen offset:0 lds\n"
      "v_add_u32 v31, %[sV], v52\n"
      "v_min_u32 v52, v52, v31\n"
      "v_lshl_add_u32 v52, v52, 8, %[gc1]\n"
      "buffer_load_dwordx4 v52, %[srdT], %[s128] offen offset:1024 lds\n"
      "v_add_u32 v31, %[sV], v56\n"
      "v_min_u32 v56, v56, v31\n"
      "v_lshl_add_u32 v56, v56, 8, %[gc2]\n"
      "buffer_load_dwordx4 v56, %[srdT], %[s128] offen offset:2048 lds\n"
      "v_add_u32 v31, %[sV], v60\n"
      "v_min_u32 v60, v60, v31\n"
      "v_lshl_add_u32 v60, v60, 8, %[gc3]\n"
      "buffer_load_dwordx4 v60, %[srdT], %[s128] offen offset:3072 lds\n"
      "buffer_load_dwordx4 v[84:87], %[vdi], %[srdI], %[s4k] offen offset:896\n"
      "s_waitcnt vmcnt(19)\n"
      "s_barrier\n"
      "s_add_u32 m0, %[sLb], 16384\n"
      "s_nop 0\n"
      "buffer_load_dwordx4 v32, %[srdT], 0 offen offset:0 lds\n"
      "buffer_load_dwordx4 v36, %[srdT], 0 offen offset:1024 lds\n"
      "buffer_load_dwordx4 v40, %[srdT], 0 offen offset:2048 lds\n"
      "buffer_load_dwordx4 v44, %[srdT], 0 offen offset:3072 lds\n"
      "s_add_u32 m0, %[sLb], 20480\n"
      "s_nop 0\n"
      "buffer_load_dwordx4 v48, %[srdT], 0 offen offset:0 lds\n"
      "buffer_load_dwordx4 v52, %[srdT], 0 offen offset:1024 lds\n"
      "buffer_load_dwordx4 v56, %[srdT], 0 offen offset:2048 lds\n"
      "buffer_load_dwordx4 v60, %[srdT], 0 offen offset:3072 lds\n"
      "buffer_load_dwordx4 v[88:91], %[vdi], %[srdI], %[s4k] offen offset:1728\n"
      "s_waitcnt vmcnt(19)\n"
      "s_barrier\n"
      "s_add_u32 m0, %[sLb], 32768\n"
      "v_add_u32 v31, %[sV], v33\n"
      "v_min_u32 v33, v33, v31\n"
      "v_lshl_add_u32 v33, v33, 8, %[gc0]\n"
      "buffer_load_dwordx4 v33, %[srdT], %[s128] offen offset:0 lds\n"
      "v_add_u32 v31, %[sV], v37\n"
      "v_min_u32 v37, v37, v31\n"
      "v_lshl_add_u32 v37, v37, 8, %[gc1]\n"
      "buffer_load_dwordx4 v37, %[srdT], %[s128] offen offset:1024 lds\n"
      "v_add_u32 v31, %[sV], v41\n"
      "v_min_u32 v41, v41, v31\n"
      "v_lshl_add_u32 v41, v41, 8, %[gc2]\n"
      "buffer_load_dwordx4 v41, %[srdT], %[s128] offen offset:2048 lds\n"
      "v_add_u32 v31, %[sV], v45\n"
      "v_min_u32 v45, v45, v31\n"
      "v_lshl_add_u32 v45, v45, 8, %[gc3]\n"
      "buffer_load_dwordx4 v45, %[srdT], %[s128] offen offset:3072 lds\n"
      "s_add_u32 m0, %[sLb], 36864\n"
      "v_add_u32 v31, %[sV], v49\n"
      "v_min_u32 v49, v49, v31\n"
      "v_lshl_add_u32 v49, v49, 8, %[gc0]\n"
      "buffer_load_dwordx4 v49, %[srdT], %[s128] offen offset:0 lds\n"
      "v_add_u32 v31, %[sV], v53\n"
      "v_min_u32 v53, v53, v31\n"
      "v_lshl_add_u32 v53, v53, 8, %[gc1]\n"
      "buffer_load_dwordx4 v53, %[srdT], %[s128] offen offset:1024 lds\n"
      "v_add_u32 v31, %[sV], v57\n"
      "v_min_u32 v57, v57, v31\n"
      "v_lshl_add_u32 v57, v57, 8, %[gc2]\n"
      "buffer_load_dwordx4 v57, %[srdT], %[s128] offen offset:2048 lds\n"
      "v_add_u32 v31, %[sV], v61\n"
      "v_min_u32 v61, v61, v31\n"
      "v_lshl_add_u32 v61, v61, 8, %[gc3]\n"
      "buffer_load_dwordx4 v61, %[srdT], %[s128] offen offset:3072 lds\n"
      "buffer_load_dwordx4 v[92:95], %[vdi], %[srdI], %[s4k] offen offset:2560\n"
      "s_waitcnt vmcnt(19)\n"
      "s_barrier\n"
      "s_add_u32 m0, %[sLb], 49152\n"
      "s_nop 0\n"
      "buffer_load_dwordx4 v33, %[srdT], 0 offen offset:0 lds\n"
      "buffer_load_dwordx4 v37, %[srdT], 0 offen offset:1024 lds\n"
      "buffer_load_dwordx4 v41, %[srdT], 0 offen offset:2048 lds\n"
      "buffer_load_dwordx4 v45, %[srdT], 0 offen offset:3072 lds\n"
      "s_add_u32 m0, %[sLb], 53248\n"
      "s_nop 0\n"
      "buffer_load_dwordx4 v49, %[srdT], 0 offen offset:0 lds\n"
      "buffer_load_dwordx4 v53, %[srdT], 0 offen offset:1024 lds\n"
      "buffer_load_dwordx4 v57, %[srdT], 0 offen offset:2048 lds\n"
      "buffer_load_dwordx4 v61, %[srdT], 0 offen offset:3072 lds\n"
      "buffer_load_dwordx4 v[96:99], %[vdi], %[srdI], 0 offen offset:80\n"
      "s_waitcnt vmcnt(19)\n"
      "s_barrier\n"
      "s_add_u32 m0, %[sLb], 0\n"
      "v_add_u32 v31, %[sV], v34\n"
      "v_min_u32 v34, v34, v31\n"
      "v_lshl_add_u32 v34, v34, 8, %[gc0]\n"
      "buffer_load_dwordx4 v34, %[srdT], %[s128] offen offset:0 lds\n"
      "v_add_u32 v31, %[sV], v38\n"
      "v_min_u32 v38, v38, v31\n"
      "v_lshl_add_u32 v38, v38, 8, %[gc1]\n"
      "buffer_load_dwordx4 v38, %[srdT], %[s128] offen offset:1024 lds\n"
      "v_add_u32 v31, %[sV], v42\n"
      "v_min_u32 v42, v42, v31\n"
      "v_lshl_add_u32 v42, v42, 8, %[gc2]\n"
      "buffer_load_dwordx4 v42, %[srdT], %[s128] offen offset:2048 lds\n"
      "v_add_u32 v31, %[sV], v46\n"
      "v_min_u32 v46, v46, v31\n"
      "v_lshl_add_u32 v46, v46, 8, %[gc3]\n"
      "buffer_load_dwordx4 v46, %[srdT], %[s128] offen offset:3072 lds\n"
      "s_add_u32 m0, %[sLb], 4096\n"
      "v_add_u32 v31, %[sV], v50\n"
      "v_min_u32 v50, v50, v31\n"
      "v_lshl_add_u32 v50, v50, 8, %[gc0]\n"
      "buffer_load_dwordx4 v50, %[srdT], %[s128] offen offset:0 lds\n"
      "v_add_u32 v31, %[sV], v54\n"
      "v_min_u32 v54, v54, v31\n"
      "v_lshl_add_u32 v54, v54, 8, %[gc1]\n"
      "buffer_load_dwordx4 v54, %[srdT], %[s128] offen offset:1024 lds\n"
      "v_add_u32 v31, %[sV], v58\n"
      "v_min_u32 v58, v58, v31\n"
      "v_lshl_add_u32 v58, v58, 8, %[gc2]\n"
      "buffer_load_dwordx4 v58, %[srdT], %[s128] offen offset:2048 lds\n"
      "v_add_u32 v31, %[sV], v62\n"
      "v_min_u32 v62, v62, v31\n"
      "v_lshl_add_u32 v62, v62, 8, %[gc3]\n"
      "buffer_load_dwordx4 v62, %[srdT], %[s128] offen offset:3072 lds\n"
      "buffer_load_dwordx4 v[100:103], %[vdi], %[srdI], 0 offen offset:912\n"
      "s_waitcnt vmcnt(19)\n"
      "s_barrier\n"
      "s_add_u32 m0, %[sLb], 16384\n"
      "s_nop 0\n"
      "buffer_load_dwordx4 v34, %[srdT], 0 offen offset:0 lds\n"
      "buffer_load_dwordx4 v38, %[srdT], 0 offen offset:1024 lds\n"
      "buffer_load_dwordx4 v42, %[srdT], 0 offen offset:2048 lds\n"
      "buffer_load_dwordx4 v46, %[srdT], 0 offen offset:3072 lds\n"
      "s_add_u32 m0, %[sLb], 20480\n"
      "s_nop 0\n"
      "buffer_load_dwordx4 v50, %[srdT], 0 offen offset:0 lds\n"
      "buffer_load_dwordx4 v54, %[srdT], 0 offen offset:1024 lds\n"
      "buffer_load_dwordx4 v58, %[srdT], 0 offen offset:2048 lds\n"
      "buffer_load_dwordx4 v62, %[srdT], 0 offen offset:3072 lds\n"
      "buffer_load_dwordx4 v[104:107], %[vdi], %[srdI], 0 offen offset:1744\n"
      "s_waitcnt vmcnt(19)\n"
      "s_barrier\n"
      "s_add_u32 m0, %[sLb], 32768\n"
      "v_add_u32 v31, %[sV], v35\n"
      "v_min_u32 v35, v35, v31\n"
      "v_lshl_add_u32 v35, v35, 8, %[gc0]\n"
      "buffer_load_dwordx4 v35, %[srdT], %[s128] offen offset:0 lds\n"
      "v_add_u32 v31, %[sV], v39\n"
      "v_min_u32 v39, v39, v31\n"
      "v_lshl_add_u32 v39, v39, 8, %[gc1]\n"
      "buffer_load_dwordx4 v39, %[srdT], %[s128] offen offset:1024 lds\n"
      "v_add_u32 v31, %[sV], v43\n"
      "v_min_u32 v43, v43, v31\n"
      "v_lshl_add_u32 v43, v43, 8, %[gc2]\n"
      "buffer_load_dwordx4 v43, %[srdT], %[s128] offen offset:2048 lds\n"
      "v_add_u32 v31, %[sV], v47\n"
      "v_min_u32 v47, v47, v31\n"
      "v_lshl_add_u32 v47, v47, 8, %[gc3]\n"
      "buffer_load_dwordx4 v47, %[srdT], %[s128] offen offset:3072 lds\n"
      "s_add_u32 m0, %[sLb], 36864\n"
      "v_add_u32 v31, %[sV], v51\n"
      "v_min_u32 v51, v51, v31\n"
      "v_lshl_add_u32 v51, v51, 8, %[gc0]\n"
      "buffer_load_dwordx4 v51, %[srdT], %[s128] offen offset:0 lds\n"
      "v_add_u32 v31, %[sV], v55\n"
      "v_min_u32 v55, v55, v31\n"
      "v_lshl_add_u32 v55, v55, 8, %[gc1]\n"
      "buffer_load_dwordx4 v55, %[srdT], %[s128] offen offset:1024 lds\n"
      "v_add_u32 v31, %[sV], v59\n"
      "v_min_u32 v59, v59, v31\n"
      "v_lshl_add_u32 v59, v59, 8, %[gc2]\n"
      "buffer_load_dwordx4 v59, %[srdT], %[s128] offen offset:2048 lds\n"
      "v_add_u32 v31, %[sV], v63\n"
      "v_min_u32 v63, v63, v31\n"
      "v_lshl_add_u32 v63, v63, 8, %[gc3]\n"
      "buffer_load_dwordx4 v63, %[srdT], %[s128] offen offset:3072 lds\n"
      "buffer_load_dwordx4 v[108:111], %[vdi], %[srdI], 0 offen offset:2576\n"
      "s_waitcnt vmcnt(19)\n"
      "s_barrier\n"
      "s_add_u32 m0, %[sLb], 49152\n"
      "s_nop 0\n"
      "buffer_load_dwordx4 v35, %[srdT], 0 offen offset:0 lds\n"
      "buffer_load_dwordx4 v39, %[srdT], 0 offen offset:1024 lds\n"
      "buffer_load_dwordx4 v43, %[srdT], 0 offen offset:2048 lds\n"
      "buffer_load_dwordx4 v47, %[srdT], 0 offen offset:3072 lds\n"
      "s_add_u32 m0, %[sLb], 53248\n"
      "s_nop 0\n"
      "buffer_load_dwordx4 v51, %[srdT], 0 offen offset:0 lds\n"
      "buffer_load_dwordx4 v55, %[srdT], 0 offen offset:1024 lds\n"
      "buffer_load_dwordx4 v59, %[srdT], 0 offen offset:2048 lds\n"
      "buffer_load_dwordx4 v63, %[srdT], 0 offen offset:3072 lds\n"
      "buffer_load_dwordx4 v[112:115], %[vdi], %[srdI], 0 offen offset:3408\n"
      "s_waitcnt vmcnt(19)\n"
      "s_barrier\n"
      "s_add_u32 m0, %[sLb], 0\n"
      "v_add_u32 v31, %[sV], v64\n"
      "v_min_u32 v64, v64, v31\n"
      "v_lshl_add_u32 v64, v64, 8, %[gc0]\n"
      "buffer_load_dwordx4 v64, %[srdT], %[s128] offen offset:0 lds\n"
      "v_add_u32 v31, %[sV], v68\n"
      "v_min_u32 v68, v68, v31\n"
      "v_lshl_add_u32 v68, v68, 8, %[gc1]\n"
      "buffer_load_dwordx4 v68, %[srdT], %[s128] offen offset:1024 lds\n"
      "v_add_u32 v31, %[sV], v72\n"
      "v_min_u32 v72, v72, v31\n"
      "v_lshl_add_u32 v72, v72, 8, %[gc2]\n"
      "buffer_load_dwordx4 v72, %[srdT], %[s128] offen offset:2048 lds\n"
      "v_add_u32 v31, %[sV], v76\n"
      "v_min_u32 v76, v76, v31\n"
      "v_lshl_add_u32 v76, v76, 8, %[gc3]\n"
      "buffer_load_dwordx4 v76, %[srdT], %[s128] offen offset:3072 lds\n"
      "s_add_u32 m0, %[sLb], 4096\n"
      "v_add_u32 v31, %[sV], v80\n"
      "v_min_u32 v80, v80, v31\n"
      "v_lshl_add_u32 v80, v80, 8, %[gc0]\n"
      "buffer_load_dwordx4 v80, %[srdT], %[s128] offen offset:0 lds\n"
      "v_add_u32 v31, %[sV], v84\n"
      "v_min_u32 v84, v84, v31\n"
      "v_lshl_add_u32 v84, v84, 8, %[gc1]\n"
      "buffer_load_dwordx4 v84, %[srdT], %[s128] offen offset:1024 lds\n"
      "v_add_u32 v31, %[sV], v88\n"
      "v_min_u32 v88, v88, v31\n"
      "v_lshl_add_u32 v88, v88, 8, %[gc2]\n"
      "buffer_load_dwordx4 v88, %[srdT], %[s128] offen offset:2048 lds\n"
      "v_add_u32 v31, %[sV], v92\n"
      "v_min_u32 v92, v92, v31\n"
      "v_lshl_add_u32 v92, v92, 8, %[gc3]\n"
      "buffer_load_dwordx4 v92, %[srdT], %[s128] offen offset:3072 lds\n"
      "buffer_load_dwordx4 v[116:119], %[vdi], %[srdI], %[s4k] offen offset:912\n"
      "s_waitcnt vmcnt(19)\n"
      "s_barrier\n"
      "s_add_u32 m0, %[sLb], 16384\n"
      "s_nop 0\n"
      "buffer_load_dwordx4 v64, %[srdT], 0 offen offset:0 lds\n"
      "buffer_load_dwordx4 v68, %[srdT], 0 offen offset:1024 lds\n"
      "buffer_load_dwordx4 v72, %[srdT], 0 offen offset:2048 lds\n"
      "buffer_load_dwordx4 v76, %[srdT], 0 offen offset:3072 lds\n"
      "s_add_u32 m0, %[sLb], 20480\n"
      "s_nop 0\n"
      "buffer_load_dwordx4 v80, %[srdT], 0 offen offset:0 lds\n"
      "buffer_load_dwordx4 v84, %[srdT], 0 offen offset:1024 lds\n"
      "buffer_load_dwordx4 v88, %[srdT], 0 offen offset:2048 lds\n"
      "buffer_load_dwordx4 v92, %[srdT], 0 offen offset:3072 lds\n"
      "buffer_load_dwordx4 v[120:123], %[vdi], %[srdI], %[s4k] offen offset:1744\n"
      "s_waitcnt vmcnt(19)\n"
      "s_barrier\n"
      "s_add_u32 m0, %[sLb], 32768\n"
      "v_add_u32 v31, %[sV], v65\n"
      "v_min_u32 v65, v65, v31\n"
      "v_lshl_add_u32 v65, v65, 8, %[gc0]\n"
      "buffer_load_dwordx4 v65, %[srdT], %[s128] offen offset:0 lds\n"
      "v_add_u32 v31, %[sV], v69\n"
      "v_min_u32 v69, v69, v31\n"
      "v_lshl_add_u32 v69, v69, 8, %[gc1]\n"
      "buffer_load_dwordx4 v69, %[srdT], %[s128] offen offset:1024 lds\n"
      "v_add_u32 v31, %[sV], v73\n"
      "v_min_u32 v73, v73, v31\n"
      "v_lshl_add_u32 v73, v73, 8, %[gc2]\n"
      "buffer_load_dwordx4 v73, %[srdT], %[s128] offen offset:2048 lds\n"
      "v_add_u32 v31, %[sV], v77\n"
      "v_min_u32 v77, v77, v31\n"
      "v_lshl_add_u32 v77, v77, 8, %[gc3]\n"
      "buffer_load_dwordx4 v77, %[srdT], %[s128] offen offset:3072 lds\n"
      "s_add_u32 m0, %[sLb], 36864\n"
      "v_add_u32 v31, %[sV], v81\n"
      "v_min_u32 v81, v81, v31\n"
      "v_lshl_add_u32 v81, v81, 8, %[gc0]\n"
      "buffer_load_dwordx4 v81, %[srdT], %[s128] offen offset:0 lds\n"
      "v_add_u32 v31, %[sV], v85\n"
      "v_min_u32 v85, v85, v31\n"
      "v_lshl_add_u32 v85, v85, 8, %[gc1]\n"
      "buffer_load_dwordx4 v85, %[srdT], %[s128] offen offset:1024 lds\n"
      "v_add_u32 v31, %[sV], v89\n"
      "v_min_u32 v89, v89, v31\n"
      "v_lshl_add_u32 v89, v89, 8, %[gc2]\n"
      "buffer_load_dwordx4 v89, %[srdT], %[s128] offen offset:2048 lds\n"
      "v_add_u32 v31, %[sV], v93\n"
      "v_min_u32 v93, v93, v31\n"
      "v_lshl_add_u32 v93, v93, 8, %[gc3]\n"
      "buffer_load_dwordx4 v93, %[srdT], %[s128] offen offset:3072 lds\n"
      "buffer_load_dwordx4 v[124:127], %[vdi], %[srdI], %[s4k] offen offset:2576\n"
      "s_waitcnt vmcnt(19)\n"
      "s_barrier\n"
      "s_add_u32 m0, %[sLb], 49152\n"
      "s_nop 0\n"
      "buffer_load_dwordx4 v65, %[srdT], 0 offen offset:0 lds\n"
      "buffer_load_dwordx4 v69, %[srdT], 0 offen offset:1024 lds\n"
      "buffer_load_dwordx4 v73, %[srdT], 0 offen offset:2048 lds\n"
      "buffer_load_dwordx4 v77, %[srdT], 0 offen offset:3072 lds\n"
      "s_add_u32 m0, %[sLb], 53248\n"
      "s_nop 0\n"
      "buffer_load_dwordx4 v81, %[srdT], 0 offen offset:0 lds\n"
      "buffer_load_dwordx4 v85, %[srdT], 0 offen offset:1024 lds\n"
      "buffer_load_dwordx4 v89, %[srdT], 0 offen offset:2048 lds\n"
      "buffer_load_dwordx4 v93, %[srdT], 0 offen offset:3072 lds\n"
      "buffer_load_dwordx4 v[32:35], %[vdi], %[srdI], 0 offen offset:96\n"
      "s_waitcnt vmcnt(19)\n"
      "s_barrier\n"
      "s_add_u32 m0, %[sLb], 0\n"
      "v_add_u32 v31, %[sV], v66\n"
      "v_min_u32 v66, v66, v31\n"
      "v_lshl_add_u32 v66, v66, 8, %[gc0]\n"
      "buffer_load_dwordx4 v66, %[srdT], %[s128] offen offset:0 lds\n"
      "v_add_u32 v31, %[sV], v70\n"
      "v_min_u32 v70, v70, v31\n"
      "v_lshl_add_u32 v70, v70, 8, %[gc1]\n"
      "buffer_load_dwordx4 v70, %[srdT], %[s128] offen offset:1024 lds\n"
      "v_add_u32 v31, %[sV], v74\n"
      "v_min_u32 v74, v74, v31\n"
      "v_lshl_add_u32 v74, v74, 8, %[gc2]\n"
      "buffer_load_dwordx4 v74, %[srdT], %[s128] offen offset:2048 lds\n"
      "v_add_u32 v31, %[sV], v78\n"
      "v_min_u32 v78, v78, v31\n"
      "v_lshl_add_u32 v78, v78, 8, %[gc3]\n"
      "buffer_load_dwordx4 v78, %[srdT], %[s128] offen offset:3072 lds\n"
      "s_add_u32 m0, %[sLb], 4096\n"
      "v_add_u32 v31, %[sV], v82\n"
      "v_min_u32 v82, v82, v31\n"
      "v_lshl_add_u32 v82, v82, 8, %[gc0]\n"
      "buffer_load_dwordx4 v82, %[srdT], %[s128] offen offset:0 lds\n"
      "v_add_u32 v31, %[sV], v86\n"
      "v_min_u32 v86, v86, v31\n"
      "v_lshl_add_u32 v86, v86, 8, %[gc1]\n"
      "buffer_load_dwordx4 v86, %[srdT], %[s128] offen offset:1024 lds\n"
      "v_add_u32 v31, %[sV], v90\n"
      "v_min_u32 v90, v90, v31\n"
      "v_lshl_add_u32 v90, v90, 8, %[gc2]\n"
      "buffer_load_dwordx4 v90, %[srdT], %[s128] offen offset:2048 lds\n"
      "v_add_u32 v31, %[sV], v94\n"
      "v_min_u32 v94, v94, v31\n"
      "v_lshl_add_u32 v94, v94, 8, %[gc3]\n"
      "buffer_load_dwordx4 v94, %[srdT], %[s128] offen offset:3072 lds\n"
      "buffer_load_dwordx4 v[36:39], %[vdi], %[srdI], 0 offen offset:928\n"
      "s_waitcnt vmcnt(19)\n"
      "s_barrier\n"
      "s_add_u32 m0, %[sLb], 16384\n"
      "s_nop 0\n"
      "buffer_load_dwordx4 v66, %[srdT], 0 offen offset:0 lds\n"
      "buffer_load_dwordx4 v70, %[srdT], 0 offen offset:1024 lds\n"
      "buffer_load_dwordx4 v74, %[srdT], 0 offen offset:2048 lds\n"
      "buffer_load_dwordx4 v78, %[srdT], 0 offen offset:3072 lds\n"
      "s_add_u32 m0, %[sLb], 20480\n"
      "s_nop 0\n"
      "buffer_load_dwordx4 v82, %[srdT], 0 offen offset:0 lds\n"
      "buffer_load_dwordx4 v86, %[srdT], 0 offen offset:1024 lds\n"
      "buffer_load_dwordx4 v90, %[srdT], 0 offen offset:2048 lds\n"
      "buffer_load_dwordx4 v94, %[srdT], 0 offen offset:3072 lds\n"
      "buffer_load_dwordx4 v[40:43], %[vdi], %[srdI], 0 offen offset:1760\n"
      "s_waitcnt vmcnt(19)\n"
      "s_barrier\n"
      "s_add_u32 m0, %[sLb], 32768\n"
      "v_add_u32 v31, %[sV], v67\n"
      "v_min_u32 v67, v67, v31\n"
      "v_lshl_add_u32 v67, v67, 8, %[gc0]\n"
      "buffer_load_dwordx4 v67, %[srdT], %[s128] offen offset:0 lds\n"
      "v_add_u32 v31, %[sV], v71\n"
      "v_min_u32 v71, v71, v31\n"
      "v_lshl_add_u32 v71, v71, 8, %[gc1]\n"
      "buffer_load_dwordx4 v71, %[srdT], %[s128] offen offset:1024 lds\n"
      "v_add_u32 v31, %[sV], v75\n"
      "v_min_u32 v75, v75, v31\n"
      "v_lshl_add_u32 v75, v75, 8, %[gc2]\n"
      "buffer_load_dwordx4 v75, %[srdT], %[s128] offen offset:2048 lds\n"
      "v_add_u32 v31, %[sV], v79\n"
      "v_min_u32 v79, v79, v31\n"
      "v_lshl_add_u32 v79, v79, 8, %[gc3]\n"
      "buffer_load_dwordx4 v79, %[srdT], %[s128] offen offset:3072 lds\n"
      "s_add_u32 m0, %[sLb], 36864\n"
      "v_add_u32 v31, %[sV], v83\n"
      "v_min_u32 v83, v83, v31\n"
      "v_lshl_add_u32 v83, v83, 8, %[gc0]\n"
      "buffer_load_dwordx4 v83, %[srdT], %[s128] offen offset:0 lds\n"
      "v_add_u32 v31, %[sV], v87\n"
      "v_min_u32 v87, v87, v31\n"
      "v_lshl_add_u32 v87, v87, 8, %[gc1]\n"
      "buffer_load_dwordx4 v87, %[srdT], %[s128] offen offset:1024 lds\n"
      "v_add_u32 v31, %[sV], v91\n"
      "v_min_u32 v91, v91, v31\n"
      "v_lshl_add_u32 v91, v91, 8, %[gc2]\n"
      "buffer_load_dwordx4 v91, %[srdT], %[s128] offen offset:2048 lds\n"
      "v_add_u32 v31, %[sV], v95\n"
      "v_min_u32 v95, v95, v31\n"
      "v_lshl_add_u32 v95, v95, 8, %[gc3]\n"
      "buffer_load_dwordx4 v95, %[srdT], %[s128] offen offset:3072 lds\n"
      "buffer_load_dwordx4 v[44:47], %[vdi], %[srdI], 0 offen offset:2592\n"
      "s_waitcnt vmcnt(19)\n"
      "s_barrier\n"
      "s_add_u32 m0, %[sLb], 49152\n"
      "s_nop 0\n"
      "buffer_load_dwordx4 v67, %[srdT], 0 offen offset:0 lds\n"
      "buffer_load_dwordx4 v71, %[srdT], 0 offen offset:1024 lds\n"
      "buffer_load_dwordx4 v75, %[srdT], 0 offen offset:2048 lds\n"
      "buffer_load_dwordx4 v79, %[srdT], 0 offen offset:3072 lds\n"
      "s_add_u32 m0, %[sLb], 53248\n"
      "s_nop 0\n"
      "buffer_load_dwordx4 v83, %[srdT], 0 offen offset:0 lds\n"
      "buffer_load_dwordx4 v87, %[srdT], 0 offen offset:1024 lds\n"
      "buffer_load_dwordx4 v91, %[srdT], 0 offen offset:2048 lds\n"
      "buffer_load_dwordx4 v95, %[srdT], 0 offen offset:3072 lds\n"
      "buffer_load_dwordx4 v[48:51], %[vdi], %[srdI], 0 offen offset:3424\n"
      "s_waitcnt vmcnt(19)\n"
      "s_barrier\n"
      "s_add_u32 m0, %[sLb], 0\n"
      "v_add_u32 v31, %[sV], v96\n"
      "v_min_u32 v96, v96, v31\n"
      "v_lshl_add_u32 v96, v96, 8, %[gc0]\n"
      "buffer_load_dwordx4 v96, %[srdT], %[s128] offen offset:0 lds\n"
      "v_add_u32 v31, %[sV], v100\n"
      "v_min_u32 v100, v100, v31\n"
      "v_lshl_add_u32 v100, v100, 8, %[gc1]\n"
      "buffer_load_dwordx4 v100, %[srdT], %[s128] offen offset:1024 lds\n"
      "v_add_u32 v31, %[sV], v104\n"
      "v_min_u32 v104, v104, v31\n"
      "v_lshl_add_u32 v104, v104, 8, %[gc2]\n"
      "buffer_load_dwordx4 v104, %[srdT], %[s128] offen offset:2048 lds\n"
      "v_add_u32 v31, %[sV], v108\n"
      "v_min_u32 v108, v108, v31\n"
      "v_lshl_add_u32 v108, v108, 8, %[gc3]\n"
      "buffer_load_dwordx4 v108, %[srdT], %[s128] offen offset:3072 lds\n"
      "s_add_u32 m0, %[sLb], 4096\n"
      "v_add_u32 v31, %[sV], v112\n"
      "v_min_u32 v112, v112, v31\n"
      "v_lshl_add_u32 v112, v112, 8, %[gc0]\n"
      "buffer_load_dwordx4 v112, %[srdT], %[s128] offen offset:0 lds\n"
      "v_add_u32 v31, %[sV], v116\n"
      "v_min_u32 v116, v116, v31\n"
      "v_lshl_add_u32 v116, v116, 8, %[gc1]\n"
      "buffer_load_dwordx4 v116, %[srdT], %[s128] offen offset:1024 lds\n"
      "v_add_u32 v31, %[sV], v120\n"
      "v_min_u32 v120, v120, v31\n"
      "v_lshl_add_u32 v120, v120, 8, %[gc2]\n"
      "buffer_load_dwordx4 v120, %[srdT], %[s128] offen offset:2048 lds\n"
      "v_add_u32 v31, %[sV], v124\n"
      "v_min_u32 v124, v124, v31\n"
      "v_lshl_add_u32 v124, v124, 8, %[gc3]\n"
      "buffer_load_dwordx4 v124, %[srdT], %[s128] offen offset:3072 lds\n"
      "buffer_load_dwordx4 v[52:55], %[vdi], %[srdI], %[s4k] offen offset:928\n"
      "s_waitcnt vmcnt(19)\n"
      "s_barrier\n"
      "s_add_u32 m0, %[sLb], 16384\n"
      "s_nop 0\n"
      "buffer_load_dwordx4 v96, %[srdT], 0 offen offset:0 lds\n"
      "buffer_load_dwordx4 v100, %[srdT], 0 offen offset:1024 lds\n"
      "buffer_load_dwordx4 v104, %[srdT], 0 offen offset:2048 lds\n"
      "buffer_load_dwordx4 v108, %[srdT], 0 offen offset:3072 lds\n"
      "s_add_u32 m0, %[sLb], 20480\n"
      "s_nop 0\n"
      "buffer_load_dwordx4 v112, %[srdT], 0 offen offset:0 lds\n"
      "buffer_load_dwordx4 v116, %[srdT], 0 offen offset:1024 lds\n"
      "buffer_load_dwordx4 v120, %[srdT], 0 offen offset:2048 lds\n"
      "buffer_load_dwordx4 v124, %[srdT], 0 offen offset:3072 lds\n"
      "buffer_load_dwordx4 v[56:59], %[vdi], %[srdI], %[s4k] offen offset:1760\n"
      "s_waitcnt vmcnt(19)\n"
      "s_barrier\n"
      "s_add_u32 m0, %[sLb], 32768\n"
      "v_add_u32 v31, %[sV], v97\n"
      "v_min_u32 v97, v97, v31\n"
      "v_lshl_add_u32 v97, v97, 8, %[gc0]\n"
      "buffer_load_dwordx4 v97, %[srdT], %[s128] offen offset:0 lds\n"
      "v_add_u32 v31, %[sV], v101\n"
      "v_min_u32 v101, v101, v31\n"
      "v_lshl_add_u32 v101, v101, 8, %[gc1]\n"
      "buffer_load_dwordx4 v101, %[srdT], %[s128] offen offset:1024 lds\n"
      "v_add_u32 v31, %[sV], v105\n"
      "v_min_u32 v105, v105, v31\n"
      "v_lshl_add_u32 v105, v105, 8, %[gc2]\n"
      "buffer_load_dwordx4 v105, %[srdT], %[s128] offen offset:2048 lds\n"
      "v_add_u32 v31, %[sV], v109\n"
      "v_min_u32 v109, v109, v31\n"
      "v_lshl_add_u32 v109, v109, 8, %[gc3]\n"
      "buffer_load_dwordx4 v109, %[srdT], %[s128] offen offset:3072 lds\n"
      "s_add_u32 m0, %[sLb], 36864\n"
      "v_add_u32 v31, %[sV], v113\n"
      "v_min_u32 v113, v113, v31\n"
      "v_lshl_add_u32 v113, v113, 8, %[gc0]\n"
      "buffer_load_dwordx4 v113, %[srdT], %[s128] offen offset:0 lds\n"
      "v_add_u32 v31, %[sV], v117\n"
      "v_min_u32 v117, v117, v31\n"
      "v_lshl_add_u32 v117, v117, 8, %[gc1]\n"
      "buffer_load_dwordx4 v117, %[srdT], %[s128] offen offset:1024 lds\n"
      "v_add_u32 v31, %[sV], v121\n"
      "v_min_u32 v121, v121, v31\n"
      "v_lshl_add_u32 v121, v121, 8, %[gc2]\n"
      "buffer_load_dwordx4 v121, %[srdT], %[s128] offen offset:2048 lds\n"
      "v_add_u32 v31, %[sV], v125\n"
      "v_min_u32 v125, v125, v31\n"
      "v_lshl_add_u32 v125, v125, 8, %[gc3]\n"
      "buffer_load_dwordx4 v125, %[srdT], %[s128] offen offset:3072 lds\n"
      "buffer_load_dwordx4 v[60:63], %[vdi], %[srdI], %[s4k] offen offset:2592\n"
      "s_waitcnt vmcnt(19)\n"
      "s_barrier\n"
      "s_add_u32 m0, %[sLb], 49152\n"
      "s_nop 0\n"
      "buffer_load_dwordx4 v97, %[srdT], 0 offen offset:0 lds\n"
      "buffer_load_dwordx4 v101, %[srdT], 0 offen offset:1024 lds\n"
      "buffer_load_dwordx4 v105, %[srdT], 0 offen offset:2048 lds\n"
      "buffer_load_dwordx4 v109, %[srdT], 0 offen offset:3072 lds\n"
      "s_add_u32 m0, %[sLb], 53248\n"
      "s_nop 0\n"
      "buffer_load_dwordx4 v113, %[srdT], 0 offen offset:0 lds\n"
      "buffer_load_dwordx4 v117, %[srdT], 0 offen offset:1024 lds\n"
      "buffer_load_dwordx4 v121, %[srdT], 0 offen offset:2048 lds\n"
      "buffer_load_dwordx4 v125, %[srdT], 0 offen offset:3072 lds\n"
      "s_waitcnt vmcnt(18)\n"
      "s_barrier\n"
      "s_add_u32 m0, %[sLb], 0\n"
      "v_add_u32 v31, %[sV], v98\n"
      "v_min_u32 v98, v98, v31\n"
      "v_lshl_add_u32 v98, v98, 8, %[gc0]\n"
      "buffer_load_dwordx4 v98, %[srdT], %[s128] offen offset:0 lds\n"
      "v_add_u32 v31, %[sV], v102\n"
      "v_min_u32 v102, v102, v31\n"
      "v_lshl_add_u32 v102, v102, 8, %[gc1]\n"
      "buffer_load_dwordx4 v102, %[srdT], %[s128] offen offset:1024 lds\n"
      "v_add_u32 v31, %[sV], v106\n"
      "v_min_u32 v106, v106, v31\n"
      "v_lshl_add_u32 v106, v106, 8, %[gc2]\n"
      "buffer_load_dwordx4 v106, %[srdT], %[s128] offen offset:2048 lds\n"
      "v_add_u32 v31, %[sV], v110\n"
      "v_min_u32 v110, v110, v31\n"
      "v_lshl_add_u32 v110, v110, 8, %[gc3]\n"
      "buffer_load_dwordx4 v110, %[srdT], %[s128] offen offset:3072 lds\n"
      "s_add_u32 m0, %[sLb], 4096\n"
      "v_add_u32 v31, %[sV], v114\n"
      "v_min_u32 v114, v114, v31\n"
      "v_lshl_add_u32 v114, v114, 8, %[gc0]\n"
      "buffer_load_dwordx4 v114, %[srdT], %[s128] offen offset:0 lds\n"
      "v_add_u32 v31, %[sV], v118\n"
      "v_min_u32 v118, v118, v31\n"
      "v_lshl_add_u32 v118, v118, 8, %[gc1]\n"
      "buffer_load_dwordx4 v118, %[srdT], %[s128] offen offset:1024 lds\n"
      "v_add_u32 v31, %[sV], v122\n"
      "v_min_u32 v122, v122, v31\n"
      "v_lshl_add_u32 v122, v122, 8, %[gc2]\n"
      "buffer_load_dwordx4 v122, %[srdT], %[s128] offen offset:2048 lds\n"
      "v_add_u32 v31, %[sV], v126\n"
      "v_min_u32 v126, v126, v31\n"
      "v_lshl_add_u32 v126, v126, 8, %[gc3]\n"
      "buffer_load_dwordx4 v126, %[srdT], %[s128] offen offset:3072 lds\n"
      "s_waitcnt vmcnt(17)\n"
      "s_barrier\n"
      "s_add_u32 m0, %[sLb], 16384\n"
      "s_nop 0\n"
      "buffer_load_dwordx4 v98, %[srdT], 0 offen offset:0 lds\n"
      "buffer_load_dwordx4 v102, %[srdT], 0 offen offset:1024 lds\n"
      "buffer_load_dwordx4 v106, %[srdT], 0 offen offset:2048 lds\n"
      "buffer_load_dwordx4 v110, %[srdT], 0 offen offset:3072 lds\n"
      "s_add_u32 m0, %[sLb], 20480\n"
      "s_nop 0\n"
      "buffer_load_dwordx4 v114, %[srdT], 0 offen offset:0 lds\n"
      "buffer_load_dwordx4 v118, %[srdT], 0 offen offset:1024 lds\n"
      "buffer_load_dwordx4 v122, %[srdT], 0 offen offset:2048 lds\n"
      "buffer_load_dwordx4 v126, %[srdT], 0 offen offset:3072 lds\n"
      "s_waitcnt vmcnt(16)\n"
      "s_barrier\n"
      "s_add_u32 m0, %[sLb], 32768\n"
      "v_add_u32 v31, %[sV], v99\n"
      "v_min_u32 v99, v99, v31\n"
      "v_lshl_add_u32 v99, v99, 8, %[gc0]\n"
      "buffer_load_dwordx4 v99, %[srdT], %[s128] offen offset:0 lds\n"
      "v_add_u32 v31, %[sV], v103\n"
      "v_min_u32 v103, v103, v31\n"
      "v_lshl_add_u32 v103, v103, 8, %[gc1]\n"
      "buffer_load_dwordx4 v103, %[srdT], %[s128] offen offset:1024 lds\n"
      "v_add_u32 v31, %[sV], v107\n"
      "v_min_u32 v107, v107, v31\n"
      "v_lshl_add_u32 v107, v107, 8, %[gc2]\n"
      "buffer_load_dwordx4 v107, %[srdT], %[s128] offen offset:2048 lds\n"
      "v_add_u32 v31, %[sV], v111\n"
      "v_min_u32 v111, v111, v31\n"
      "v_lshl_add_u32 v111, v111, 8, %[gc3]\n"
      "buffer_load_dwordx4 v111, %[srdT], %[s128] offen offset:3072 lds\n"
      "s_add_u32 m0, %[sLb], 36864\n"
      "v_add_u32 v31, %[sV], v115\n"
      "v_min_u32 v115, v115, v31\n"
      "v_lshl_add_u32 v115, v115, 8, %[gc0]\n"
      "buffer_load_dwordx4 v115, %[srdT], %[s128] offen offset:0 lds\n"
      "v_add_u32 v31, %[sV], v119\n"
      "v_min_u32 v119, v119, v31\n"
      "v_lshl_add_u32 v119, v119, 8, %[gc1]\n"
      "buffer_load_dwordx4 v119, %[srdT], %[s128] offen offset:1024 lds\n"
      "v_add_u32 v31, %[sV], v123\n"
      "v_min_u32 v123, v123, v31\n"
      "v_lshl_add_u32 v123, v123, 8, %[gc2]\n"
      "buffer_load_dwordx4 v123, %[srdT], %[s128] offen offset:2048 lds\n"
      "v_add_u32 v31, %[sV], v127\n"
      "v_min_u32 v127, v127, v31\n"
      "v_lshl_add_u32 v127, v127, 8, %[gc3]\n"
      "buffer_load_dwordx4 v127, %[srdT], %[s128] offen offset:3072 lds\n"
      "s_waitcnt vmcnt(16)\n"
      "s_barrier\n"
      "s_add_u32 m0, %[sLb], 49152\n"
      "s_nop 0\n"
      "buffer_load_dwordx4 v99, %[srdT], 0 offen offset:0 lds\n"
      "buffer_load_dwordx4 v103, %[srdT], 0 offen offset:1024 lds\n"
      "buffer_load_dwordx4 v107, %[srdT], 0 offen offset:2048 lds\n"
      "buffer_load_dwordx4 v111, %[srdT], 0 offen offset:3072 lds\n"
      "s_add_u32 m0, %[sLb], 53248\n"
      "s_nop 0\n"
      "buffer_load_dwordx4 v115, %[srdT], 0 offen offset:0 lds\n"
      "buffer_load_dwordx4 v119, %[srdT], 0 offen offset:1024 lds\n"
      "buffer_load_dwordx4 v123, %[srdT], 0 offen offset:2048 lds\n"
      "buffer_load_dwordx4 v127, %[srdT], 0 offen offset:3072 lds\n"
      "s_waitcnt vmcnt(16)\n"
      "s_barrier\n"
      "s_add_u32 m0, %[sLb], 0\n"
      "v_add_u32 v31, %[sV], v32\n"
      "v_min_u32 v32, v32, v31\n"
      "v_lshl_add_u32 v32, v32, 8, %[gc0]\n"
      "buffer_load_dwordx4 v32, %[srdT], %[s128] offen offset:0 lds\n"
      "v_add_u32 v31, %[sV], v36\n"
      "v_min_u32 v36, v36, v31\n"
      "v_lshl_add_u32 v36, v36, 8, %[gc1]\n"
      "buffer_load_dwordx4 v36, %[srdT], %[s128] offen offset:1024 lds\n"
      "v_add_u32 v31, %[sV], v40\n"
      "v_min_u32 v40, v40, v31\n"
      "v_lshl_add_u32 v40, v40, 8, %[gc2]\n"
      "buffer_load_dwordx4 v40, %[srdT], %[s128] offen offset:2048 lds\n"
      "v_add_u32 v31, %[sV], v44\n"
      "v_min_u32 v44, v44, v31\n"
      "v_lshl_add_u32 v44, v44, 8, %[gc3]\n"
      "buffer_load_dwordx4 v44, %[srdT], %[s128] offen offset:3072 lds\n"
      "s_add_u32 m0, %[sLb], 4096\n"
      "v_add_u32 v31, %[sV], v48\n"
      "v_min_u32 v48, v48, v31\n"
      "v_lshl_add_u32 v48, v48, 8, %[gc0]\n"
      "buffer_load_dwordx4 v48, %[srdT], %[s128] offen offset:0 lds\n"
      "v_add_u32 v31, %[sV], v52\n"
      "v_min_u32 v52, v52, v31\n"
      "v_lshl_add_u32 v52, v52, 8, %[gc1]\n"
      "buffer_load_dwordx4 v52, %[srdT], %[s128] offen offset:1024 lds\n"
      "v_add_u32 v31, %[sV], v56\n"
      "v_min_u32 v56, v56, v31\n"
      "v_lshl_add_u32 v56, v56, 8, %[gc2]\n"
      "buffer_load_dwordx4 v56, %[srdT], %[s128] offen offset:2048 lds\n"
      "v_add_u32 v31, %[sV], v60\n"
      "v_min_u32 v60, v60, v31\n"
      "v_lshl_add_u32 v60, v60, 8, %[gc3]\n"
      "buffer_load_dwordx4 v60, %[srdT], %[s128] offen offset:3072 lds\n"
      "s_waitcnt vmcnt(16)\n"
      "s_barrier\n"
      "s_add_u32 m0, %[sLb], 16384\n"
      "s_nop 0\n"
      "buffer_load_dwordx4 v32, %[srdT], 0 offen offset:0 lds\n"
      "buffer_load_dwordx4 v36, %[srdT], 0 offen offset:1024 lds\n"
      "buffer_load_dwordx4 v40, %[srdT], 0 offen offset:2048 lds\n"
      "buffer_load_dwordx4 v44, %[srdT], 0 offen offset:3072 lds\n"
      "s_add_u32 m0, %[sLb], 20480\n"
      "s_nop 0\n"
      "buffer_load_dwordx4 v48, %[srdT], 0 offen offset:0 lds\n"
      "buffer_load_dwordx4 v52, %[srdT], 0 offen offset:1024 lds\n"
      "buffer_load_dwordx4 v56, %[srdT], 0 offen offset:2048 lds\n"
      "buffer_load_dwordx4 v60, %[srdT], 0 offen offset:3072 lds\n"
      "s_waitcnt vmcnt(16)\n"
      "s_barrier\n"
      "s_add_u32 m0, %[sLb], 32768\n"
      "v_add_u32 v31, %[sV], v33\n"
      "v_min_u32 v33, v33, v31\n"
      "v_lshl_add_u32 v33, v33, 8, %[gc0]\n"
      "buffer_load_dwordx4 v33, %[srdT], %[s128] offen offset:0 lds\n"
      "v_add_u32 v31, %[sV], v37\n"
      "v_min_u32 v37, v37, v31\n"
      "v_lshl_add_u32 v37, v37, 8, %[gc1]\n"
      "buffer_load_dwordx4 v37, %[srdT], %[s128] offen offset:1024 lds\n"
      "v_add_u32 v31, %[sV], v41\n"
      "v_min_u32 v41, v41, v31\n"
      "v_lshl_add_u32 v41, v41, 8, %[gc2]\n"
      "buffer_load_dwordx4 v41, %[srdT], %[s128] offen offset:2048 lds\n"
      "v_add_u32 v31, %[sV], v45\n"
      "v_min_u32 v45, v45, v31\n"
      "v_lshl_add_u32 v45, v45, 8, %[gc3]\n"
      "buffer_load_dwordx4 v45, %[srdT], %[s128] offen offset:3072 lds\n"
      "s_add_u32 m0, %[sLb], 36864\n"
      "v_add_u32 v31, %[sV], v49\n"
      "v_min_u32 v49, v49, v31\n"
      "v_lshl_add_u32 v49, v49, 8, %[gc0]\n"
      "buffer_load_dwordx4 v49, %[srdT], %[s128] offen offset:0 lds\n"
      "v_add_u32 v31, %[sV], v53\n"
      "v_min_u32 v53, v53, v31\n"
      "v_lshl_add_u32 v53, v53, 8, %[gc1]\n"
      "buffer_load_dwordx4 v53, %[srdT], %[s128] offen offset:1024 lds\n"
      "v_add_u32 v31, %[sV], v57\n"
      "v_min_u32 v57, v57, v31\n"
      "v_lshl_add_u32 v57, v57, 8, %[gc2]\n"
      "buffer_load_dwordx4 v57, %[srdT], %[s128] offen offset:2048 lds\n"
      "v_add_u32 v31, %[sV], v61\n"
      "v_min_u32 v61, v61, v31\n"
      "v_lshl_add_u32 v61, v61, 8, %[gc3]\n"
      "buffer_load_dwordx4 v61, %[srdT], %[s128] offen offset:3072 lds\n"
      "s_waitcnt vmcnt(16)\n"
      "s_barrier\n"
      "s_add_u32 m0, %[sLb], 49152\n"
      "s_nop 0\n"
      "buffer_load_dwordx4 v33, %[srdT], 0 offen offset:0 lds\n"
      "buffer_load_dwordx4 v37, %[srdT], 0 offen offset:1024 lds\n"
      "buffer_load_dwordx4 v41, %[srdT], 0 offen offset:2048 lds\n"
      "buffer_load_dwordx4 v45, %[srdT], 0 offen offset:3072 lds\n"
      "s_add_u32 m0, %[sLb], 53248\n"
      "s_nop 0\n"
      "buffer_load_dwordx4 v49, %[srdT], 0 offen offset:0 lds\n"
      "buffer_load_dwordx4 v53, %[srdT], 0 offen offset:1024 lds\n"
      "buffer_load_dwordx4 v57, %[srdT], 0 offen offset:2048 lds\n"
      "buffer_load_dwordx4 v61, %[srdT], 0 offen offset:3072 lds\n"
      "s_waitcnt vmcnt(16)\n"
      "s_barrier\n"
      "s_waitcnt vmcnt(8)\n"
      "s_barrier\n"
      "s_waitcnt vmcnt(0)\n"
      "s_barrier\n"
      :
      : [gc0] "v"(gc0), [gc1] "v"(gc1), [gc2] "v"(gc2), [gc3] "v"(gc3), [vdi] "v"(vdi), [srdT] "s"(srdT), [srdI] "s"(srdI),
        [sLb] "s"(sLb), [sV] "s"(V), [s4k] "s"(3328u), [s128] "s"(128u)
      : "memory", "scc", "v31", "v32", "v33", "v34", "v35", "v36", "v37", "v38", "v39", "v40", "v41", "v42", "v43", "v44", "v45", "v46", "v47", "v48", "v49", "v50", "v51", "v52", "v53", "v54", "v55", "v56", "v57", "v58", "v59", "v60", "v61", "v62", "v63", "v64", "v65", "v66", "v67", "v68", "v69", "v70", "v71", "v72", "v73", "v74", "v75", "v76", "v77", "v78", "v79", "v80", "v81", "v82", "v83", "v84", "v85", "v86", "v87", "v88", "v89", "v90", "v91", "v92", "v93", "v94", "v95", "v96", "v97", "v98", "v99", "v100", "v101", "v102", "v103", "v104", "v105", "v106", "v107", "v108", "v109", "v110", "v111", "v112", "v113", "v114", "v115", "v116", "v117", "v118", "v119", "v120", "v121", "v122", "v123", "v124", "v125", "v126", "v127");
    }
  } else {
    gu64* gran = (gu64*)(ws + WS_SLOT_OFF) + row0 + 2u * lane;
    gu64* aword = (gu64*)(ws + WS_CTL_OFF + mt * 256u);
    gu32* bnonce = (gu32*)(ws + WS_CTL_OFF + mt * 256u + 128u);
    const u32x4 srdW = make_srd(W1, KK * HH * 2);
    const u32 tv = ((lane >> 5) * 128u + (jj >> 1) * 8u + ((lane >> 2) & 7u)) * 1024u + h * 512u + (lane & 3u) * 128u;
    u32 sink = 0;
#define TOUCHW(st) if (LEADT > 0) asm volatile("buffer_load_dword %0, %1, %2, %3 offen" : "+v"(sink) : "v"(tv), "s"(srdW), "s"((u32)(st) * 131072u) : "memory")
    asm volatile("s_nop 4");
#pragma unroll
    for (int s = 0; s < LEADT; s += 2) TOUCHW(s);
    u32 tag = 0, seen = 0, nv = 0; bool pub = false; float wd0 = 0.f, wd1 = 0.f; u64 aw = 0, ga0 = 0, gb0 = 0;
    u32 xv[ND], wv[7], bv0 = 0, bv1 = 0;
    if (h == 0u) {
      ga0 = __hip_atomic_load(gran, RLX_AGENT); gb0 = __hip_atomic_load(gran + 1, RLX_AGENT);
      const u32* x0 = (const u32*)(xw + (row0 + 2u * lane) * ND);
#pragma unroll
      for (int i = 0; i < ND; ++i) xv[i] = x0[i];
#pragma unroll
      for (int i = 0; i < 6; ++i) wv[i] = ((const u32*)ww)[i];
      wv[6] = ww[12]; bv0 = bw[0]; bv1 = b2[0];
    } else {
      aw = __hip_atomic_load(aword, RLX_AGENT);
    }
#pragma unroll
    for (int s = 0; s < FF; ++s) {
      BAR();
      BAR();
      if (s == 2) {
        if (h == 0u) {
          const u32 ta = (u32)(ga0 >> 32), tb = (u32)(gb0 >> 32);
          tag = __builtin_amdgcn_readfirstlane(ta) + 1u;
          while (__any(ta == tag || tb == tag)) ++tag;
#pragma unroll
          for (int i = 0; i < ND; ++i) {
            const float wf = __uint_as_float((i & 1) ? (wv[i >> 1] & 0xffff0000u) : (wv[i >> 1] << 16));
            const int i1 = ND + i;
            wd0 += __uint_as_float((i & 1) ? (xv[i >> 1] & 0xffff0000u) : (xv[i >> 1] << 16)) * wf;
            wd1 += __uint_as_float((i1 & 1) ? (xv[i1 >> 1] & 0xffff0000u) : (xv[i1 >> 1] << 16)) * wf;
          }
          const float bias = __uint_as_float(bv0 << 16) + __uint_as_float(bv1 << 16);
          wd0 += bias; wd1 += bias;
        } else {
          tag = __builtin_amdgcn_readfirstlane((u32)aw) + 1u;
          if (lane == 0u) __hip_atomic_store(bnonce, tag, RLX_AGENT);
        }
      }
      if (h == 0u && (s == 9 || s == 20)) {
        const u32 n = __builtin_amdgcn_readfirstlane(nv);
        if ((!pub || n != seen) && lane == 0u) __hip_atomic_store(aword, ((u64)tag << 32) | n, RLX_AGENT);
        seen = n; pub = true;
      }
      if (h == 0u && (s == 8 || s == 19)) nv = __hip_atomic_load(bnonce, RLX_AGENT);
      if (h == 1u && s == FF - 2) aw = __hip_atomic_load(aword, RLX_AGENT);
      if ((s & 1) == (LEADT & 1)) TOUCHW(s + LEADT);
    }
    u64 ga = 0, gb = 0;
    if (h == 0u) { ga = __hip_atomic_load(gran, RLX_AGENT); gb = __hip_atomic_load(gran + 1, RLX_AGENT); }
    BAR();
    float s0 = 0.f, s1 = 0.f;
#pragma unroll
    for (int i = 0; i < 4; ++i) { const float2 v = *(const float2*)(red + i * 128 + 2u * lane); s0 += v.x; s1 += v.y; }
    if (h == 1u) {
      for (u32 spins = 0; (u32)aw != tag && spins < SPIN_CAP; ++spins) { __builtin_amdgcn_s_sleep(2); aw = __hip_atomic_load(aword, RLX_AGENT); }
      const u64 th = aw & 0xffffffff00000000ull;
      __hip_atomic_store(gran, th | __float_as_uint(s0), RLX_AGENT);
      __hip_atomic_store(gran + 1, th | __float_as_uint(s1), RLX_AGENT);
    } else {
      for (u32 spins = 0;; ++spins) {
        if (__all((u32)(ga >> 32) == tag && (u32)(gb >> 32) == tag) || spins >= SPIN_CAP) break;
        if ((spins & 7u) == 7u) {
          const u32 n = __builtin_amdgcn_readfirstlane(__hip_atomic_load(bnonce, RLX_AGENT));
          if ((!pub || n != seen) && lane == 0u) __hip_atomic_store(aword, ((u64)tag << 32) | n, RLX_AGENT);
          seen = n; pub = true;
        }
        ga = __hip_atomic_load(gran, RLX_AGENT); gb = __hip_atomic_load(gran + 1, RLX_AGENT);
      }
      const float z0 = (s0 + __uint_as_float((u32)ga)) + wd0, z1 = (s1 + __uint_as_float((u32)gb)) + wd1;
      __hip_bfloat162 o;
      o.x = __float2bfloat16(1.f / (1.f + __expf(-z0))); o.y = __float2bfloat16(1.f / (1.f + __expf(-z1)));
      *(__hip_bfloat162*)(out + row0 + 2u * lane) = o;
    }
    asm volatile("s_waitcnt vmcnt(0)" : "+v"(sink) :: "memory");
  }
}

extern "C" void kernel_launch(void* const* d_in, const int* in_sizes, int n_in,
                              void* d_out, int out_size, void* d_ws, size_t ws_size,
                              hipStream_t stream) {
  const int V = in_sizes[1] / DD;
  (void)hipFuncSetAttribute((const void*)dcwd_main, hipFuncAttributeMaxDynamicSharedMemorySize, LDS_BYTES);
  dcwd_main<<<256, NW * 64, LDS_BYTES, stream>>>((const int*)d_in[0], (const u16*)d_in[1], (const u16*)d_in[2], (const u16*)d_in[3],
      (const u16*)d_in[4], (const u16*)d_in[5], (const u16*)d_in[6], (const u16*)d_in[7], (const u16*)d_in[8], (char*)d_ws,
      (__hip_bfloat16*)d_out, V);
}
```
